```python
import torch

def _softmax(x, axis=-1):
    return torch.softmax(x, dim=axis)

def _gelu(x):
    return torch.nn.functional.gelu(x, approximate='tanh')

def _silu(x):
    return torch.nn.functional.silu(x)

def _sigmoid(x):
    return torch.sigmoid(x)

def _layernorm(x, axis=-1, eps=1e-05):
    m = x.mean(dim=axis, keepdim=True)
    v = x.var(dim=axis, keepdim=True, unbiased=False)
    return (x - m) / torch.sqrt(v + eps)

def _scatter_add(tgt, idx, src, axis=0):
    return tgt.clone().index_add_(axis, idx, src)

def _segment_sum(src, seg, n):
    out = torch.zeros((n,) + src.shape[1:], dtype=src.dtype)
    return out.index_add_(0, seg, src)

def _pad(x, pw):
    flat = tuple((v for pair in reversed(pw) for v in pair))
    return torch.nn.functional.pad(x, flat)
kernel_name = 'recurrent_scan__ssm_conv_s4d__mixer_gmlp__prompt'

def setup_inputs():

    _w = lambda a: (lambda t: t.to(torch.bfloat16) if t.is_floating_point() else t)(torch.as_tensor(a))

    def _A_setup():
        Bsz, L, D, N = (1, 2048, 448, 256)
        return {'u': _w((lambda *s: torch.randn(*s, dtype=torch.bfloat16) * 0.05)(Bsz, L, D)), 'log_A_real': _w((lambda *s: torch.randn(*s, dtype=torch.bfloat16) * 6.4)(D, N) * 0.5), 'A_imag': _w((lambda *s: torch.randn(*s, dtype=torch.bfloat16) * 0.05)(D, N) * 3.14), 'C_real': _w((lambda *s: torch.randn(*s, dtype=torch.bfloat16) * 0.05)(D, N) / N ** 0.5), 'C_imag': _w((lambda *s: torch.randn(*s, dtype=torch.bfloat16) * 0.05)(D, N) / N ** 0.5), 'log_dt': _w((lambda *s: torch.randn(*s, dtype=torch.bfloat16) * 0.05)(D) * 0.5 - 4.0), 'D_skip': _w((lambda *s: torch.randn(*s, dtype=torch.bfloat16) * 0.05)(D))}
    _A_in = _A_setup()

    def _B_setup():
        B, S, D, F = (1, 2048, 448, 1792)
        Fh = F // 2
        return {'x': _w((lambda *s: torch.randn(*s, dtype=torch.bfloat16) * 0.05)(B, S, D)), 'w_in': _w((lambda *s: torch.randn(*s, dtype=torch.bfloat16) * 0.4)(D, 2 * Fh)), 'w_sp': _w(0.01 * (lambda *s: torch.randn(*s, dtype=torch.bfloat16) * 0.4)(S, S)), 'b_sp': _w(1.0 + 0.01 * (lambda *s: torch.randn(*s, dtype=torch.bfloat16) * 0.05)(S)), 'w_out': _w((lambda *s: torch.randn(*s, dtype=torch.bfloat16) * 0.05)(Fh, D))}
    _B_in = _B_setup()
    return {'A_u': _A_in['u'], 'A_log_A_real': _A_in['log_A_real'], 'A_A_imag': _A_in['A_imag'], 'A_C_real': _A_in['C_real'], 'A_C_imag': _A_in['C_imag'], 'A_log_dt': _A_in['log_dt'], 'A_D_skip': _A_in['D_skip'], 'B_x': _B_in['x'], 'B_w_in': _B_in['w_in'], 'B_w_sp': _B_in['w_sp'], 'B_b_sp': _B_in['b_sp'], 'B_w_out': _B_in['w_out']}

def reference(A_u, A_log_A_real, A_A_imag, A_C_real, A_C_imag, A_log_dt, A_D_skip, B_x, B_w_in, B_w_sp, B_b_sp, B_w_out):

    def _A_ref(u, log_A_real, A_imag, C_real, C_imag, log_dt, D_skip):
        L = u.shape[1]
        dt = torch.exp(log_dt)
        A = -torch.exp(log_A_real) + 1j * A_imag
        C = (C_real + 1j * C_imag) * dt[:, None]
        pos = (lambda x, dt: x.to(dt))(torch.arange(L), log_dt.dtype)
        P = torch.exp((A * dt[:, None])[:, :, None] * pos[None, None, :])
        K = 2.0 * torch.einsum('dn,dnl->dl', C, P).real
        L2 = 2 * L
        u_t = (lambda x, *d: x.permute(*d))(u, 0, 2, 1)
        Kf = (lambda x, *a, **k: torch.fft.rfft(x.float(), *a, **k))(K, L2)
        Uf = (lambda x, *a, **k: torch.fft.rfft(x.float(), *a, **k))(u_t, L2)
        y = (lambda x, *a, **k: torch.fft.irfft(x, *a, **k).to(torch.bfloat16))(Uf * Kf[None, :, :], L2)[:, :, :L]
        y = (lambda x, *d: x.permute(*d))(y, 0, 2, 1)
        return y + u * D_skip[None, None, :]

    def _B_ref(x, w_in, w_sp, b_sp, w_out):
        B, S, D = x.shape
        F = w_in.shape[-1]
        Fh = F // 2
        z = _gelu(torch.einsum('bsd,df->bsf', x, w_in))
        z1 = z[..., :Fh].clone()
        z2 = _layernorm(z[..., Fh:], axis=-1)
        g = torch.einsum('ts,bsf->btf', w_sp, z2) + b_sp[None, :, None]
        return torch.einsum('bsf,fd->bsd', z1 * g, w_out)
    x = A_u
    xn = _layernorm(x, axis=-1)
    s = _A_ref(xn, A_log_A_real, A_A_imag, A_C_real, A_C_imag, A_log_dt, A_D_skip)
    g = _silu(_B_ref(xn, B_w_in, B_w_sp, B_b_sp, B_w_out))
    return x + s * g
```

```cpp
#include <hip/hip_runtime.h>
#include <hip/hip_bf16.h>
#include <stdint.h>

#define LAS __attribute__((address_space(3)))
#define GAS __attribute__((address_space(1)))
typedef float f32x2 __attribute__((ext_vector_type(2)));
typedef float f32x4 __attribute__((ext_vector_type(4)));
typedef unsigned u32x2 __attribute__((ext_vector_type(2)));
typedef unsigned u32x4 __attribute__((ext_vector_type(4)));
typedef short s16x4 __attribute__((ext_vector_type(4)));
typedef short s16x8 __attribute__((ext_vector_type(8)));
typedef __bf16 bf16x2 __attribute__((ext_vector_type(2)));
typedef __bf16 bf16x8 __attribute__((ext_vector_type(8)));
typedef _Float16 f16x2 __attribute__((ext_vector_type(2)));
typedef _Float16 f16x8 __attribute__((ext_vector_type(8)));

constexpr int SEQ = 2048, DM = 448, FH = 896, FF = 1792, NST = 256;

constexpr size_t WS_Z1D = 0;
constexpr size_t WS_Z2F = WS_Z1D + (size_t)SEQ * FH * 2;
constexpr size_t WS_HF  = WS_Z2F + (size_t)SEQ * FH * 2;
constexpr size_t WS_XT  = WS_HF + (size_t)SEQ * FH * 2;
constexpr size_t WS_ST  = WS_XT + (size_t)SEQ * DM * 2;
constexpr size_t WS_WOF = WS_ST + (size_t)SEQ * DM * 2;
constexpr size_t WS_XCH = WS_WOF + (size_t)FH * DM * 2;
constexpr size_t WS_EP  = WS_XCH + 32 * 64 * 8 * 16;
constexpr size_t WS_END = WS_EP + 32 * 128;

__device__ __forceinline__ float bf2f(unsigned short h) { return __uint_as_float((unsigned)h << 16); }
__device__ __forceinline__ float bflo(unsigned w) { return __uint_as_float(w << 16); }
__device__ __forceinline__ float bfhi(unsigned w) { return __uint_as_float(w & 0xffff0000u); }
__device__ __forceinline__ unsigned pk_bf16(float lo, float hi) {
  bf16x2 v = {(__bf16)lo, (__bf16)hi};
  return __builtin_bit_cast(unsigned, v);
}
__device__ __forceinline__ float rbf(float x) { return (float)(__bf16)x; }
__device__ __forceinline__ unsigned pk_f16(float lo, float hi) {
  f16x2 v = {(_Float16)lo, (_Float16)hi};
  return __builtin_bit_cast(unsigned, v);
}
__device__ __forceinline__ float gelu_tanh(float x) {
  const float c1 = -2.0f * 0.7978845608028654f * 1.4426950408889634f, c2 = c1 * 0.044715f;
  float t = x * x;
  float a = x * __builtin_fmaf(t, c2, c1);
  float e = __builtin_amdgcn_exp2f(a);
  return x * __builtin_amdgcn_rcpf(1.0f + e);
}
template <int CTRL> __device__ __forceinline__ float dpp_add(float v) {
  int t = __builtin_amdgcn_update_dpp(0, __float_as_int(v), CTRL, 0xf, 0xf, true);
  return v + __int_as_float(t);
}
__device__ __forceinline__ float red16(float v) {
  v = dpp_add<0xB1>(v);
  v = dpp_add<0x4E>(v);
  v = dpp_add<0x141>(v);
  v = dpp_add<0x140>(v);
  return v;
}
__device__ __forceinline__ float red8(float v) {
  v = dpp_add<0xB1>(v); v = dpp_add<0x4E>(v); v = dpp_add<0x141>(v);
  return v;
}
__device__ __forceinline__ void glds16(const void* sbase, unsigned voff, unsigned lds) {
  asm volatile("s_mov_b32 m0, %2\n\ts_nop 0\n\tglobal_load_lds_dwordx4 %0, %1" ::"v"(voff), "s"(sbase), "s"(lds) : "memory");
}
#define WAIT_VM(n) asm volatile("s_waitcnt vmcnt(%0)" ::"n"(n) : "memory")
#define WAIT_LGKM0() asm volatile("s_waitcnt lgkmcnt(0)" ::: "memory")
#define BAR() do { asm volatile("s_waitcnt lgkmcnt(0)" ::: "memory"); __builtin_amdgcn_s_barrier(); asm volatile("" ::: "memory"); } while (0)
__device__ __forceinline__ s16x4 tr_read(LAS const char* p) {
  typedef short v4i16 __attribute__((ext_vector_type(4)));
  return __builtin_amdgcn_ds_read_tr16_b64_v4i16((LAS v4i16*)p);
}
__device__ __forceinline__ f32x4 mfma_bf(s16x8 a, s16x8 b, f32x4 c) {
  return __builtin_amdgcn_mfma_f32_16x16x32_bf16(__builtin_bit_cast(bf16x8, a), __builtin_bit_cast(bf16x8, b), c, 0, 0, 0);
}
__device__ __forceinline__ float dot2bf(unsigned a, unsigned b, float c) {
  return __builtin_amdgcn_fdot2_f32_bf16(__builtin_bit_cast(bf16x2, a), __builtin_bit_cast(bf16x2, b), c, false);
}

constexpr int K1_XS = 928;
constexpr int K1_X = 0;
constexpr int K1_W = 59392;
constexpr int K1_SLOT = 14336;
constexpr int K1_S = K1_W + 7 * K1_SLOT;
constexpr int K1_P = K1_S;
constexpr int K1_M = K1_S + 2048;
constexpr int K1_R = K1_M + 256;
constexpr int K1_M2 = K1_R + 256;
constexpr int K1_R2 = K1_M2 + 256;
constexpr int K1_LDS = 163840;

template <bool Z2>
__device__ __forceinline__ void k1_gemm_half(LAS char* smem, const void* w_in, int wave, int lane, int rh, int cg, unsigned voffw0, unsigned voffw1,
                                             unsigned other_half_off, f32x4 (&acc)[2][2], float (&w1)[2]) {
  const int nct = (cg == 3) ? 1 : 2;
  const int g = lane >> 4;
  LAS const char* abase = smem + K1_X + (32 * rh + (lane & 15)) * K1_XS + g * 16;
  LAS const char* bbase = smem + K1_W + (4 * g + ((lane & 15) >> 2)) * 224 + (lane & 3) * 8 + cg * 64;
#pragma unroll
  for (int c = 0; c < 7; ++c) {
    if (Z2) { if (c == 0) WAIT_VM(12); else WAIT_VM(10); }
    else { if (c <= 1) WAIT_VM(10); else if (c == 2) WAIT_VM(8); else if (c == 3) WAIT_VM(6); else if (c == 4) WAIT_VM(4); else if (c == 5) WAIT_VM(2); else WAIT_VM(0); }
    BAR();
    {
      const int it = (Z2 ? 0 : 7) + c;
      const int nx = it + 6;
      if (it >= 1 && nx <= 13 && wave < 7) {
        const int slot = (it - 1) % 7;
        const unsigned koff = (unsigned)(nx - 7) * 64u * 3584u + other_half_off;
        glds16(w_in, voffw0 + koff, (unsigned)(K1_W + slot * K1_SLOT + wave * 2048));
        glds16(w_in, voffw1 + koff, (unsigned)(K1_W + slot * K1_SLOT + wave * 2048 + 1024));
      }
    }
#pragma unroll
    for (int ks = 0; ks < 2; ++ks) {
      s16x8 a[2], b[2];
#pragma unroll
      for (int t = 0; t < 2; ++t) a[t] = *(LAS const s16x8*)(abase + t * 16 * K1_XS + c * 128 + ks * 64);
#pragma unroll
      for (int n = 0; n < 2; ++n) {
        if (n < nct) {
          s16x4 lo = tr_read(bbase + c * K1_SLOT + ks * 32 * 224 + n * 32);
          s16x4 hi = tr_read(bbase + c * K1_SLOT + ks * 32 * 224 + 16 * 224 + n * 32);
          b[n] = __builtin_shufflevector(lo, hi, 0, 1, 2, 3, 4, 5, 6, 7);
        }
      }
#pragma unroll
      for (int n = 0; n < 2; ++n) {
        if (n < nct) {
#pragma unroll
          for (int t = 0; t < 2; ++t) acc[t][n] = Z2 ? mfma_bf(a[t], b[n], acc[t][n]) : mfma_bf(b[n], a[t], acc[t][n]);
          u32x4 bw = __builtin_bit_cast(u32x4, b[n]);
#pragma unroll
          for (int e = 0; e < 4; ++e) w1[n] = dot2bf(bw[e], 0x3f803f80u, w1[n]);
        }
      }
    }
  }
#pragma unroll
  for (int n = 0; n < 2; ++n) { w1[n] += __shfl_xor(w1[n], 16); w1[n] += __shfl_xor(w1[n], 32); }
}

__global__ __launch_bounds__(512, 2) void k1_gemm1(const unsigned short* __restrict__ x, const unsigned short* __restrict__ w_in, char* __restrict__ ws) {
  extern __shared__ __attribute__((aligned(16))) char smem_raw[];
  LAS char* smem = (LAS char*)smem_raw;
  const int tid = threadIdx.x, lane = tid & 63;
  const int wave = __builtin_amdgcn_readfirstlane(tid >> 6);
  const int b = blockIdx.x, xcd = b & 7, rr = b >> 3;
  const int ti = 4 * xcd + (rr >> 3), tj = rr & 7;
  const int rh = wave >> 2, cg = wave & 3;

  unsigned ep;
  {
    const char* epp = ws + WS_EP + ti * 128;
    unsigned zero = 0;
    asm volatile("global_load_dword %0, %1, %2 sc1" : "=v"(ep) : "v"(zero), "s"(epp) : "memory");
  }
  {
    const char* xb = (const char*)x + (size_t)ti * 64 * 896;
#pragma unroll
    for (int m = 0; m < 8; ++m) {
      int n = wave + 8 * m; n = n > 57 ? 57 : n;
      const int p = 64 * n + lane, row = p / 58; int c = p - row * 58; c = c > 55 ? 55 : c;
      glds16(xb, (unsigned)(row * 896 + c * 16), (unsigned)(K1_X + n * 1024));
    }
  }
  unsigned voffw0, voffw1;
  {
    const int w7 = wave < 7 ? wave : 6;
    int p = 128 * w7 + lane, r = p / 14, c = p - r * 14;
    int k = (r & 32) + 8 * ((r >> 2) & 3) + 4 * ((r >> 4) & 1) + (r & 3);
    voffw0 = (unsigned)(k * 3584 + c * 16 + tj * 224);
    p += 64; r = p / 14; c = p - r * 14;
    k = (r & 32) + 8 * ((r >> 2) & 3) + 4 * ((r >> 4) & 1) + (r & 3);
    voffw1 = (unsigned)(k * 3584 + c * 16 + tj * 224);
    if (wave < 7) {
#pragma unroll
      for (int c2 = 0; c2 < 7; ++c2) {
        glds16(w_in, voffw0 + (unsigned)(c2 * 64 * 3584 + 1792), (unsigned)(K1_W + c2 * K1_SLOT + wave * 2048));
        glds16(w_in, voffw1 + (unsigned)(c2 * 64 * 3584 + 1792), (unsigned)(K1_W + c2 * K1_SLOT + wave * 2048 + 1024));
      }
    }
  }
  if (wave < 7) asm volatile("s_waitcnt vmcnt(14)" : "+v"(ep)::"memory"); else asm volatile("s_waitcnt vmcnt(0)" : "+v"(ep)::"memory");
  BAR();
  const unsigned tag = ep + 1u;
  {
    const int row = tid >> 3, c8 = tid & 7;
    float s = 0.f, q = 0.f;
#pragma unroll
    for (int m = 0; m < 7; ++m) {
      u32x4 v = *(LAS const u32x4*)(smem + K1_X + row * K1_XS + (c8 + 8 * m) * 16);
#pragma unroll
      for (int e = 0; e < 4; ++e) { s = dot2bf(v[e], 0x3f803f80u, s); q = dot2bf(v[e], v[e], q); }
    }
    s = red8(s); q = red8(q);
    const float mean = s * (1.0f / 448.0f);
    float var = q * (1.0f / 448.0f) - mean * mean; var = var < 0.f ? 0.f : var;
    const float mb = rbf(mean), den = rbf(__builtin_sqrtf(rbf(rbf(var) + 1e-5f)));
    if (c8 == 0) { *(LAS float*)(smem + K1_M + row * 4) = mb; *(LAS float*)(smem + K1_R + row * 4) = 1.0f / den; }
  }
  BAR();
  if (tid < 448) {
    const int dl = tid >> 3, sc = tid & 7, d = 56 * tj + dl;
    float v[8];
    f32x4 m0 = *(LAS const f32x4*)(smem + K1_M + sc * 32), m1 = *(LAS const f32x4*)(smem + K1_M + sc * 32 + 16);
    f32x4 r0 = *(LAS const f32x4*)(smem + K1_R + sc * 32), r1 = *(LAS const f32x4*)(smem + K1_R + sc * 32 + 16);
#pragma unroll
    for (int e = 0; e < 8; ++e) {
      const float xv = bf2f(*(LAS const unsigned short*)(smem + K1_X + (8 * sc + e) * K1_XS + d * 2));
      const float mm = e < 4 ? m0[e & 3] : m1[e & 3], r_ = e < 4 ? r0[e & 3] : r1[e & 3];
      v[e] = rbf(rbf(xv - mm) * r_);
    }
    u32x4 o = {pk_f16(v[0], v[1]), pk_f16(v[2], v[3]), pk_f16(v[4], v[5]), pk_f16(v[6], v[7])};
    *(u32x4*)(ws + WS_XT + ((size_t)d * SEQ + 64 * ti + 8 * sc) * 2) = o;
  }

  const int q4 = lane >> 4, l15 = lane & 15;
  const int nct = (cg == 3) ? 1 : 2;
  f32x4 acc2[2][2] = {};
  float w1[2] = {0.f, 0.f};
  k1_gemm_half<true>(smem, w_in, wave, lane, rh, cg, voffw0, voffw1, 0u, acc2, w1);
  {
    float ssum[2][4], ssq[2][4];
#pragma unroll
    for (int t = 0; t < 2; ++t) {
      const f32x4 mv = *(LAS const f32x4*)(smem + K1_M + (32 * rh + 16 * t + 4 * q4) * 4);
      const f32x4 rv = *(LAS const f32x4*)(smem + K1_R + (32 * rh + 16 * t + 4 * q4) * 4);
#pragma unroll
      for (int j = 0; j < 4; ++j) {
        float s = 0.f, q = 0.f;
#pragma unroll
        for (int n = 0; n < 2; ++n) {
          if (n < nct) {
            const float pre = rbf(rv[j] * (acc2[t][n][j] - mv[j] * w1[n]));
            const float z = rbf(gelu_tanh(pre));
            acc2[t][n][j] = z; s += z; q += z * z;
          }
        }
        ssum[t][j] = red16(s); ssq[t][j] = red16(q);
      }
    }
    if (l15 == 0) {
#pragma unroll
      for (int t = 0; t < 2; ++t)
#pragma unroll
        for (int j = 0; j < 4; ++j)
          *(LAS f32x2*)(smem + K1_P + ((32 * rh + 16 * t + 4 * q4 + j) * 4 + cg) * 8) = (f32x2){ssum[t][j], ssq[t][j]};
    }
    BAR();
    if (wave == 0) {
      const f32x4 p0 = *(LAS const f32x4*)(smem + K1_P + lane * 32), p1 = *(LAS const f32x4*)(smem + K1_P + lane * 32 + 16);
      const float s = (p0[0] + p0[2]) + (p1[0] + p1[2]), q = (p0[1] + p0[3]) + (p1[1] + p1[3]);
      u32x4 gr = {__float_as_uint(s), tag, __float_as_uint(q), tag};
      const char* xp = ws + WS_XCH + (size_t)ti * 64 * 128 + tj * 16;
      const unsigned vo = lane * 128;
      asm volatile("global_store_dwordx4 %0, %1, %2 sc1\n\ts_nop 1" ::"v"(vo), "v"(gr), "s"(xp) : "memory");
    }
  }
  {
    f32x4 acc1[2][2] = {};
    float w1b[2] = {0.f, 0.f};
    k1_gemm_half<false>(smem, w_in, wave, lane, rh, cg, voffw0, voffw1, 0u, acc1, w1b);
#pragma unroll
    for (int n = 0; n < 2; ++n) {
      if (n < nct) {
        float wv[4];
#pragma unroll
        for (int j = 0; j < 4; ++j) wv[j] = __shfl(w1b[n], 4 * q4 + j);
#pragma unroll
        for (int t = 0; t < 2; ++t) {
          const int srow = 32 * rh + 16 * t + l15;
          const float mm = *(LAS const float*)(smem + K1_M + srow * 4), r_ = *(LAS const float*)(smem + K1_R + srow * 4);
          float z[4];
#pragma unroll
          for (int j = 0; j < 4; ++j) z[j] = gelu_tanh(rbf(r_ * (acc1[t][n][j] - mm * wv[j])));
          u32x2 o = {pk_bf16(z[0], z[1]), pk_bf16(z[2], z[3])};
          const int tt = 4 * ti + 2 * rh + t, ft = 7 * tj + 2 * cg + n;
          *(u32x2*)(ws + WS_Z1D + ((size_t)(tt * 56 + ft) * 64 + lane) * 8) = o;
        }
      }
    }
  }
  {
    const int row = tid >> 3, jj = tid & 7;
    const char* xp = ws + WS_XCH + (size_t)ti * 64 * 128;
    const unsigned vo = row * 128 + jj * 16;
    u32x4 gr;
    for (unsigned spins = 0;; ++spins) {
      asm volatile("global_load_dwordx4 %0, %1, %2 sc1\n\ts_waitcnt vmcnt(0)" : "=&v"(gr) : "v"(vo), "s"(xp) : "memory");
      const bool ok = (gr[1] == tag) && (gr[3] == tag);
      if (__all(ok) || spins > (1u << 22)) break;
      __builtin_amdgcn_s_sleep(1);
    }
    const float s = red8(__uint_as_float(gr[0])), q = red8(__uint_as_float(gr[2]));
    const float mean = s * (1.0f / 896.0f);
    float var = q * (1.0f / 896.0f) - mean * mean; var = var < 0.f ? 0.f : var;
    const float mb = rbf(mean), den = rbf(__builtin_sqrtf(rbf(rbf(var) + 1e-5f)));
    if (jj == 0) { *(LAS float*)(smem + K1_M2 + row * 4) = mb; *(LAS float*)(smem + K1_R2 + row * 4) = 1.0f / den; }
    BAR();
    if (tj == 0 && tid == 0) __hip_atomic_store((GAS unsigned*)(ws + WS_EP + ti * 128), tag, __ATOMIC_RELAXED, __HIP_MEMORY_SCOPE_AGENT);
    unsigned pz[2][2][2];
#pragma unroll
    for (int t = 0; t < 2; ++t) {
      const f32x4 mv = *(LAS const f32x4*)(smem + K1_M2 + (32 * rh + 16 * t + 4 * q4) * 4);
      const f32x4 rv = *(LAS const f32x4*)(smem + K1_R2 + (32 * rh + 16 * t + 4 * q4) * 4);
#pragma unroll
      for (int n = 0; n < 2; ++n) {
        float z[4];
#pragma unroll
        for (int j = 0; j < 4; ++j) z[j] = rbf(acc2[t][n][j] - mv[j]) * rv[j];
        pz[n][t][0] = pk_bf16(z[0], z[1]); pz[n][t][1] = pk_bf16(z[2], z[3]);
      }
    }
#pragma unroll
    for (int n = 0; n < 2; ++n) {
      if (n < nct) {
        u32x4 o;
#pragma unroll
        for (int e = 0; e < 2; ++e) {
          auto r1 = __builtin_amdgcn_permlane32_swap(pz[n][0][e], pz[n][1][e], false, false);
          auto r2 = __builtin_amdgcn_permlane16_swap(r1[0], r1[1], false, false);
          o[e] = r2[0]; o[2 + e] = r2[1];
        }
        const int ft = 7 * tj + 2 * cg + n, sb = 2 * ti + rh;
        *(u32x4*)(ws + WS_Z2F + ((size_t)(ft * 64 + sb) * 64 + lane) * 16) = o;
      }
    }
  }
}


constexpr int K2_RING = 0;
constexpr int K2_CH = 65536;
constexpr int K2_CHS = 49152;
constexpr int CH_FT = 0;
constexpr int CH_FW = 16416, CH_FS = 288;
constexpr int CH_CS = 4160;
constexpr int CH_U = 33280;
constexpr int CH_X = CH_U + 4608;
constexpr int CH_CNT = CH_X + 8192;
constexpr int K2_LDS = 163840;

__device__ __forceinline__ void lds_sync(LAS char* cntp, unsigned target, int lane) {
  LAS unsigned* cnt = (LAS unsigned*)cntp;
  asm volatile("s_waitcnt lgkmcnt(0)" ::: "memory");
  if (lane == 0) __hip_atomic_fetch_add(cnt, 1u, __ATOMIC_RELAXED, __HIP_MEMORY_SCOPE_WORKGROUP);
  while ((unsigned)__builtin_amdgcn_readfirstlane(*(volatile LAS unsigned*)cnt) < target) __builtin_amdgcn_s_sleep(1);
  asm volatile("" ::: "memory");
}
__device__ __forceinline__ __amdgpu_buffer_rsrc_t mk_rsrc(const void* p, unsigned bytes) {
  return __builtin_amdgcn_make_buffer_rsrc((void*)p, (short)0, (int)bytes, 0x00020000);
}
__device__ __forceinline__ void bglds16(__amdgpu_buffer_rsrc_t rs, unsigned voff, unsigned soff, unsigned lds) {
  asm volatile("s_mov_b32 m0, %3\n\ts_nop 0\n\tbuffer_load_dwordx4 %0, %1, %2 offen lds" ::"v"(voff), "s"(rs), "s"(soff), "s"(lds) : "memory");
}
__device__ __forceinline__ void bload16(u32x4& dst, __amdgpu_buffer_rsrc_t rs, unsigned voff, unsigned soff) {
  asm volatile("buffer_load_dwordx4 %0, %1, %2, %3 offen" : "=v"(dst) : "v"(voff), "s"(rs), "s"(soff) : "memory");
}

__device__ __forceinline__ void k2_gemm(LAS char* smem, const unsigned short* __restrict__ w_sp, const unsigned short* __restrict__ b_sp, char* __restrict__ ws,
                                        int wave, int lane, int T, int J) {
  const __amdgpu_buffer_rsrc_t zs = mk_rsrc(ws + WS_Z2F + (size_t)(7 * J * 64 + 16 * wave) * 1024, 7 * 65536 - 16 * wave * 1024);
  const __amdgpu_buffer_rsrc_t wr = mk_rsrc((const char*)w_sp + (size_t)T * 64 * 4096 + wave * 1024, 64 * 4096 - wave * 1024);
  const unsigned voffz = lane * 16;
  const unsigned voffw = (lane >> 2) * 4096 + (((lane & 3) ^ ((lane & 32) ? 3 : 0)) * 16);
  const unsigned ring = K2_RING + wave * 16384;
  LAS const char* wrd = smem + ring + (lane & 15) * 64 + (((lane >> 4) ^ ((lane & 8) ? 3 : 0)) * 16);
  u32x4 z[3][7];
  f32x4 acc[7][4];
#pragma unroll
  for (int n = 0; n < 7; ++n)
#pragma unroll
    for (int m = 0; m < 4; ++m) acc[n][m] = (f32x4){0.f, 0.f, 0.f, 0.f};
#define K2_ISSUE(it)                                                                                        \
  do {                                                                                                      \
    _Pragma("unroll") for (int m = 0; m < 4; ++m) bglds16(wr, voffw, m * 65536 + (it) * 64, ring + ((it) & 3) * 4096 + m * 1024); \
    _Pragma("unroll") for (int n = 0; n < 7; ++n) bload16(z[(it) % 3][n], zs, voffz, n * 65536 + (it) * 1024); \
  } while (0)
  K2_ISSUE(0); K2_ISSUE(1); K2_ISSUE(2);
#pragma unroll
  for (int it = 0; it < 16; ++it) {
    u32x4(&zc)[7] = z[it % 3];
    if (it <= 13)
      asm volatile("s_waitcnt vmcnt(22)" : "+v"(zc[0]), "+v"(zc[1]), "+v"(zc[2]), "+v"(zc[3]), "+v"(zc[4]), "+v"(zc[5]), "+v"(zc[6])::"memory");
    else if (it == 14)
      asm volatile("s_waitcnt vmcnt(11)" : "+v"(zc[0]), "+v"(zc[1]), "+v"(zc[2]), "+v"(zc[3]), "+v"(zc[4]), "+v"(zc[5]), "+v"(zc[6])::"memory");
    else
      asm volatile("s_waitcnt vmcnt(0)" : "+v"(zc[0]), "+v"(zc[1]), "+v"(zc[2]), "+v"(zc[3]), "+v"(zc[4]), "+v"(zc[5]), "+v"(zc[6])::"memory");
    s16x8 wf[4];
#pragma unroll
    for (int m = 0; m < 4; ++m) wf[m] = *(LAS const s16x8*)(wrd + (it & 3) * 4096 + m * 1024);
#pragma unroll
    for (int m = 0; m < 4; ++m)
#pragma unroll
      for (int n = 0; n < 7; ++n) acc[n][m] = mfma_bf(__builtin_bit_cast(s16x8, zc[n]), wf[m], acc[n][m]);
    __builtin_amdgcn_sched_barrier(0);
    if (it + 3 < 16) K2_ISSUE(it + 3);
    __builtin_amdgcn_sched_barrier(0);
  }
#undef K2_ISSUE
  const int tt = 4 * T + wave;
  u32x2 z1v[7];
#pragma unroll
  for (int n = 0; n < 7; ++n) z1v[n] = *(const u32x2*)(ws + WS_Z1D + ((size_t)(tt * 56 + 7 * J + n) * 64 + lane) * 8);
  const float bs = bf2f(b_sp[16 * tt + (lane & 15)]);
  LAS char* cnt = smem + K2_CH + CH_CNT + 64;
  {
    LAS char* mine = smem + ring + lane * 16;
    LAS char* other = smem + K2_RING + (wave ^ 2) * 16384 + lane * 16;
    LAS char* o2 = smem + K2_RING + (wave ^ 3) * 16384 + lane * 16;
    if (wave & 2) {
#pragma unroll
      for (int n = 0; n < 7; ++n) { *(LAS f32x4*)(mine + n * 1024) = acc[n][0]; *(LAS f32x4*)(mine + (7 + n) * 1024) = acc[n][1]; acc[n][0] = acc[n][2]; acc[n][1] = acc[n][3]; }
    } else {
#pragma unroll
      for (int n = 0; n < 7; ++n) { *(LAS f32x4*)(mine + n * 1024) = acc[n][2]; *(LAS f32x4*)(mine + (7 + n) * 1024) = acc[n][3]; }
    }
    lds_sync(cnt, 4, lane);
#pragma unroll
    for (int n = 0; n < 7; ++n) { acc[n][0] += *(LAS const f32x4*)(other + n * 1024); acc[n][1] += *(LAS const f32x4*)(other + (7 + n) * 1024); }
    asm volatile("s_waitcnt lgkmcnt(0)" ::: "memory");
    if (wave & 1) {
#pragma unroll
      for (int n = 0; n < 7; ++n) { *(LAS f32x4*)(other + n * 1024) = acc[n][0]; acc[n][0] = acc[n][1]; }
    } else {
#pragma unroll
      for (int n = 0; n < 7; ++n) *(LAS f32x4*)(other + n * 1024) = acc[n][1];
    }
    lds_sync(cnt, 8, lane);
    const bool jodd = J & 1;
    unsigned hp[7][2];
#pragma unroll
    for (int n = 0; n < 7; ++n) {
      const f32x4 v = acc[n][0] + *(LAS const f32x4*)(o2 + n * 1024);
      float h[4];
#pragma unroll
      for (int j = 0; j < 4; ++j) {
        const float g = rbf(rbf(v[j]) + bs);
        const float zz = (j & 1) ? bfhi(z1v[n][j >> 1]) : bflo(z1v[n][j >> 1]);
        h[j] = zz * g;
      }
      hp[n][0] = pk_bf16(h[0], h[1]); hp[n][1] = pk_bf16(h[2], h[3]);
    }
    char* hb = ws + WS_HF + ((size_t)(tt * 28 + (7 * J >> 1)) * 64 + lane) * 16;
    if (!jodd) {
#pragma unroll
      for (int p = 0; p < 3; ++p) *(u32x4*)(hb + p * 1024) = (u32x4){hp[2 * p][0], hp[2 * p][1], hp[2 * p + 1][0], hp[2 * p + 1][1]};
      *(u32x2*)(hb + 3 * 1024) = (u32x2){hp[6][0], hp[6][1]};
    } else {
      *(u32x2*)(hb + 8) = (u32x2){hp[0][0], hp[0][1]};
#pragma unroll
      for (int p = 0; p < 3; ++p) *(u32x4*)(hb + (p + 1) * 1024) = (u32x4){hp[2 * p + 1][0], hp[2 * p + 1][1], hp[2 * p + 2][0], hp[2 * p + 2][1]};
    }
  }
}

__device__ __forceinline__ f32x2 cmul(f32x2 a, f32x2 b) {
  return (f32x2){a.x, a.x} * b + (f32x2){-a.y, a.y} * (f32x2){b.y, b.x};
}
__device__ __forceinline__ f32x2 cexp_p(float al, float be, float p) {
  const float e = __builtin_amdgcn_exp2f(al * p), th = __builtin_amdgcn_fractf(be * p);
  return (f32x2){e * __builtin_amdgcn_cosf(th), e * __builtin_amdgcn_sinf(th)};
}
__device__ __forceinline__ f32x4 mfma_h(u32x4 a, u32x4 b, f32x4 c) {
  return __builtin_amdgcn_mfma_f32_16x16x32_f16(__builtin_bit_cast(f16x8, a), __builtin_bit_cast(f16x8, b), c, 0, 0, 0);
}

__device__ __forceinline__ void k2_s4d(LAS char* cb, const unsigned short* __restrict__ logA, const unsigned short* __restrict__ Aim, const unsigned short* __restrict__ Cre,
                                       const unsigned short* __restrict__ Cim, const unsigned short* __restrict__ logdt, const unsigned short* __restrict__ Dsk,
                                       char* __restrict__ ws, int d, int hw, int lane) {
  LAS char* cnt = cb + CH_CNT;
  float la[2], ai[2], cr[4], ci[4];
#pragma unroll
  for (int p = 0; p < 2; ++p) { la[p] = bf2f(logA[d * NST + 128 * hw + 64 * p + lane]); ai[p] = bf2f(Aim[d * NST + 128 * hw + 64 * p + lane]); }
#pragma unroll
  for (int p = 0; p < 4; ++p) { const int n = (128 * hw + 64 * p + lane) & 255; cr[p] = bf2f(Cre[d * NST + n]); ci[p] = bf2f(Cim[d * NST + n]); }
  const float dt = rbf(expf(bf2f(logdt[d]))), dsk = bf2f(Dsk[d]);
  {
    const u32x4* src = (const u32x4*)(ws + WS_XT + (size_t)d * SEQ * 2 + hw * 2048);
    const u32x4 u0 = src[lane], u1 = src[64 + lane];
    *(LAS u32x4*)(cb + CH_U + 512 + hw * 2048 + lane * 16) = u0;
    *(LAS u32x4*)(cb + CH_U + 512 + hw * 2048 + 1024 + lane * 16) = u1;
    if (hw == 0 && lane < 32) *(LAS u32x4*)(cb + CH_U + lane * 16) = (u32x4){0u, 0u, 0u, 0u};
  }
  float sabs = 0.f;
#pragma unroll
  for (int p = 0; p < 4; ++p) sabs += __builtin_fabsf(cr[p]) + __builtin_fabsf(ci[p]);
  sabs = red16(sabs); sabs += __shfl_xor(sabs, 16); sabs += __shfl_xor(sabs, 32);
  sabs *= 2.0f * dt;
  int ex = (int)((__float_as_uint(sabs) >> 23) & 0xff) - 126;
  ex = ex < -100 ? -100 : (ex > 100 ? 100 : ex);
  const float scale = __uint_as_float((unsigned)(127 + 15 - ex) << 23), iscale = __uint_as_float((unsigned)(127 - 15 + ex) << 23);
  f32x4 kacc[3] = {};
  LAS char* fw = cb + CH_FT + hw * CH_FW;
#pragma unroll 1
  for (int p = 0; p < 2; ++p) {
    const float lap = p ? la[1] : la[0], aip = p ? ai[1] : ai[0], crp = p ? cr[1] : cr[0], cip = p ? ci[1] : ci[0];
    const float lre = -rbf(expf(lap)) * dt, lim = aip * dt;
    const float al = lre * 1.4426950408889634f, be = lim * 0.15915494309189535f;
    LAS char* wp = fw + lane * 4;
#define PUTF(row, v) *(LAS unsigned*)(wp + (row) * CH_FS) = pk_f16((v).x, (v).y)
    {
      const f32x2 w = cexp_p(al, be, 16.f);
      const float cs = 2.0f * dt * scale;
      f32x2 a0 = {crp * cs, cip * cs};
      const f32x2 w2 = cmul(w, w), w4 = cmul(w2, w2);
      f32x2 a1 = cmul(a0, w), a2 = cmul(a0, w2), a3 = cmul(a1, w2);
      PUTF(0, a0); PUTF(1, a1); PUTF(2, a2); PUTF(3, a3);
#pragma unroll
      for (int k = 1; k < 4; ++k) {
        a0 = cmul(a0, w4); a1 = cmul(a1, w4); a2 = cmul(a2, w4); a3 = cmul(a3, w4);
        PUTF(4 * k, a0); PUTF(4 * k + 1, a1); PUTF(4 * k + 2, a2); PUTF(4 * k + 3, a3);
      }
    }
    {
      f32x2 e[16];
      e[0] = (f32x2){1.f, 0.f};
      e[1] = cexp_p(al, -be, 1.f);
      e[2] = cmul(e[1], e[1]); e[3] = cmul(e[2], e[1]); e[4] = cmul(e[2], e[2]);
      e[5] = cmul(e[4], e[1]); e[6] = cmul(e[4], e[2]); e[7] = cmul(e[4], e[3]); e[8] = cmul(e[4], e[4]);
#pragma unroll
      for (int k = 9; k < 16; ++k) e[k] = cmul(e[8], e[k - 8]);
#pragma unroll
      for (int k = 0; k < 16; ++k) PUTF(16 + k, e[k]);
      const f32x2 z256 = cexp_p(al, -be, 256.f), z512 = cexp_p(al, -be, 512.f), z1024 = cexp_p(al, -be, 1024.f), z2048 = cexp_p(al, -be, 2048.f);
      const f32x2 z768 = cmul(z512, z256);
      PUTF(32, z256);
#pragma unroll
      for (int k = 1; k < 8; ++k) { const f32x2 t = cmul(z256, e[2 * k]); PUTF(32 + k, t); }
      PUTF(40, z512); PUTF(44, z768);
#pragma unroll
      for (int k = 1; k < 4; ++k) { const f32x2 t = cmul(z512, e[4 * k]), t2 = cmul(z768, e[4 * k]); PUTF(40 + k, t); PUTF(44 + k, t2); }
      const f32x2 z1280 = cmul(z1024, z256), z1536 = cmul(z1024, z512), z1792 = cmul(z1024, z768);
      PUTF(48, z1024); PUTF(50, z1280); PUTF(52, z1536); PUTF(54, z1792);
      { const f32x2 t0 = cmul(z1024, e[8]), t1 = cmul(z1280, e[8]), t2 = cmul(z1536, e[8]), t3 = cmul(z1792, e[8]); PUTF(49, t0); PUTF(51, t1); PUTF(53, t2); PUTF(55, t3); }
      PUTF(56, z2048);
    }
#undef PUTF
    asm volatile("s_waitcnt lgkmcnt(0)" ::: "memory");
    LAS const char* rp = fw + (lane & 15) * CH_FS + (lane >> 4) * 16;
#pragma unroll
    for (int kk = 0; kk < 4; ++kk) {
      const u32x4 a = *(LAS const u32x4*)(rp + kk * 64);
#pragma unroll
      for (int t = 0; t < 3; ++t) kacc[t] = mfma_h(a, *(LAS const u32x4*)(rp + (16 + 16 * t) * CH_FS + kk * 64), kacc[t]);
    }
    asm volatile("s_waitcnt lgkmcnt(0)" ::: "memory");
  }
#pragma unroll
  for (int t = 0; t < 3; ++t) *(LAS f32x4*)(cb + CH_X + hw * 3072 + t * 1024 + lane * 16) = kacc[t];
  lds_sync(cnt, 2, lane);
  {
    const int c = lane & 15, q = lane >> 4;
#pragma unroll
    for (int t = 0; t < 3; ++t) {
      const f32x4 o = *(LAS const f32x4*)(cb + CH_X + (hw ^ 1) * 3072 + t * 1024 + lane * 16);
      int e; bool ok = true;
      if (t == 0) e = c;
      else if (t == 1) e = c < 8 ? 256 + 2 * c : 512 + 256 * ((c - 8) >> 2) + 4 * (c & 3);
      else { e = c < 8 ? 1024 + 256 * (c >> 1) + 8 * (c & 1) : 2048; ok = c <= 8; }
#pragma unroll
      for (int jj = 0; jj < 2; ++jj) {
        const int j = 2 * hw + jj, a = 4 * q + j;
        const f32x4 sum4 = kacc[t] + o;
        const float v = hw ? (jj ? sum4[3] : sum4[2]) : (jj ? sum4[1] : sum4[0]);
        if (ok && (e != 2048 || a == 0)) *(LAS _Float16*)(cb + CH_FT + (16 * a + e) * 2) = (_Float16)v;
      }
    }
  }
  lds_sync(cnt, 4, lane);
  {
    const int x0 = 1024 * hw + 16 * lane;
    unsigned w[8];
#pragma unroll
    for (int k = 0; k < 8; ++k) {
      unsigned short h[2];
#pragma unroll
      for (int b = 0; b < 2; ++b) {
        const int j = 2047 - (x0 + 2 * k + b);
        const int pp = (int)rbf((float)j);
        h[b] = *(LAS const unsigned short*)(cb + CH_FT + pp * 2);
      }
      w[k] = (unsigned)h[0] | ((unsigned)h[1] << 16);
    }
    *(LAS u32x4*)(cb + CH_X + x0 * 2) = (u32x4){w[0], w[1], w[2], w[3]};
    *(LAS u32x4*)(cb + CH_X + x0 * 2 + 16) = (u32x4){w[4], w[5], w[6], w[7]};
    if (hw == 1 && lane < 4) *(LAS u32x4*)(cb + CH_X + 4096 + lane * 16) = (u32x4){0u, 0u, 0u, 0u};
  }
  lds_sync(cnt, 6, lane);
#pragma unroll 1
  for (int it = 0; it < 3; ++it) {
    const int y = hw * 64 + lane + 128 * it;
    if (y <= 256) {
      const u32x4 r0 = *(LAS const u32x4*)(cb + CH_X + y * 16), r1 = *(LAS const u32x4*)(cb + CH_X + y * 16 + 16);
      unsigned W[8] = {r0[0], r0[1], r0[2], r0[3], r1[0], r1[1], r1[2], r1[3]}, V[7];
#pragma unroll
      for (int i = 0; i < 7; ++i) V[i] = __builtin_amdgcn_alignbit(W[i + 1], W[i], 16);
#pragma unroll
      for (int m = 0; m < 4; ++m) {
        *(LAS u32x4*)(cb + CH_FT + (2 * m) * CH_CS + y * 16) = (u32x4){W[m], W[m + 1], W[m + 2], W[m + 3]};
        *(LAS u32x4*)(cb + CH_FT + (2 * m + 1) * CH_CS + y * 16) = (u32x4){V[m], V[m + 1], V[m + 2], V[m + 3]};
      }
    }
  }
  lds_sync(cnt, 8, lane);
  f32x4 acc[8] = {};
  {
    const int r = lane & 15, h = lane >> 4;
    LAS const char* ab = cb + CH_FT + ((7 - r) & 7) * CH_CS + (255 - (r >> 3) - 2 * (h >> 1) + (h & 1)) * 16 - (32 * 14 + 512 * 7) - hw * 256;
    LAS const char* bb = cb + CH_U + 512 + 32 * r - 32 * (h >> 1) + 16 * (h & 1) - 32 * 6 - hw * 256;
#pragma unroll 1
    for (int ar = 0; ar < 4; ++ar) {
      u32x4 A[8], B[8];
#pragma unroll
      for (int k = 0; k < 8; ++k) {
        A[k] = *(LAS const u32x4*)(ab + (32 * 14 + 512 * 7) - 512 * k - 64 * ar);
        B[k] = *(LAS const u32x4*)(bb + 32 * 6 + 512 * k - 64 * ar);
      }
#pragma unroll
      for (int al = 0; al < 8; ++al)
#pragma unroll
        for (int be = 0; be + al < 8; ++be) acc[al + be] = mfma_h(A[al], B[be], acc[al + be]);
    }
  }
  lds_sync(cnt, 10, lane);
  if (hw) {
#pragma unroll
    for (int t = 0; t < 4; ++t) { *(LAS f32x4*)(cb + CH_X + 4096 + t * 1024 + lane * 16) = acc[t]; acc[t] = acc[4 + t]; }
  } else {
#pragma unroll
    for (int t = 0; t < 4; ++t) *(LAS f32x4*)(cb + CH_X + t * 1024 + lane * 16) = acc[4 + t];
  }
  lds_sync(cnt, 12, lane);
  {
    const int i = lane & 15, q = lane >> 4;
#pragma unroll
    for (int t = 0; t < 4; ++t) {
      const f32x4 v = acc[t] + *(LAS const f32x4*)(cb + CH_X + (hw ^ 1) * 4096 + t * 1024 + lane * 16);
      const int l0 = 256 * (4 * hw + t) + 16 * i + 4 * q;
      const u32x2 uw = *(LAS const u32x2*)(cb + CH_U + 512 + l0 * 2);
      const unsigned uw0 = uw[0], uw1 = uw[1];
      const f16x2 ua = __builtin_bit_cast(f16x2, uw0), ub = __builtin_bit_cast(f16x2, uw1);
      const float uu[4] = {(float)ua[0], (float)ua[1], (float)ub[0], (float)ub[1]};
      float sv[4];
#pragma unroll
      for (int j = 0; j < 4; ++j) sv[j] = rbf(v[j] * iscale) + rbf(uu[j] * dsk);
      *(u32x2*)(ws + WS_ST + ((size_t)d * SEQ + l0) * 2) = (u32x2){pk_bf16(sv[0], sv[1]), pk_bf16(sv[2], sv[3])};
    }
  }
}

__global__ __launch_bounds__(512, 2) void k2_mix(const unsigned short* __restrict__ w_sp, const unsigned short* __restrict__ b_sp, const unsigned short* __restrict__ logA,
                                                 const unsigned short* __restrict__ Aim, const unsigned short* __restrict__ Cre, const unsigned short* __restrict__ Cim,
                                                 const unsigned short* __restrict__ logdt, const unsigned short* __restrict__ Dsk, const unsigned short* __restrict__ w_out,
                                                 char* __restrict__ ws) {
  extern __shared__ __attribute__((aligned(16))) char smem_raw[];
  LAS char* smem = (LAS char*)smem_raw;
  const int tid = threadIdx.x, lane = tid & 63;
  const int wave = __builtin_amdgcn_readfirstlane(tid >> 6);
  const int b = blockIdx.x, xcd = b & 7, rr = b >> 3;
  if (tid < 64) {
    if (lane < 32) *(LAS unsigned*)(smem + K2_CH + CH_CNT + lane * 4) = 0u;
    else *(LAS unsigned*)(smem + K2_CH + K2_CHS + CH_CNT + (lane - 32) * 4) = 0u;
  }
  BAR();
  if (wave < 4) {
    const int T = 8 * (xcd >> 1) + (rr & 7), J = 4 * (xcd & 1) + (rr >> 3);
    k2_gemm(smem, w_sp, b_sp, ws, wave, lane, T, J);
  } else {
    const int sw = wave - 4;
    if (b < 224) {
      k2_s4d(smem + K2_CH + (sw >> 1) * K2_CHS, logA, Aim, Cre, Cim, logdt, Dsk, ws, 2 * b + (sw >> 1), sw & 1, lane);
    } else {
      for (int fr = (b - 224) * 4 + sw; fr < 784; fr += 128) {
        const int dt = fr / 28, fb = fr - dt * 28;
        const int dcol = 16 * dt + (lane & 15), q = lane >> 4;
        unsigned short v[8];
#pragma unroll
        for (int jj = 0; jj < 8; ++jj) v[jj] = w_out[(size_t)(32 * fb + 16 * (jj >> 2) + 4 * q + (jj & 3)) * DM + dcol];
        u32x4 o = {(unsigned)v[0] | ((unsigned)v[1] << 16), (unsigned)v[2] | ((unsigned)v[3] << 16), (unsigned)v[4] | ((unsigned)v[5] << 16), (unsigned)v[6] | ((unsigned)v[7] << 16)};
        *(u32x4*)(ws + WS_WOF + ((size_t)fr * 64 + lane) * 16) = o;
      }
    }
  }
}

constexpr int K3_LDS = 49152 + 8448;
__global__ __launch_bounds__(256, 1) void k3_out(const unsigned short* __restrict__ x, char* __restrict__ ws, unsigned short* __restrict__ out) {
  extern __shared__ __attribute__((aligned(16))) char smem_raw[];
  LAS char* smem = (LAS char*)smem_raw;
  const int tid = threadIdx.x, lane = tid & 63;
  const int wave = __builtin_amdgcn_readfirstlane(tid >> 6);
  const int b = blockIdx.x, xcd = b & 7, rr = b >> 3;
  const int T = 4 * xcd + rr / 7, Dt = rr % 7;
  const u32x4* hp = (const u32x4*)(ws + WS_HF + ((size_t)(4 * T * 28 + 7 * wave) * 64 + lane) * 16);
  const u32x4* wp = (const u32x4*)(ws + WS_WOF + ((size_t)(4 * Dt * 28 + 7 * wave) * 64 + lane) * 16);
  u32x4 hf[7][4], wf[7][4];
#pragma unroll
  for (int k = 0; k < 7; ++k)
#pragma unroll
    for (int m = 0; m < 4; ++m) { hf[k][m] = hp[(m * 28 + k) * 64]; wf[k][m] = wp[(m * 28 + k) * 64]; }
  const int erow = tid >> 2, ec = tid & 3;
  const u32x4 st0 = *(const u32x4*)(ws + WS_ST + ((size_t)(64 * Dt + erow) * SEQ + 64 * T + 16 * ec) * 2);
  const u32x4 st1 = *(const u32x4*)(ws + WS_ST + ((size_t)(64 * Dt + erow) * SEQ + 64 * T + 16 * ec + 8) * 2);
  const u32x4 x0 = *(const u32x4*)(x + (size_t)(64 * T + erow) * DM + 64 * Dt + 16 * ec);
  const u32x4 x1 = *(const u32x4*)(x + (size_t)(64 * T + erow) * DM + 64 * Dt + 16 * ec + 8);
  __builtin_amdgcn_sched_barrier(0);
  f32x4 acc[4][4];
#pragma unroll
  for (int n = 0; n < 4; ++n)
#pragma unroll
    for (int m = 0; m < 4; ++m) acc[n][m] = (f32x4){0.f, 0.f, 0.f, 0.f};
#pragma unroll
  for (int k = 0; k < 7; ++k)
#pragma unroll
    for (int n = 0; n < 4; ++n)
#pragma unroll
      for (int m = 0; m < 4; ++m) acc[n][m] = mfma_bf(__builtin_bit_cast(s16x8, wf[k][n]), __builtin_bit_cast(s16x8, hf[k][m]), acc[n][m]);
  LAS char* sl = smem + 49152;
  {
    LAS unsigned* p = (LAS unsigned*)(sl + erow * 132 + ec * 32);
#pragma unroll
    for (int e = 0; e < 4; ++e) { p[e] = st0[e]; p[4 + e] = st1[e]; }
  }
#pragma unroll
  for (int m = 0; m < 4; ++m) {
    if (m != wave) {
      const int slot = m > wave ? m - 1 : m;
#pragma unroll
      for (int n = 0; n < 4; ++n) *(LAS f32x4*)(smem + ((wave * 3 + slot) * 4 + n) * 1024 + lane * 16) = acc[n][m];
    }
  }
  __syncthreads();
  f32x4 o[4];
  if (wave == 0) { for (int n = 0; n < 4; ++n) o[n] = acc[n][0]; }
  else if (wave == 1) { for (int n = 0; n < 4; ++n) o[n] = acc[n][1]; }
  else if (wave == 2) { for (int n = 0; n < 4; ++n) o[n] = acc[n][2]; }
  else { for (int n = 0; n < 4; ++n) o[n] = acc[n][3]; }
#pragma unroll
  for (int sw = 0; sw < 4; ++sw) {
    if (sw != wave) {
      const int slot = wave > sw ? wave - 1 : wave;
#pragma unroll
      for (int n = 0; n < 4; ++n) o[n] += *(LAS const f32x4*)(smem + ((sw * 3 + slot) * 4 + n) * 1024 + lane * 16);
    }
  }
  __syncthreads();
  {
    const int t = 16 * wave + (lane & 15), q = lane >> 4;
#pragma unroll
    for (int n = 0; n < 4; ++n) {
      f32x4 g;
#pragma unroll
      for (int j = 0; j < 4; ++j) {
        const float v = rbf(o[n][j]);
        g[j] = rbf(v * __builtin_amdgcn_rcpf(1.0f + __builtin_amdgcn_exp2f(-1.4426950408889634f * v)));
      }
      *(LAS f32x4*)(smem + t * 272 + (16 * n + 4 * q) * 4) = g;
    }
  }
  __syncthreads();
  {
    unsigned ow[8];
#pragma unroll
    for (int k = 0; k < 4; ++k) {
      const f32x4 g = *(LAS const f32x4*)(smem + erow * 272 + (16 * ec + 4 * k) * 4);
      float r[4];
#pragma unroll
      for (int j = 0; j < 4; ++j) {
        const int dl = 16 * ec + 4 * k + j;
        const float sv = bf2f(*(LAS const unsigned short*)(sl + dl * 132 + erow * 2));
        const unsigned xw = (k < 2 ? x0 : x1)[((4 * k + j) >> 1) & 3];
        const float xv = (j & 1) ? bfhi(xw) : bflo(xw);
        r[j] = xv + rbf(sv * g[j]);
      }
      ow[2 * k] = pk_bf16(r[0], r[1]); ow[2 * k + 1] = pk_bf16(r[2], r[3]);
    }
    u32x4* op = (u32x4*)(out + (size_t)(64 * T + erow) * DM + 64 * Dt + 16 * ec);
    op[0] = (u32x4){ow[0], ow[1], ow[2], ow[3]};
    op[1] = (u32x4){ow[4], ow[5], ow[6], ow[7]};
  }
}

extern "C" void kernel_launch(void* const* d_in, const int* in_sizes, int n_in, void* d_out, int out_size, void* d_ws, size_t ws_size, hipStream_t stream) {
  const unsigned short* x = (const unsigned short*)d_in[0];
  const unsigned short* logA = (const unsigned short*)d_in[1];
  const unsigned short* Aim = (const unsigned short*)d_in[2];
  const unsigned short* Cre = (const unsigned short*)d_in[3];
  const unsigned short* Cim = (const unsigned short*)d_in[4];
  const unsigned short* logdt = (const unsigned short*)d_in[5];
  const unsigned short* Dsk = (const unsigned short*)d_in[6];
  const unsigned short* w_in = (const unsigned short*)d_in[8];
  const unsigned short* w_sp = (const unsigned short*)d_in[9];
  const unsigned short* b_sp = (const unsigned short*)d_in[10];
  const unsigned short* w_out = (const unsigned short*)d_in[11];
  char* ws = (char*)d_ws;
  (void)hipFuncSetAttribute((const void*)k1_gemm1, hipFuncAttributeMaxDynamicSharedMemorySize, K1_LDS);
  (void)hipFuncSetAttribute((const void*)k2_mix, hipFuncAttributeMaxDynamicSharedMemorySize, K2_LDS);
  (void)hipFuncSetAttribute((const void*)k3_out, hipFuncAttributeMaxDynamicSharedMemorySize, K3_LDS);
  k1_gemm1<<<256, 512, K1_LDS, stream>>>(x, w_in, ws);
  k2_mix<<<256, 512, K2_LDS, stream>>>(w_sp, b_sp, logA, Aim, Cre, Cim, logdt, Dsk, w_out, ws);
  k3_out<<<224, 256, K3_LDS, stream>>>(x, ws, (unsigned short*)d_out);
}
```

```cpp
#include <hip/hip_runtime.h>
#include <hip/hip_bf16.h>
#include <stdint.h>

#define LAS __attribute__((address_space(3)))
#define GAS __attribute__((address_space(1)))
typedef float f32x2 __attribute__((ext_vector_type(2)));
typedef float f32x4 __attribute__((ext_vector_type(4)));
typedef unsigned u32x2 __attribute__((ext_vector_type(2)));
typedef unsigned u32x4 __attribute__((ext_vector_type(4)));
typedef short s16x4 __attribute__((ext_vector_type(4)));
typedef short s16x8 __attribute__((ext_vector_type(8)));
typedef __bf16 bf16x2 __attribute__((ext_vector_type(2)));
typedef __bf16 bf16x8 __attribute__((ext_vector_type(8)));
typedef _Float16 f16x2 __attribute__((ext_vector_type(2)));
typedef _Float16 f16x8 __attribute__((ext_vector_type(8)));

constexpr int SEQ = 2048, DM = 448, FH = 896, FF = 1792, NST = 256;

constexpr size_t WS_Z1D = 0;
constexpr size_t WS_Z2F = WS_Z1D + (size_t)SEQ * FH * 2;
constexpr size_t WS_HF  = WS_Z2F + (size_t)SEQ * FH * 2;
constexpr size_t WS_XT  = WS_HF + (size_t)SEQ * FH * 2;
constexpr size_t WS_ST  = WS_XT + (size_t)SEQ * DM * 2;
constexpr size_t WS_WOF = WS_ST + (size_t)SEQ * DM * 2;
constexpr size_t WS_XCH = WS_WOF + (size_t)FH * DM * 2;
constexpr size_t WS_EP  = WS_XCH + 32 * 64 * 8 * 16;
constexpr size_t WS_END = WS_EP + 32 * 128;

constexpr size_t WS_DBG = 32u << 20;
#define STAMP(kern, slot) do {} while (0)
#define MARK_DECL do {} while (0)
#define MARK(i) do {} while (0)
#define MARK_DUMP(kern) do {} while (0)
__device__ __forceinline__ float bf2f(unsigned short h) { return __uint_as_float((unsigned)h << 16); }
__device__ __forceinline__ float bflo(unsigned w) { return __uint_as_float(w << 16); }
__device__ __forceinline__ float bfhi(unsigned w) { return __uint_as_float(w & 0xffff0000u); }
__device__ __forceinline__ unsigned pk_bf16(float lo, float hi) {
  bf16x2 v = {(__bf16)lo, (__bf16)hi};
  return __builtin_bit_cast(unsigned, v);
}
__device__ __forceinline__ float rbf(float x) { return (float)(__bf16)x; }
__device__ __forceinline__ unsigned pk_f16(float lo, float hi) {
  f16x2 v = {(_Float16)lo, (_Float16)hi};
  return __builtin_bit_cast(unsigned, v);
}
__device__ __forceinline__ float gelu_tanh(float x) {
  const float c1 = -2.0f * 0.7978845608028654f * 1.4426950408889634f, c2 = c1 * 0.044715f;
  float t = x * x;
  float a = x * __builtin_fmaf(t, c2, c1);
  float e = __builtin_amdgcn_exp2f(a);
  return x * __builtin_amdgcn_rcpf(1.0f + e);
}
template <int CTRL> __device__ __forceinline__ float dpp_add(float v) {
  int t = __builtin_amdgcn_update_dpp(0, __float_as_int(v), CTRL, 0xf, 0xf, true);
  return v + __int_as_float(t);
}
__device__ __forceinline__ float red16(float v) {
  v = dpp_add<0xB1>(v);
  v = dpp_add<0x4E>(v);
  v = dpp_add<0x141>(v);
  v = dpp_add<0x140>(v);
  return v;
}
__device__ __forceinline__ float red8(float v) {
  v = dpp_add<0xB1>(v); v = dpp_add<0x4E>(v); v = dpp_add<0x141>(v);
  return v;
}
__device__ __forceinline__ void glds16(const void* sbase, unsigned voff, unsigned lds) {
  asm volatile("s_mov_b32 m0, %2\n\ts_nop 0\n\tglobal_load_lds_dwordx4 %0, %1" ::"v"(voff), "s"(sbase), "s"(lds) : "memory");
}
#define WAIT_VM(n) asm volatile("s_waitcnt vmcnt(%0)" ::"n"(n) : "memory")
#define WAIT_LGKM0() asm volatile("s_waitcnt lgkmcnt(0)" ::: "memory")
#define BAR() do { asm volatile("s_waitcnt lgkmcnt(0)" ::: "memory"); __builtin_amdgcn_s_barrier(); asm volatile("" ::: "memory"); } while (0)
__device__ __forceinline__ s16x4 tr_read(LAS const char* p) {
  typedef short v4i16 __attribute__((ext_vector_type(4)));
  return __builtin_amdgcn_ds_read_tr16_b64_v4i16((LAS v4i16*)p);
}
__device__ __forceinline__ f32x4 mfma_bf(s16x8 a, s16x8 b, f32x4 c) {
  return __builtin_amdgcn_mfma_f32_16x16x32_bf16(__builtin_bit_cast(bf16x8, a), __builtin_bit_cast(bf16x8, b), c, 0, 0, 0);
}
__device__ __forceinline__ float dot2bf(unsigned a, unsigned b, float c) {
  return __builtin_amdgcn_fdot2_f32_bf16(__builtin_bit_cast(bf16x2, a), __builtin_bit_cast(bf16x2, b), c, false);
}

constexpr int K1_X = 0;
constexpr int K1_W = 57344;
constexpr int K1_SLOT = 16384;
constexpr int K1_S = K1_W + 6 * K1_SLOT;
constexpr int K1_P = K1_S;
constexpr int K1_SUM = K1_S + 2048;
constexpr int K1_SQ = K1_SUM + 256;
constexpr int K1_M2 = K1_SQ + 256;
constexpr int K1_R2 = K1_M2 + 256;
constexpr int K1_W1 = K1_R2 + 256;
constexpr int K1_LDS = 163840;
constexpr unsigned ONES_BF = 0x3f803f80u;

__device__ __forceinline__ float xsum16_32(float v) {
  auto a = __builtin_amdgcn_permlane16_swap(__float_as_uint(v), __float_as_uint(v), false, false);
  v = __uint_as_float(a[0]) + __uint_as_float(a[1]);
  auto b = __builtin_amdgcn_permlane32_swap(__float_as_uint(v), __float_as_uint(v), false, false);
  return __uint_as_float(b[0]) + __uint_as_float(b[1]);
}
__device__ __forceinline__ f32x2 up_bf(unsigned w) { return (f32x2){bflo(w), bfhi(w)}; }
__device__ __forceinline__ f32x2 gelu2(f32x2 x) {
  const float c1 = -2.0f * 0.7978845608028654f * 1.4426950408889634f, c2 = c1 * 0.044715f;
  const f32x2 t = x * x;
  const f32x2 a = x * (t * c2 + c1);
  f32x2 d = {__builtin_amdgcn_exp2f(a.x), __builtin_amdgcn_exp2f(a.y)};
  d = d + 1.0f;
  return x * (f32x2){__builtin_amdgcn_rcpf(d.x), __builtin_amdgcn_rcpf(d.y)};
}
__device__ __forceinline__ void ln_scalars(float s, float q, float invn, float& mb, float& rr) {
  const float mean = s * invn;
  float var = q * invn - mean * mean; var = var < 0.f ? 0.f : var;
  mb = rbf(mean);
  rr = __builtin_amdgcn_rcpf(rbf(__builtin_amdgcn_sqrtf(rbf(rbf(var) + 1e-5f))));
}
__device__ __forceinline__ unsigned gelu_pair(f32x4 acc, f32x4 w1, int p, float m, float r) {
  const f32x2 a2 = {acc[2 * p], acc[2 * p + 1]}, w2 = {w1[2 * p], w1[2 * p + 1]};
  const f32x2 pre = (a2 - w2 * m) * r;
  const f32x2 z = gelu2(up_bf(pk_bf16(pre.x, pre.y)));
  return pk_bf16(z.x, z.y);
}

#define K1_ABL 0
struct K1Ctx {
  LAS char* smem;
  const unsigned short* w_in;
  char* ws;
  LAS const char *xb[2][2], *bb0, *bb1;
  unsigned voffw[2], voffx;
  const char* xsrc;
  int wave, lane, rh, cg, ti, tj;
  unsigned tag;
};
#define K1_FRAGS(ks, buf)                                                                                              \
  do {                                                                                                                 \
    const int g_ = (ks) >> 1, so_ = (g_ % 6) * K1_SLOT + ((ks) & 1) * 32 * 256;                                        \
    if ((ks) < 14 && (!(K1_ABL & 16) || (ks) == 0)) {                                                                                                   \
      _Pragma("unroll") for (int t = 0; t < 2; ++t) xa[(ks) % 14][t] = *(LAS const s16x8*)(c.xb[t][(ks) & 1] + g_ * 8192); \
    }                                                                                                                  \
    wb[buf][0] = __builtin_shufflevector(tr_read(c.bb0 + so_), tr_read(c.bb0 + so_ + 16 * 256), 0, 1, 2, 3, 4, 5, 6, 7); \
    wb[buf][1] = __builtin_shufflevector(tr_read(c.bb1 + so_), tr_read(c.bb1 + so_ + 16 * 256), 0, 1, 2, 3, 4, 5, 6, 7); \
  } while (0)
#define K1_XPIECE(g) glds16(c.xsrc + (g) * 128, c.voffx, (unsigned)(K1_X + (g) * 8192 + c.wave * 1024))
#define K1_WPIECE(G, e)                                                                                                \
  glds16(c.w_in, c.voffw[e] + (unsigned)(((G) % 7) * 64 * 3584 + ((G) < 7 ? 1792 : 0)), (unsigned)(K1_W + ((G) % 6) * K1_SLOT + c.wave * 2048 + (e) * 1024))

template <bool W1, bool SQ>
__device__ __forceinline__ void k1_main(const K1Ctx& c) {
  LAS char* smem = c.smem;
  char* ws = c.ws;
  const int lane = c.lane, wave = c.wave, rh = c.rh, cg = c.cg, q4 = lane >> 4, l15 = lane & 15;
  const int tsw = cg & 1;
  s16x8 xa[14][2], wb[2][2];
  const s16x8 ones = __builtin_bit_cast(s16x8, (u32x4){ONES_BF, ONES_BF, ONES_BF, ONES_BF});
  f32x4 acc[2][2] = {}, accw[2] = {}, accs = {};
  f32x4 acc1[2][2] = {}, accw1[2] = {};
  float m1[2], r1[2], ss[2] = {0.f, 0.f}, sq[2] = {0.f, 0.f};
  unsigned zp[2][2][2];
  f32x4 w1z2[2];
  MARK_DECL;
  MARK(0);
  WAIT_VM(3);
  BAR();
  MARK(1);
  STAMP(0, 1);
  K1_FRAGS(0, 0);
#pragma unroll
  for (int ks = 0; ks < 28; ++ks) {
    const int G = ks >> 1, cur = ks & 1;
    if (cur == 0 && G >= 1) {
      if (G == 1) WAIT_VM(6); else if (G == 2) WAIT_VM(9); else if (G == 3) WAIT_VM(8); else if (G == 4) WAIT_VM(7); else if (G <= 9) WAIT_VM(6);
      else if (G == 10) WAIT_VM(4); else if (G == 11) WAIT_VM(2); else WAIT_VM(0);
      if (G == 7) {
        if (W1 && l15 == 0) {
#pragma unroll
          for (int n = 0; n < 2; ++n) *(LAS f32x4*)(smem + K1_W1 + ((cg * 2 + n) * 4 + q4) * 16) = accw[n];
        }
        const int srow = 32 * rh + 16 * tsw + l15;
        if (SQ) {
          const int j = lane & 3;
          const float v = j == 0 ? accs[0] : j == 1 ? accs[1] : j == 2 ? accs[2] : accs[3];
          if (q4 == (l15 >> 2)) *(LAS float*)(smem + K1_SQ + srow * 4) = v;
        } else if (q4 == 0) *(LAS float*)(smem + K1_SUM + srow * 4) = accs[0];
        asm volatile("s_waitcnt lgkmcnt(0)" ::: "memory");
      }
      if (G == 12) asm volatile("s_waitcnt lgkmcnt(0)" ::: "memory");
      MARK(2 * G);
      if (!(K1_ABL & 2)) __builtin_amdgcn_s_barrier();
      asm volatile("" ::: "memory");
      MARK(2 * G + 1);
      if (G == 7) STAMP(0, 3);
    }
    __builtin_amdgcn_sched_barrier(0);
    if (ks + 1 < 28) { if (cur == 0) K1_FRAGS((ks + 1 < 28 ? ks + 1 : ks), 1); else K1_FRAGS((ks + 1 < 28 ? ks + 1 : ks), 0); }
    if (ks < 14) {
#pragma unroll
      for (int n = 0; n < 2; ++n)
#pragma unroll
        for (int t = 0; t < 2; ++t) acc[n][t] = mfma_bf(wb[cur][n], xa[(K1_ABL & 16) ? 0 : ks % 14][t], acc[n][t]);
      if (W1 && !(K1_ABL & 32)) {
#pragma unroll
        for (int n = 0; n < 2; ++n) accw[n] = mfma_bf(wb[cur][n], ones, accw[n]);
      }
      if (!(K1_ABL & 32)) accs = mfma_bf(SQ ? xa[(K1_ABL & 16) ? 0 : ks % 14][0] : ones, xa[(K1_ABL & 16) ? 0 : ks % 14][0], accs);
    } else {
#pragma unroll
      for (int n = 0; n < 2; ++n)
#pragma unroll
        for (int t = 0; t < 2; ++t) acc1[n][t] = mfma_bf(wb[cur][n], xa[(K1_ABL & 16) ? 0 : ks % 14][t], acc1[n][t]);
      if (W1) {
#pragma unroll
        for (int n = 0; n < 2; ++n) accw1[n] = mfma_bf(wb[cur][n], ones, accw1[n]);
      }
      const int j = (K1_ABL & 1) ? 99 : ks - 14;
      if (j == 0) {
#pragma unroll
        for (int t = 0; t < 2; ++t) {
          const int srow = 32 * rh + 16 * (t ^ tsw) + l15;
          ln_scalars(*(LAS const float*)(smem + K1_SUM + srow * 4), *(LAS const float*)(smem + K1_SQ + srow * 4), 1.0f / 448.0f, m1[t], r1[t]);
        }
#pragma unroll
        for (int n = 0; n < 2; ++n) w1z2[n] = *(LAS const f32x4*)(smem + K1_W1 + ((cg * 2 + n) * 4 + q4) * 16);
      } else if (j <= 8) {
        const int pi = j - 1, n = pi >> 2, t = (pi >> 1) & 1, p = pi & 1;
        const unsigned zz = gelu_pair(acc[n][t], w1z2[n], p, m1[t], r1[t]);
        zp[n][t][p] = zz;
        ss[t] = dot2bf(zz, ONES_BF, ss[t]); sq[t] = dot2bf(zz, zz, sq[t]);
      } else if (j == 9) {
#pragma unroll
        for (int t = 0; t < 2; ++t) {
          const float s = xsum16_32(ss[t]), q = xsum16_32(sq[t]);
          if (q4 == 0) *(LAS f32x2*)(smem + K1_P + ((32 * rh + 16 * (t ^ tsw) + l15) * 4 + cg) * 8) = (f32x2){s, q};
        }
      } else if (j == 10) {
        if (wave == 0) {
          const f32x4 p0 = *(LAS const f32x4*)(smem + K1_P + lane * 32), p1 = *(LAS const f32x4*)(smem + K1_P + lane * 32 + 16);
          const float s = (p0[0] + p0[2]) + (p1[0] + p1[2]), q = (p0[1] + p0[3]) + (p1[1] + p1[3]);
          u32x4 gr = {__float_as_uint(s), c.tag, __float_as_uint(q), c.tag};
          const char* xp = ws + WS_XCH + (size_t)c.ti * 64 * 128 + c.tj * 16;
          const unsigned vo = lane * 128;
          asm volatile("global_store_dwordx4 %0, %1, %2 sc1\n\ts_nop 1" ::"v"(vo), "v"(gr), "s"(xp) : "memory");
        }
      }
    }
#pragma unroll
    for (int i = 0; i < 7; ++i) {
      __builtin_amdgcn_sched_group_barrier(0x008, 1, 0);
      __builtin_amdgcn_sched_group_barrier(0x100, 1, 0);
      __builtin_amdgcn_sched_group_barrier(0x002, 5, 0);
    }
    __builtin_amdgcn_sched_barrier(0);
    if (!(K1_ABL & 4)) {
    if (ks < 4) { K1_XPIECE(ks + 3); K1_WPIECE(ks + 3, 0); K1_WPIECE(ks + 3, 1); }
    else if (ks < 18) K1_WPIECE(5 + (ks >> 1), cur);
    }
    __builtin_amdgcn_sched_barrier(0);
  }
  STAMP(0, 5);
  MARK(29);
  if (K1_ABL & 8) {
    f32x4 tsum = accs;
    _Pragma("unroll") for (int n = 0; n < 2; ++n) { tsum += accw[n] + accw1[n]; _Pragma("unroll") for (int t = 0; t < 2; ++t) tsum += acc[n][t] + acc1[n][t]; }
    if (K1_ABL & 1) { m1[0] = m1[1] = r1[0] = r1[1] = 0.f; }
    else { tsum[0] += __uint_as_float(zp[0][0][0] ^ zp[0][0][1] ^ zp[0][1][0] ^ zp[0][1][1] ^ zp[1][0][0] ^ zp[1][0][1] ^ zp[1][1][0] ^ zp[1][1][1]); }
    *(f32x4*)(ws + WS_Z1D + (size_t)(blockIdx.x * 512 + threadIdx.x) * 16) = tsum;
    STAMP(0, 8);
    return;
  }
  if (K1_ABL & 1) { m1[0] = m1[1] = r1[0] = r1[1] = 1.f; _Pragma("unroll") for (int i = 0; i < 8; ++i) zp[i >> 2][(i >> 1) & 1][i & 1] = __float_as_uint(acc[i >> 2][(i >> 1) & 1][i & 1]); }
  if (W1 && l15 == 0) {
#pragma unroll
    for (int n = 0; n < 2; ++n) *(LAS f32x4*)(smem + K1_W1 + 512 + ((cg * 2 + n) * 4 + q4) * 16) = accw1[n];
  }
  BAR();
  u32x4 gr;
  const int tid = wave * 64 + lane, row = tid >> 3, jj = tid & 7;
  const char* xp = ws + WS_XCH + (size_t)c.ti * 64 * 128;
  const unsigned vo = row * 128 + (jj < 7 ? jj : 0) * 16;
  asm volatile("global_load_dwordx4 %0, %1, %2 sc1" : "=v"(gr) : "v"(vo), "s"(xp) : "memory");
#pragma unroll
  for (int n = 0; n < 2; ++n) {
    const f32x4 w1 = *(LAS const f32x4*)(smem + K1_W1 + 512 + ((cg * 2 + n) * 4 + q4) * 16);
#pragma unroll
    for (int t = 0; t < 2; ++t) {
      const unsigned o0 = gelu_pair(acc1[n][t], w1, 0, m1[t], r1[t]), o1 = gelu_pair(acc1[n][t], w1, 1, m1[t], r1[t]);
      const int tt = 4 * c.ti + 2 * rh + (t ^ tsw), ft = 8 * c.tj + 2 * cg + n;
      *(u32x2*)(ws + WS_Z1D + ((size_t)(tt * 56 + ft) * 64 + lane) * 8) = (u32x2){o0, o1};
    }
  }
  STAMP(0, 6);
  asm volatile("s_waitcnt vmcnt(4)" : "+v"(gr)::"memory");
  for (unsigned spins = 0;; ++spins) {
    const bool ok = (gr[1] == c.tag) && (gr[3] == c.tag);
    if (__all(ok) || spins > (1u << 22)) break;
    __builtin_amdgcn_s_sleep(1);
    asm volatile("global_load_dwordx4 %0, %1, %2 sc1\n\ts_waitcnt vmcnt(0)" : "=&v"(gr) : "v"(vo), "s"(xp) : "memory");
  }
  {
    const float s = red8(jj < 7 ? __uint_as_float(gr[0]) : 0.f), q = red8(jj < 7 ? __uint_as_float(gr[2]) : 0.f);
    float mb, rr2;
    ln_scalars(s, q, 1.0f / 896.0f, mb, rr2);
    if (jj == 0) { *(LAS float*)(smem + K1_M2 + row * 4) = mb; *(LAS float*)(smem + K1_R2 + row * 4) = rr2; }
  }
  BAR();
  STAMP(0, 7);
  if (c.tj == 0 && tid == 0) __hip_atomic_store((GAS unsigned*)(ws + WS_EP + c.ti * 128), c.tag, __ATOMIC_RELAXED, __HIP_MEMORY_SCOPE_AGENT);
  {
    LAS char* tb = smem + K1_W + wave * 2560;
#pragma unroll
    for (int t = 0; t < 2; ++t) {
      const int sl = 16 * (t ^ tsw) + l15, srow = 32 * rh + sl;
      const float mb2 = *(LAS const float*)(smem + K1_M2 + srow * 4), rr2 = *(LAS const float*)(smem + K1_R2 + srow * 4);
#pragma unroll
      for (int n = 0; n < 2; ++n) {
        unsigned o[2];
#pragma unroll
        for (int p = 0; p < 2; ++p) {
          const f32x2 cc = up_bf(zp[n][t][p]) - mb2;
          const f32x2 v = up_bf(pk_bf16(cc.x, cc.y)) * rr2;
          o[p] = pk_bf16(v.x, v.y);
        }
        *(LAS u32x2*)(tb + n * 1280 + sl * 40 + q4 * 8) = (u32x2){o[0], o[1]};
      }
    }
    asm volatile("s_waitcnt lgkmcnt(0)" ::: "memory");
#pragma unroll
    for (int n = 0; n < 2; ++n) {
      LAS const char* rp = tb + n * 1280 + (8 * q4 + (l15 >> 2)) * 40 + (lane & 3) * 8;
      const s16x4 lo = tr_read(rp), hi = tr_read(rp + 4 * 40);
      const s16x8 o = __builtin_shufflevector(lo, hi, 0, 1, 2, 3, 4, 5, 6, 7);
      const int ft = 8 * c.tj + 2 * cg + n, sb = 2 * c.ti + rh;
      *(s16x8*)(ws + WS_Z2F + ((size_t)(ft * 64 + sb) * 64 + lane) * 16) = o;
    }
  }
  STAMP(0, 8);
  MARK(28);
  MARK_DUMP(0);
}

__device__ __forceinline__ void k1_xt(LAS char* smem, const unsigned short* __restrict__ x, char* __restrict__ ws, int v, int tid) {
  const int row = tid >> 3, c8 = tid & 7;
  u32x4 xv[7];
#pragma unroll
  for (int m = 0; m < 7; ++m) xv[m] = *(const u32x4*)(x + (size_t)(64 * v + row) * DM + (c8 + 8 * m) * 8);
  float s = 0.f, q = 0.f;
#pragma unroll
  for (int m = 0; m < 7; ++m)
#pragma unroll
    for (int e = 0; e < 4; ++e) { const unsigned w = xv[m][e]; s = dot2bf(w, ONES_BF, s); q = dot2bf(w, w, q); }
  s = red8(s); q = red8(q);
  float mb, rr;
  ln_scalars(s, q, 1.0f / 448.0f, mb, rr);
#pragma unroll
  for (int m = 0; m < 7; ++m)
#pragma unroll
    for (int e = 0; e < 4; ++e) {
      const unsigned w = xv[m][e];
      const f32x2 cc = up_bf(w) - mb;
      const f32x2 n2 = up_bf(pk_bf16(cc.x, cc.y)) * rr;
      const f32x2 n3 = up_bf(pk_bf16(n2.x, n2.y));
      const int d = (c8 + 8 * m) * 8 + 2 * e;
      *(LAS _Float16*)(smem + d * 144 + row * 2) = (_Float16)n3.x;
      *(LAS _Float16*)(smem + (d + 1) * 144 + row * 2) = (_Float16)n3.y;
    }
  __syncthreads();
#pragma unroll
  for (int m = 0; m < 7; ++m) {
    const int d = row + 64 * m;
    *(u32x4*)(ws + WS_XT + ((size_t)d * SEQ + 64 * v + 8 * c8) * 2) = *(LAS const u32x4*)(smem + d * 144 + c8 * 16);
  }
}

__global__ __launch_bounds__(512, 2) void k1_gemm1(const unsigned short* __restrict__ x, const unsigned short* __restrict__ w_in, char* __restrict__ ws) {
  extern __shared__ __attribute__((aligned(16))) char smem_raw[];
  LAS char* smem = (LAS char*)smem_raw;
  const int tid = threadIdx.x, lane = tid & 63;
  const int wave = __builtin_amdgcn_readfirstlane(tid >> 6);
  const int b = blockIdx.x;
  if (b >= 224) { k1_xt(smem, x, ws, b - 224, tid); return; }
  const int xcd = b & 7, rr = b >> 3;
  K1Ctx c;
  c.smem = smem; c.w_in = w_in; c.ws = ws; c.wave = wave; c.lane = lane;
  c.ti = 4 * xcd + rr / 7; c.tj = rr % 7; c.rh = wave >> 2; c.cg = wave & 3;
  const int q4 = lane >> 4, l15 = lane & 15;

  STAMP(0, 0);
  unsigned ep;
  {
    const char* epp = ws + WS_EP + c.ti * 128;
    unsigned zero = 0;
    asm volatile("global_load_dword %0, %1, %2 sc1" : "=v"(ep) : "v"(zero), "s"(epp) : "memory");
  }
  const int xrow = 8 * wave + (lane >> 3);
  c.voffx = (unsigned)(xrow * 896 + (((lane & 7) ^ ((xrow >> 1) & 7)) * 16));
  c.xsrc = (const char*)x + (size_t)c.ti * 64 * 896;
#pragma unroll
  for (int e = 0; e < 2; ++e) {
    const int r = 8 * wave + 4 * e + q4;
    const int k = (r & 32) + 8 * ((r >> 2) & 3) + 4 * ((r >> 4) & 1) + (r & 3);
    c.voffw[e] = (unsigned)(k * 3584 + ((l15 ^ (2 * (r & 7))) * 16) + c.tj * 256);
  }
#pragma unroll
  for (int g = 0; g < 3; ++g) { K1_XPIECE(g); K1_WPIECE(g, 0); K1_WPIECE(g, 1); }
  {
    const int tsw = c.cg & 1, sw = (l15 >> 1) & 7;
#pragma unroll
    for (int t = 0; t < 2; ++t) {
      const int row0 = 32 * c.rh + 16 * (t ^ tsw) + l15;
      c.xb[t][0] = smem + K1_X + row0 * 128 + ((q4 ^ sw) * 16);
      c.xb[t][1] = smem + K1_X + row0 * 128 + (((4 + q4) ^ sw) * 16);
    }
    const int rrow = 4 * q4 + (l15 >> 2), p4 = lane & 3, X = 2 * (rrow & 7);
    c.bb0 = smem + K1_W + rrow * 256 + (((4 * c.cg + (p4 >> 1)) ^ X) * 16) + (p4 & 1) * 8;
    c.bb1 = smem + K1_W + rrow * 256 + (((4 * c.cg + 2 + (p4 >> 1)) ^ X) * 16) + (p4 & 1) * 8;
  }
  c.tag = 0;
  {
    K1Ctx c2 = c;
    asm volatile("s_waitcnt vmcnt(9)" : "+v"(ep)::"memory");
    c2.tag = ep + 1u;
    const bool w1 = c.rh == 0, sqv = (c.cg >> 1) != 0;
    if (w1) { if (sqv) k1_main<true, true>(c2); else k1_main<true, false>(c2); }
    else { if (sqv) k1_main<false, true>(c2); else k1_main<false, false>(c2); }
  }
}
#undef K1_FRAGS
#undef K1_WPIECE
#undef K1_XPIECE

constexpr int K2_RING = 0;
constexpr int K2_CH = 65536;
constexpr int K2_CHS = 49152;
constexpr int CH_FT = 0;
constexpr int CH_FW = 16416, CH_FS = 288;
constexpr int CH_CS = 4160;
constexpr int CH_U = 33280;
constexpr int CH_X = CH_U + 4608;
constexpr int CH_T = CH_X + 4224;
constexpr int CH_CNT = CH_T + 6144;
constexpr int K2_LDS = 163840;

__device__ __forceinline__ void lds_sync(LAS char* cntp, unsigned target, int lane) {
  LAS unsigned* cnt = (LAS unsigned*)cntp;
  asm volatile("s_waitcnt lgkmcnt(0)" ::: "memory");
  if (lane == 0) __hip_atomic_fetch_add(cnt, 1u, __ATOMIC_RELAXED, __HIP_MEMORY_SCOPE_WORKGROUP);
  while ((unsigned)__builtin_amdgcn_readfirstlane(*(volatile LAS unsigned*)cnt) < target) __builtin_amdgcn_s_sleep(1);
  asm volatile("" ::: "memory");
}
__device__ __forceinline__ __amdgpu_buffer_rsrc_t mk_rsrc(const void* p, unsigned bytes) {
  return __builtin_amdgcn_make_buffer_rsrc((void*)p, (short)0, (int)bytes, 0x00020000);
}
__device__ __forceinline__ void bglds16(__amdgpu_buffer_rsrc_t rs, unsigned voff, unsigned soff, unsigned lds) {
  asm volatile("s_mov_b32 m0, %3\n\ts_nop 0\n\tbuffer_load_dwordx4 %0, %1, %2 offen lds" ::"v"(voff), "s"(rs), "s"(soff), "s"(lds) : "memory");
}
__device__ __forceinline__ void bload16(u32x4& dst, __amdgpu_buffer_rsrc_t rs, unsigned voff, unsigned soff) {
  asm volatile("buffer_load_dwordx4 %0, %1, %2, %3 offen" : "=v"(dst) : "v"(voff), "s"(rs), "s"(soff) : "memory");
}

template <int W>
__device__ __forceinline__ void k2_epi(LAS char* smem, char* __restrict__ ws, f32x4 (&acc)[7][4], const u32x2 (&z1v)[7], float bs, int lane, int tt, int J) {
  LAS char* cnt = smem + K2_CH + CH_CNT + 64;
  LAS char* mine = smem + K2_RING + W * 16384 + lane * 16;
  LAS char* other = smem + K2_RING + (W ^ 2) * 16384 + lane * 16;
  LAS char* o2 = smem + K2_RING + (W ^ 3) * 16384 + lane * 16;
  constexpr int K0 = W & 2, S0 = K0 ^ 2;
#pragma unroll
  for (int n = 0; n < 7; ++n) { *(LAS f32x4*)(mine + n * 1024) = acc[n][S0]; *(LAS f32x4*)(mine + (7 + n) * 1024) = acc[n][S0 + 1]; }
  lds_sync(cnt, 4, lane);
#pragma unroll
  for (int n = 0; n < 7; ++n) { acc[n][K0] += *(LAS const f32x4*)(other + n * 1024); acc[n][K0 + 1] += *(LAS const f32x4*)(other + (7 + n) * 1024); }
  asm volatile("s_waitcnt lgkmcnt(0)" ::: "memory");
#pragma unroll
  for (int n = 0; n < 7; ++n) *(LAS f32x4*)(other + n * 1024) = acc[n][W ^ 1];
  lds_sync(cnt, 8, lane);
  STAMP(1, 2);
  unsigned hp[7][2];
#pragma unroll
  for (int n = 0; n < 7; ++n) {
    const f32x4 v = acc[n][W] + *(LAS const f32x4*)(o2 + n * 1024);
#pragma unroll
    for (int p = 0; p < 2; ++p) {
      const f32x2 g0 = up_bf(pk_bf16(v[2 * p], v[2 * p + 1])) + bs;
      const unsigned zw = z1v[n][p];
      const f32x2 h = up_bf(pk_bf16(g0.x, g0.y)) * up_bf(zw);
      hp[n][p] = pk_bf16(h.x, h.y);
    }
  }
  char* hb = ws + WS_HF + ((size_t)(tt * 28 + (7 * J >> 1)) * 64 + lane) * 16;
  if (!(J & 1)) {
#pragma unroll
    for (int p = 0; p < 3; ++p) *(u32x4*)(hb + p * 1024) = (u32x4){hp[2 * p][0], hp[2 * p][1], hp[2 * p + 1][0], hp[2 * p + 1][1]};
    *(u32x2*)(hb + 3 * 1024) = (u32x2){hp[6][0], hp[6][1]};
  } else {
    *(u32x2*)(hb + 8) = (u32x2){hp[0][0], hp[0][1]};
#pragma unroll
    for (int p = 0; p < 3; ++p) *(u32x4*)(hb + (p + 1) * 1024) = (u32x4){hp[2 * p + 1][0], hp[2 * p + 1][1], hp[2 * p + 2][0], hp[2 * p + 2][1]};
  }
}

__device__ __forceinline__ void k2_gemm(LAS char* smem, const unsigned short* __restrict__ w_sp, const unsigned short* __restrict__ b_sp, char* __restrict__ ws,
                                        int wave, int lane, int T, int J, int rot) {
  const __amdgpu_buffer_rsrc_t zs = mk_rsrc(ws + WS_Z2F + (size_t)(7 * J * 64 + 16 * wave) * 1024, 7 * 65536 - 16 * wave * 1024);
  const __amdgpu_buffer_rsrc_t wr = mk_rsrc((const char*)w_sp + (size_t)T * 64 * 4096 + wave * 1024, 64 * 4096 - wave * 1024);
  const unsigned voffz = lane * 16;
  const unsigned voffw0 = (lane >> 3) * 4096 + (((lane & 7) ^ ((lane >> 4) & 3)) * 16);
  const unsigned voffw1 = (8 + (lane >> 3)) * 4096 + (((lane & 7) ^ (4 + ((lane >> 4) & 3))) * 16);
  const unsigned ring = K2_RING + wave * 16384;
  LAS const char* wrd0 = smem + ring + (lane & 15) * 128 + (((lane >> 4) ^ ((lane >> 1) & 7)) * 16);
  LAS const char* wrd1 = smem + ring + (lane & 15) * 128 + (((4 + (lane >> 4)) ^ ((lane >> 1) & 7)) * 16);
  u32x4 z[3][7];
  f32x4 acc[7][4];
#pragma unroll
  for (int n = 0; n < 7; ++n)
#pragma unroll
    for (int m = 0; m < 4; ++m) acc[n][m] = (f32x4){0.f, 0.f, 0.f, 0.f};
#define K2_ISSUE_W(pp)                                                                                      \
  do {                                                                                                      \
    const unsigned kw_ = (unsigned)((2 * (pp) + rot) & 15) * 64;                                            \
    _Pragma("unroll") for (int m = 0; m < 4; ++m) {                                                         \
      bglds16(wr, voffw0, m * 65536 + kw_, ring + ((pp) & 1) * 8192 + m * 2048);                            \
      bglds16(wr, voffw1, m * 65536 + kw_, ring + ((pp) & 1) * 8192 + m * 2048 + 1024);                     \
    }                                                                                                       \
  } while (0)
#define K2_ISSUE_Z(it)                                                                                      \
  do {                                                                                                      \
    const unsigned kz_ = (unsigned)(((it) + rot) & 15) * 1024;                                              \
    _Pragma("unroll") for (int n = 0; n < 7; ++n) bload16(z[(it) % 3][n], zs, voffz, n * 65536 + kz_);      \
  } while (0)
  STAMP(1, 0);
  K2_ISSUE_W(0); K2_ISSUE_Z(0); K2_ISSUE_Z(1); K2_ISSUE_W(1); K2_ISSUE_Z(2);
  const int tt = 4 * T + wave;
  u32x2 z1v[7];
  unsigned bsw;
#define K2_WAITZ(N) asm volatile("s_waitcnt vmcnt(" #N ")" : "+v"(zc[0]), "+v"(zc[1]), "+v"(zc[2]), "+v"(zc[3]), "+v"(zc[4]), "+v"(zc[5]), "+v"(zc[6])::"memory")
#pragma unroll
  for (int it = 0; it < 16; ++it) {
    u32x4(&zc)[7] = z[it % 3];
    if (it <= 13) K2_WAITZ(22); else if (it == 14) K2_WAITZ(15); else K2_WAITZ(8);
    s16x8 wf[4];
#pragma unroll
    for (int m = 0; m < 4; ++m) wf[m] = *(LAS const s16x8*)(((it & 1) ? wrd1 : wrd0) + ((it >> 1) & 1) * 8192 + m * 2048);
    const unsigned kz = (unsigned)((it + 3 + rot) & 15) * 1024, kw = (unsigned)((it + 3 + rot) & 15) * 64;
#pragma unroll
    for (int n = 0; n < 7; ++n) {
#pragma unroll
      for (int m = 0; m < 4; ++m) acc[n][m] = mfma_bf(__builtin_bit_cast(s16x8, zc[n]), wf[m], acc[n][m]);
      __builtin_amdgcn_sched_barrier(0);
      if (it + 3 < 16) {
        bload16(zc[n], zs, voffz, n * 65536 + kz);
        if ((it & 1) && n >= 1 && n <= 4) {
          const int m = n - 1;
          bglds16(wr, voffw0, m * 65536 + kw, ring + (((it + 3) >> 1) & 1) * 8192 + m * 2048);
          bglds16(wr, voffw1, m * 65536 + kw, ring + (((it + 3) >> 1) & 1) * 8192 + m * 2048 + 1024);
        }
      }
      if (it == 13) {
        const char* zb = ws + WS_Z1D + (size_t)(tt * 56 + 7 * J + n) * 512;
        const unsigned vo = lane * 8;
        asm volatile("global_load_dwordx2 %0, %1, %2" : "=v"(z1v[n]) : "v"(vo), "s"(zb) : "memory");
        if (n == 6) {
          const unsigned vb = (unsigned)(16 * tt + (lane & 15)) * 2u;
          asm volatile("global_load_ushort %0, %1, %2" : "=v"(bsw) : "v"(vb), "s"(b_sp) : "memory");
        }
      }
      __builtin_amdgcn_sched_barrier(0);
    }
  }
#undef K2_WAITZ
#undef K2_ISSUE_W
#undef K2_ISSUE_Z
  STAMP(1, 1);
  asm volatile("s_waitcnt vmcnt(0)" : "+v"(z1v[0]), "+v"(z1v[1]), "+v"(z1v[2]), "+v"(z1v[3]), "+v"(z1v[4]), "+v"(z1v[5]), "+v"(z1v[6]), "+v"(bsw)::"memory");
  const float bs = bf2f((unsigned short)bsw);
  if (wave == 0) k2_epi<0>(smem, ws, acc, z1v, bs, lane, tt, J);
  else if (wave == 1) k2_epi<1>(smem, ws, acc, z1v, bs, lane, tt, J);
  else if (wave == 2) k2_epi<2>(smem, ws, acc, z1v, bs, lane, tt, J);
  else k2_epi<3>(smem, ws, acc, z1v, bs, lane, tt, J);
  STAMP(1, 3);
}

__device__ __forceinline__ f32x2 cmul(f32x2 a, f32x2 b) {
  f32x2 t, r;
  asm("v_pk_mul_f32 %0, %1, %2 op_sel:[0,0] op_sel_hi:[0,1]" : "=v"(t) : "v"(a), "v"(b));
  asm("v_pk_fma_f32 %0, %1, %2, %3 op_sel:[1,1,0] op_sel_hi:[1,0,1] neg_lo:[1,0,0]" : "=v"(r) : "v"(a), "v"(b), "v"(t));
  return r;
}
__device__ __forceinline__ unsigned hmul(unsigned a, unsigned b) {
  unsigned t, r;
  asm("v_pk_mul_f16 %0, %1, %2 op_sel:[0,0] op_sel_hi:[0,1]" : "=v"(t) : "v"(a), "v"(b));
  asm("v_pk_fma_f16 %0, %1, %2, %3 op_sel:[1,1,0] op_sel_hi:[1,0,1] neg_lo:[1,0,0]" : "=v"(r) : "v"(a), "v"(b), "v"(t));
  return r;
}
__device__ __forceinline__ unsigned hcv(f32x2 v) { return pk_f16(v.x, v.y); }
__device__ __forceinline__ f32x2 cexp_p(float al, float be, float p) {
  const float e = __builtin_amdgcn_exp2f(al * p), th = __builtin_amdgcn_fractf(be * p);
  return (f32x2){__builtin_amdgcn_cosf(th), __builtin_amdgcn_sinf(th)} * e;
}
__device__ __forceinline__ f32x4 mfma_h(u32x4 a, u32x4 b, f32x4 c) {
  return __builtin_amdgcn_mfma_f32_16x16x32_f16(__builtin_bit_cast(f16x8, a), __builtin_bit_cast(f16x8, b), c, 0, 0, 0);
}
struct PosTab { unsigned short v[2048]; };
constexpr int pos_bf16(int j) {
  if (j < 256) return j;
  int sh = 1; while ((j >> sh) >= 256) ++sh;
  const int q = j >> sh, r = j & ((1 << sh) - 1), h = 1 << (sh - 1);
  return (r > h || (r == h && (q & 1)) ? q + 1 : q) << sh;
}
constexpr PosTab make_postab() {
  PosTab t = {};
  for (int x = 0; x < 2048; ++x) {
    const int p = pos_bf16(2047 - x);
    int col = 0, a = 0;
    if (p < 256) { a = p >> 4; col = p & 15; }
    else if (p < 512) { const int r = p - 256; a = r >> 4; col = 16 + ((r & 15) >> 1); }
    else if (p < 1024) { const int r = p - 512; a = (r & 255) >> 4; col = 24 + 4 * (r >> 8) + ((r & 15) >> 2); }
    else if (p < 2048) { const int r = p - 1024; a = (r & 255) >> 4; col = 32 + 2 * (r >> 8) + ((r & 15) >> 3); }
    else { a = 0; col = 40; }
    t.v[x] = (unsigned short)((col * 16 + a) * 4);
  }
  return t;
}
__device__ const PosTab POSTAB = make_postab();

template <int HW>
__device__ __forceinline__ void s4d_conv(LAS char* cb, char* __restrict__ ws, int d, int lane, float iscale, float dsk) {
  constexpr int NF = HW ? 7 : 8;
  f32x4 acc[8] = {};
  const int r = lane & 15, h = lane >> 4;
  LAS const char* ab = cb + CH_FT + ((7 - r) & 7) * CH_CS + (255 - (r >> 3) - 2 * (h >> 1) + (h & 1)) * 16 - (64 * 7 + 512 * 7);
  LAS const char* bb = cb + CH_U + 512 + 32 * r - 32 * (h >> 1) + 16 * (h & 1) - 64 * 7;
  u32x4 A[2][8], B[2][8];
#define CONV_LOAD(ar, buf)                                                                                   \
  _Pragma("unroll") for (int k = 0; k < NF; ++k) {                                                           \
    B[buf][k] = *(LAS const u32x4*)(bb + 512 * k + 64 * (7 - (ar)));                                         \
    A[buf][k] = *(LAS const u32x4*)(ab + 512 * (7 - k) + 64 * (7 - (ar)));                                   \
  }
  CONV_LOAD(0, 0);
#pragma unroll
  for (int ar = 0; ar < 8; ++ar) {
    __builtin_amdgcn_sched_barrier(0);
    if (ar < 7) { CONV_LOAD(ar + 1, (ar + 1) & 1); }
#pragma unroll
    for (int al = 0; al < NF; ++al)
#pragma unroll
      for (int be = 0; be < NF; ++be) {
        const int tau = al + be;
        if (HW ? (tau >= 4 && tau <= 6) : (tau <= 3 || tau == 7)) acc[tau] = mfma_h(A[ar & 1][al], B[ar & 1][be], acc[tau]);
      }
#pragma unroll
    for (int i = 0; i < 16; ++i) { __builtin_amdgcn_sched_group_barrier(0x008, 1, 0); __builtin_amdgcn_sched_group_barrier(0x100, 1, 0); }
    __builtin_amdgcn_sched_barrier(0);
  }
#undef CONV_LOAD
  STAMP(1, 4);
  const int q = lane >> 4;
#pragma unroll
  for (int t = 0; t < 8; ++t) {
    if (HW ? (t >= 4 && t <= 6) : (t <= 3 || t == 7)) {
      const f32x4 v = acc[t];
      const int l0 = 256 * t + 16 * r + 4 * q;
      const u32x2 uw = *(LAS const u32x2*)(cb + CH_U + 512 + l0 * 2);
      const unsigned uw0 = uw[0], uw1 = uw[1];
      const f16x2 ua = __builtin_bit_cast(f16x2, uw0), ub = __builtin_bit_cast(f16x2, uw1);
      const float uu[4] = {(float)ua[0], (float)ua[1], (float)ub[0], (float)ub[1]};
      float sv[4];
#pragma unroll
      for (int j = 0; j < 4; ++j) sv[j] = rbf(v[j] * iscale) + rbf(uu[j] * dsk);
      *(u32x2*)(ws + WS_ST + ((size_t)d * SEQ + l0) * 2) = (u32x2){pk_bf16(sv[0], sv[1]), pk_bf16(sv[2], sv[3])};
    }
  }
}

__device__ __forceinline__ void k2_s4d(LAS char* cb, const unsigned short* __restrict__ logA, const unsigned short* __restrict__ Aim, const unsigned short* __restrict__ Cre,
                                       const unsigned short* __restrict__ Cim, const unsigned short* __restrict__ logdt, const unsigned short* __restrict__ Dsk,
                                       char* __restrict__ ws, int d, int hw, int lane) {
  LAS char* cnt = cb + CH_CNT;
  unsigned la_[2], ai_[2], cr_[4], ci_[4], ldt_, dsk_;
  u32x4 u0, u1, pt0, pt1;
  {
    const unsigned vo = (unsigned)(d * NST + 128 * hw + lane) * 2u, vo2 = (unsigned)(d * NST + ((128 * hw + 128 + lane) & 255)) * 2u, vd = (unsigned)d * 2u;
    asm volatile("global_load_ushort %0, %1, %2" : "=v"(la_[0]) : "v"(vo), "s"(logA) : "memory");
    asm volatile("global_load_ushort %0, %1, %2 offset:128" : "=v"(la_[1]) : "v"(vo), "s"(logA) : "memory");
    asm volatile("global_load_ushort %0, %1, %2" : "=v"(ai_[0]) : "v"(vo), "s"(Aim) : "memory");
    asm volatile("global_load_ushort %0, %1, %2 offset:128" : "=v"(ai_[1]) : "v"(vo), "s"(Aim) : "memory");
    asm volatile("global_load_ushort %0, %1, %2" : "=v"(cr_[0]) : "v"(vo), "s"(Cre) : "memory");
    asm volatile("global_load_ushort %0, %1, %2 offset:128" : "=v"(cr_[1]) : "v"(vo), "s"(Cre) : "memory");
    asm volatile("global_load_ushort %0, %1, %2" : "=v"(cr_[2]) : "v"(vo2), "s"(Cre) : "memory");
    asm volatile("global_load_ushort %0, %1, %2 offset:128" : "=v"(cr_[3]) : "v"(vo2), "s"(Cre) : "memory");
    asm volatile("global_load_ushort %0, %1, %2" : "=v"(ci_[0]) : "v"(vo), "s"(Cim) : "memory");
    asm volatile("global_load_ushort %0, %1, %2 offset:128" : "=v"(ci_[1]) : "v"(vo), "s"(Cim) : "memory");
    asm volatile("global_load_ushort %0, %1, %2" : "=v"(ci_[2]) : "v"(vo2), "s"(Cim) : "memory");
    asm volatile("global_load_ushort %0, %1, %2 offset:128" : "=v"(ci_[3]) : "v"(vo2), "s"(Cim) : "memory");
    asm volatile("global_load_ushort %0, %1, %2" : "=v"(ldt_) : "v"(vd), "s"(logdt) : "memory");
    asm volatile("global_load_ushort %0, %1, %2" : "=v"(dsk_) : "v"(vd), "s"(Dsk) : "memory");
    const char* ub = ws + WS_XT + (size_t)d * SEQ * 2 + hw * 2048;
    const unsigned vu = lane * 16;
    asm volatile("global_load_dwordx4 %0, %1, %2" : "=v"(u0) : "v"(vu), "s"(ub) : "memory");
    asm volatile("global_load_dwordx4 %0, %1, %2 offset:1024" : "=v"(u1) : "v"(vu), "s"(ub) : "memory");
    const char* pb = (const char*)POSTAB.v + hw * 2048;
    const unsigned vp = lane * 32;
    asm volatile("global_load_dwordx4 %0, %1, %2" : "=v"(pt0) : "v"(vp), "s"(pb) : "memory");
    asm volatile("global_load_dwordx4 %0, %1, %2 offset:16" : "=v"(pt1) : "v"(vp), "s"(pb) : "memory");
    if (hw == 0 && lane < 32) *(LAS u32x4*)(cb + CH_U + lane * 16) = (u32x4){0u, 0u, 0u, 0u};
    if (hw == 1 && lane < 4) *(LAS u32x4*)(cb + CH_X + 4096 + lane * 16) = (u32x4){0u, 0u, 0u, 0u};
  }
  BAR();
#define K2_PRIO 3
  __builtin_amdgcn_s_setprio(K2_PRIO);
  STAMP(1, 0);
  asm volatile("s_waitcnt vmcnt(0)" : "+v"(la_[0]), "+v"(la_[1]), "+v"(ai_[0]), "+v"(ai_[1]), "+v"(cr_[0]), "+v"(cr_[1]), "+v"(cr_[2]), "+v"(cr_[3]), "+v"(ci_[0]), "+v"(ci_[1]),
               "+v"(ci_[2]), "+v"(ci_[3]), "+v"(ldt_), "+v"(dsk_), "+v"(u0), "+v"(u1), "+v"(pt0), "+v"(pt1)::"memory");
  float cr[4], ci[4];
#pragma unroll
  for (int p = 0; p < 4; ++p) { cr[p] = bf2f((unsigned short)cr_[p]); ci[p] = bf2f((unsigned short)ci_[p]); }
  const float dt = rbf(expf(bf2f((unsigned short)ldt_))), dsk = bf2f((unsigned short)dsk_);
  *(LAS u32x4*)(cb + CH_U + 512 + hw * 2048 + lane * 16) = u0;
  *(LAS u32x4*)(cb + CH_U + 512 + hw * 2048 + 1024 + lane * 16) = u1;
  float sabs = 0.f;
#pragma unroll
  for (int p = 0; p < 4; ++p) sabs += __builtin_fabsf(cr[p]) + __builtin_fabsf(ci[p]);
  sabs = xsum16_32(red16(sabs));
  sabs *= 2.0f * dt;
  int ex = (int)((__float_as_uint(sabs) >> 23) & 0xff) - 126;
  ex = ex < -100 ? -100 : (ex > 100 ? 100 : ex);
  const float scale = __uint_as_float((unsigned)(127 + 15 - ex) << 23), iscale = __uint_as_float((unsigned)(127 - 15 + ex) << 23);
  STAMP(1, 1);
  f32x4 kacc[3] = {};
  LAS char* fw = cb + CH_FT + hw * CH_FW;
#pragma unroll 1
  for (int p = 0; p < 2; ++p) {
    const float lap = bf2f((unsigned short)(p ? la_[1] : la_[0])), aip = bf2f((unsigned short)(p ? ai_[1] : ai_[0]));
    const float crp = p ? cr[1] : cr[0], cip = p ? ci[1] : ci[0];
    const float lre = -rbf(expf(lap)) * dt, lim = aip * dt;
    const float al = lre * 1.4426950408889634f, be = lim * 0.15915494309189535f;
    LAS char* wp = fw + lane * 4;
#define PUTH(row, v) *(LAS unsigned*)(wp + (row) * CH_FS) = (v)
    {
      const f32x2 w = cexp_p(al, be, 16.f);
      const float cs = 2.0f * dt * scale;
      const f32x2 c0 = {crp * cs, cip * cs};
      const f32x2 w2 = cmul(w, w), c1 = cmul(c0, w), w4 = cmul(w2, w2), c2 = cmul(c0, w2), c3 = cmul(c1, w2), w8 = cmul(w4, w4), w12 = cmul(w8, w4);
      const unsigned hc[4] = {hcv(c0), hcv(c1), hcv(c2), hcv(c3)}, hw4[3] = {hcv(w4), hcv(w8), hcv(w12)};
#pragma unroll
      for (int j = 0; j < 4; ++j) {
        PUTH(j, hc[j]);
#pragma unroll
        for (int k = 0; k < 3; ++k) PUTH(4 * (k + 1) + j, hmul(hc[j], hw4[k]));
      }
    }
    {
      const f32x2 e1 = cexp_p(al, -be, 1.f);
      const f32x2 e2 = cmul(e1, e1), e3 = cmul(e2, e1), e4 = cmul(e2, e2), e8 = cmul(e4, e4), e12 = cmul(e8, e4);
      unsigned E[16];
      E[0] = 0x00003c00u; E[1] = hcv(e1); E[2] = hcv(e2); E[3] = hcv(e3); E[4] = hcv(e4); E[8] = hcv(e8); E[12] = hcv(e12);
#pragma unroll
      for (int m = 1; m < 4; ++m)
#pragma unroll
        for (int j = 1; j < 4; ++j) E[4 * m + j] = hmul(E[4 * m], E[j]);
#pragma unroll
      for (int k = 0; k < 16; ++k) PUTH(16 + k, E[k]);
      const f32x2 z256 = cexp_p(al, -be, 256.f), z512 = cexp_p(al, -be, 512.f), z1024 = cexp_p(al, -be, 1024.f), z2048 = cexp_p(al, -be, 2048.f);
      const f32x2 z768 = cmul(z512, z256);
      const unsigned h256 = hcv(z256), h512 = hcv(z512), h768 = hcv(z768);
      PUTH(32, h256);
#pragma unroll
      for (int k = 1; k < 8; ++k) PUTH(32 + k, hmul(h256, E[2 * k]));
      PUTH(40, h512); PUTH(44, h768);
#pragma unroll
      for (int k = 1; k < 4; ++k) { PUTH(40 + k, hmul(h512, E[4 * k])); PUTH(44 + k, hmul(h768, E[4 * k])); }
      const unsigned hb[4] = {hcv(z1024), hcv(cmul(z1024, z256)), hcv(cmul(z1024, z512)), hcv(cmul(z1024, z768))};
#pragma unroll
      for (int k = 0; k < 4; ++k) { PUTH(48 + 2 * k, hb[k]); PUTH(49 + 2 * k, hmul(hb[k], E[8])); }
      PUTH(56, hcv(z2048));
    }
#undef PUTH
    asm volatile("s_waitcnt lgkmcnt(0)" ::: "memory");
    LAS const char* rp = fw + (lane & 15) * CH_FS + (lane >> 4) * 16;
    u32x4 fa[4], fb[4][3];
#pragma unroll
    for (int kk = 0; kk < 4; ++kk) {
      fa[kk] = *(LAS const u32x4*)(rp + kk * 64);
#pragma unroll
      for (int t = 0; t < 3; ++t) fb[kk][t] = *(LAS const u32x4*)(rp + (16 + 16 * t) * CH_FS + kk * 64);
    }
#pragma unroll
    for (int kk = 0; kk < 4; ++kk)
#pragma unroll
      for (int t = 0; t < 3; ++t) kacc[t] = mfma_h(fa[kk], fb[kk][t], kacc[t]);
    asm volatile("" ::: "memory");
  }
  STAMP(1, 2);
#pragma unroll
  for (int t = 0; t < 3; ++t) *(LAS f32x4*)(cb + CH_T + hw * 3072 + (16 * t + (lane & 15)) * 64 + (lane >> 4) * 16) = kacc[t];
  lds_sync(cnt, 2, lane);
  {
    const int x0 = 1024 * hw + 16 * lane;
    const unsigned po[8] = {pt0[0], pt0[1], pt0[2], pt0[3], pt1[0], pt1[1], pt1[2], pt1[3]};
    unsigned w[8];
#pragma unroll
    for (int k = 0; k < 8; ++k) {
      LAS const char* a0 = cb + CH_T + (po[k] & 0xffffu);
      LAS const char* a1 = cb + CH_T + (po[k] >> 16);
      w[k] = pk_f16(*(LAS const float*)a0 + *(LAS const float*)(a0 + 3072), *(LAS const float*)a1 + *(LAS const float*)(a1 + 3072));
    }
    *(LAS u32x4*)(cb + CH_X + x0 * 2) = (u32x4){w[0], w[1], w[2], w[3]};
    *(LAS u32x4*)(cb + CH_X + x0 * 2 + 16) = (u32x4){w[4], w[5], w[6], w[7]};
  }
  lds_sync(cnt, 4, lane);
#pragma unroll
  for (int it = 0; it < 3; ++it) {
    const int y = hw * 64 + lane + 128 * it;
    if (it < 2 || y <= 256) {
      const u32x4 r0 = *(LAS const u32x4*)(cb + CH_X + y * 16), r1 = *(LAS const u32x4*)(cb + CH_X + y * 16 + 16);
      unsigned W[8] = {r0[0], r0[1], r0[2], r0[3], r1[0], r1[1], r1[2], r1[3]}, V[7];
#pragma unroll
      for (int i = 0; i < 7; ++i) V[i] = __builtin_amdgcn_alignbit(W[i + 1], W[i], 16);
#pragma unroll
      for (int m = 0; m < 4; ++m) {
        *(LAS u32x4*)(cb + CH_FT + (2 * m) * CH_CS + y * 16) = (u32x4){W[m], W[m + 1], W[m + 2], W[m + 3]};
        *(LAS u32x4*)(cb + CH_FT + (2 * m + 1) * CH_CS + y * 16) = (u32x4){V[m], V[m + 1], V[m + 2], V[m + 3]};
      }
    }
  }
  lds_sync(cnt, 6, lane);
  STAMP(1, 3);
  if (hw) s4d_conv<1>(cb, ws, d, lane, iscale, dsk); else s4d_conv<0>(cb, ws, d, lane, iscale, dsk);
  STAMP(1, 5);
}

__global__ __launch_bounds__(512, 2) void k2_mix(const unsigned short* __restrict__ w_sp, const unsigned short* __restrict__ b_sp, const unsigned short* __restrict__ logA,
                                                 const unsigned short* __restrict__ Aim, const unsigned short* __restrict__ Cre, const unsigned short* __restrict__ Cim,
                                                 const unsigned short* __restrict__ logdt, const unsigned short* __restrict__ Dsk, const unsigned short* __restrict__ w_out,
                                                 char* __restrict__ ws) {
  extern __shared__ __attribute__((aligned(16))) char smem_raw[];
  LAS char* smem = (LAS char*)smem_raw;
  const int tid = threadIdx.x, lane = tid & 63;
  const int wave = __builtin_amdgcn_readfirstlane(tid >> 6);
  const int b = blockIdx.x, xcd = b & 7, rr = b >> 3;
  if (tid < 64) {
    if (lane < 32) *(LAS unsigned*)(smem + K2_CH + CH_CNT + lane * 4) = 0u;
    else *(LAS unsigned*)(smem + K2_CH + K2_CHS + CH_CNT + (lane - 32) * 4) = 0u;
  }
  if (wave < 4) {
    BAR();
    const int T = 8 * (xcd >> 1) + (rr & 7), J = 4 * (xcd & 1) + (rr >> 3);
#define K2_ROT 0
    k2_gemm(smem, w_sp, b_sp, ws, wave, lane, T, J, K2_ROT == 1 ? (2 * (rr & 7) + 4 * (rr >> 3)) & 15 : K2_ROT == 2 ? 2 * (rr & 1) : 0);
  } else {
    const int sw = wave - 4;
    if (b < 224) {
      k2_s4d(smem + K2_CH + (sw >> 1) * K2_CHS, logA, Aim, Cre, Cim, logdt, Dsk, ws, 2 * b + (sw >> 1), sw & 1, lane);
    } else {
      BAR();
      for (int fr = (b - 224) * 4 + sw; fr < 784; fr += 128) {
        const int dt = fr / 28, fb = fr - dt * 28;
        const int dcol = 16 * dt + (lane & 15), q = lane >> 4;
        unsigned short v[8];
#pragma unroll
        for (int jj = 0; jj < 8; ++jj) v[jj] = w_out[(size_t)(32 * fb + 16 * (jj >> 2) + 4 * q + (jj & 3)) * DM + dcol];
        u32x4 o = {(unsigned)v[0] | ((unsigned)v[1] << 16), (unsigned)v[2] | ((unsigned)v[3] << 16), (unsigned)v[4] | ((unsigned)v[5] << 16), (unsigned)v[6] | ((unsigned)v[7] << 16)};
        *(u32x4*)(ws + WS_WOF + ((size_t)fr * 64 + lane) * 16) = o;
      }
    }
  }
}

constexpr int K3_LDS = 49152 + 8448;
__global__ __launch_bounds__(256, 1) void k3_out(const unsigned short* __restrict__ x, char* __restrict__ ws, unsigned short* __restrict__ out) {
  extern __shared__ __attribute__((aligned(16))) char smem_raw[];
  LAS char* smem = (LAS char*)smem_raw;
  const int tid = threadIdx.x, lane = tid & 63;
  const int wave = __builtin_amdgcn_readfirstlane(tid >> 6);
  const int b = blockIdx.x, xcd = b & 7, rr = b >> 3;
  const int T = 4 * xcd + rr / 7, Dt = rr % 7;
  STAMP(2, 0);
  const u32x4* hp = (const u32x4*)(ws + WS_HF + ((size_t)(4 * T * 28 + 7 * wave) * 64 + lane) * 16);
  const u32x4* wp = (const u32x4*)(ws + WS_WOF + ((size_t)(4 * Dt * 28 + 7 * wave) * 64 + lane) * 16);
  u32x4 hf[7][4], wf[7][4];
#pragma unroll
  for (int k = 0; k < 7; ++k)
#pragma unroll
    for (int m = 0; m < 4; ++m) { hf[k][m] = hp[(m * 28 + k) * 64]; wf[k][m] = wp[(m * 28 + k) * 64]; }
  const int erow = tid >> 2, ec = tid & 3;
  const u32x4 st0 = *(const u32x4*)(ws + WS_ST + ((size_t)(64 * Dt + erow) * SEQ + 64 * T + 16 * ec) * 2);
  const u32x4 st1 = *(const u32x4*)(ws + WS_ST + ((size_t)(64 * Dt + erow) * SEQ + 64 * T + 16 * ec + 8) * 2);
  const u32x4 x0 = *(const u32x4*)(x + (size_t)(64 * T + erow) * DM + 64 * Dt + 16 * ec);
  const u32x4 x1 = *(const u32x4*)(x + (size_t)(64 * T + erow) * DM + 64 * Dt + 16 * ec + 8);
  __builtin_amdgcn_sched_barrier(0);
  f32x4 acc[4][4];
#pragma unroll
  for (int n = 0; n < 4; ++n)
#pragma unroll
    for (int m = 0; m < 4; ++m) acc[n][m] = (f32x4){0.f, 0.f, 0.f, 0.f};
#pragma unroll
  for (int k = 0; k < 7; ++k)
#pragma unroll
    for (int n = 0; n < 4; ++n)
#pragma unroll
      for (int m = 0; m < 4; ++m) acc[n][m] = mfma_bf(__builtin_bit_cast(s16x8, wf[k][n]), __builtin_bit_cast(s16x8, hf[k][m]), acc[n][m]);
  STAMP(2, 1);
  LAS char* sl = smem + 49152;
  {
    LAS unsigned* p = (LAS unsigned*)(sl + erow * 132 + ec * 32);
#pragma unroll
    for (int e = 0; e < 4; ++e) { p[e] = st0[e]; p[4 + e] = st1[e]; }
  }
#pragma unroll
  for (int m = 0; m < 4; ++m) {
    if (m != wave) {
      const int slot = m > wave ? m - 1 : m;
#pragma unroll
      for (int n = 0; n < 4; ++n) *(LAS f32x4*)(smem + ((wave * 3 + slot) * 4 + n) * 1024 + lane * 16) = acc[n][m];
    }
  }
  __syncthreads();
  f32x4 o[4];
  if (wave == 0) { for (int n = 0; n < 4; ++n) o[n] = acc[n][0]; }
  else if (wave == 1) { for (int n = 0; n < 4; ++n) o[n] = acc[n][1]; }
  else if (wave == 2) { for (int n = 0; n < 4; ++n) o[n] = acc[n][2]; }
  else { for (int n = 0; n < 4; ++n) o[n] = acc[n][3]; }
#pragma unroll
  for (int sw = 0; sw < 4; ++sw) {
    if (sw != wave) {
      const int slot = wave > sw ? wave - 1 : wave;
#pragma unroll
      for (int n = 0; n < 4; ++n) o[n] += *(LAS const f32x4*)(smem + ((sw * 3 + slot) * 4 + n) * 1024 + lane * 16);
    }
  }
  __syncthreads();
  {
    const int t = 16 * wave + (lane & 15), q = lane >> 4;
#pragma unroll
    for (int n = 0; n < 4; ++n) {
      f32x4 g;
#pragma unroll
      for (int j = 0; j < 4; ++j) {
        const float v = rbf(o[n][j]);
        g[j] = rbf(v * __builtin_amdgcn_rcpf(1.0f + __builtin_amdgcn_exp2f(-1.4426950408889634f * v)));
      }
      *(LAS f32x4*)(smem + t * 272 + (16 * n + 4 * q) * 4) = g;
    }
  }
  __syncthreads();
  {
    unsigned ow[8];
#pragma unroll
    for (int k = 0; k < 4; ++k) {
      const f32x4 g = *(LAS const f32x4*)(smem + erow * 272 + (16 * ec + 4 * k) * 4);
      float r[4];
#pragma unroll
      for (int j = 0; j < 4; ++j) {
        const int dl = 16 * ec + 4 * k + j;
        const float sv = bf2f(*(LAS const unsigned short*)(sl + dl * 132 + erow * 2));
        const unsigned xw = (k < 2 ? x0 : x1)[((4 * k + j) >> 1) & 3];
        const float xv = (j & 1) ? bfhi(xw) : bflo(xw);
        r[j] = xv + rbf(sv * g[j]);
      }
      ow[2 * k] = pk_bf16(r[0], r[1]); ow[2 * k + 1] = pk_bf16(r[2], r[3]);
    }
    u32x4* op = (u32x4*)(out + (size_t)(64 * T + erow) * DM + 64 * Dt + 16 * ec);
    op[0] = (u32x4){ow[0], ow[1], ow[2], ow[3]};
    op[1] = (u32x4){ow[4], ow[5], ow[6], ow[7]};
  }
  STAMP(2, 2);
}

extern "C" void kernel_launch(void* const* d_in, const int* in_sizes, int n_in, void* d_out, int out_size, void* d_ws, size_t ws_size, hipStream_t stream) {
  const unsigned short* x = (const unsigned short*)d_in[0];
  const unsigned short* logA = (const unsigned short*)d_in[1];
  const unsigned short* Aim = (const unsigned short*)d_in[2];
  const unsigned short* Cre = (const unsigned short*)d_in[3];
  const unsigned short* Cim = (const unsigned short*)d_in[4];
  const unsigned short* logdt = (const unsigned short*)d_in[5];
  const unsigned short* Dsk = (const unsigned short*)d_in[6];
  const unsigned short* w_in = (const unsigned short*)d_in[8];
  const unsigned short* w_sp = (const unsigned short*)d_in[9];
  const unsigned short* b_sp = (const unsigned short*)d_in[10];
  const unsigned short* w_out = (const unsigned short*)d_in[11];
  char* ws = (char*)d_ws;
  (void)hipFuncSetAttribute((const void*)k1_gemm1, hipFuncAttributeMaxDynamicSharedMemorySize, K1_LDS);
  (void)hipFuncSetAttribute((const void*)k2_mix, hipFuncAttributeMaxDynamicSharedMemorySize, K2_LDS);
  (void)hipFuncSetAttribute((const void*)k3_out, hipFuncAttributeMaxDynamicSharedMemorySize, K3_LDS);
  k1_gemm1<<<256, 512, K1_LDS, stream>>>(x, w_in, ws);
  k2_mix<<<256, 512, K2_LDS, stream>>>(w_sp, b_sp, logA, Aim, Cre, Cim, logdt, Dsk, w_out, ws);
  k3_out<<<224, 256, K3_LDS, stream>>>(x, ws, (unsigned short*)d_out);
}
```

```cpp
#include <hip/hip_runtime.h>
#include <hip/hip_bf16.h>
#include <stdint.h>

#define LAS __attribute__((address_space(3)))
#define GAS __attribute__((address_space(1)))
typedef float f32x2 __attribute__((ext_vector_type(2)));
typedef float f32x4 __attribute__((ext_vector_type(4)));
typedef unsigned u32x2 __attribute__((ext_vector_type(2)));
typedef unsigned u32x4 __attribute__((ext_vector_type(4)));
typedef short s16x4 __attribute__((ext_vector_type(4)));
typedef short s16x8 __attribute__((ext_vector_type(8)));
typedef __bf16 bf16x2 __attribute__((ext_vector_type(2)));
typedef __bf16 bf16x8 __attribute__((ext_vector_type(8)));
typedef _Float16 f16x2 __attribute__((ext_vector_type(2)));
typedef _Float16 f16x8 __attribute__((ext_vector_type(8)));

constexpr int SEQ = 2048, DM = 448, FH = 896, FF = 1792, NST = 256;

constexpr size_t WS_Z1D = 0;
constexpr size_t WS_Z2F = WS_Z1D + (size_t)SEQ * FH * 2;
constexpr size_t WS_HF  = WS_Z2F + (size_t)SEQ * FH * 2;
constexpr size_t WS_XT  = WS_HF + (size_t)SEQ * FH * 2;
constexpr size_t WS_ST  = WS_XT + (size_t)SEQ * DM * 2;
constexpr size_t WS_WOF = WS_ST + (size_t)SEQ * DM * 2;
constexpr size_t WS_XCH = WS_WOF + (size_t)FH * DM * 2;
constexpr size_t WS_EP  = WS_XCH + 32 * 64 * 8 * 16;
constexpr size_t WS_XS1 = WS_EP + 32 * 128;
constexpr size_t WS_END = WS_XS1 + 32 * 1024;

constexpr size_t WS_DBG = 32u << 20;
#define STAMP(kern, slot) do {} while (0)
#define MARK_DECL do {} while (0)
#define MARK(i) do {} while (0)
#define MARK_DUMP(kern) do {} while (0)
__device__ __forceinline__ float bf2f(unsigned short h) { return __uint_as_float((unsigned)h << 16); }
__device__ __forceinline__ float bflo(unsigned w) { return __uint_as_float(w << 16); }
__device__ __forceinline__ float bfhi(unsigned w) { return __uint_as_float(w & 0xffff0000u); }
__device__ __forceinline__ unsigned pk_bf16(float lo, float hi) {
  bf16x2 v = {(__bf16)lo, (__bf16)hi};
  return __builtin_bit_cast(unsigned, v);
}
__device__ __forceinline__ float rbf(float x) { return (float)(__bf16)x; }
__device__ __forceinline__ unsigned pk_f16(float lo, float hi) {
  f16x2 v = {(_Float16)lo, (_Float16)hi};
  return __builtin_bit_cast(unsigned, v);
}
__device__ __forceinline__ float gelu_tanh(float x) {
  const float c1 = -2.0f * 0.7978845608028654f * 1.4426950408889634f, c2 = c1 * 0.044715f;
  float t = x * x;
  float a = x * __builtin_fmaf(t, c2, c1);
  float e = __builtin_amdgcn_exp2f(a);
  return x * __builtin_amdgcn_rcpf(1.0f + e);
}
template <int CTRL> __device__ __forceinline__ float dpp_add(float v) {
  int t = __builtin_amdgcn_update_dpp(0, __float_as_int(v), CTRL, 0xf, 0xf, true);
  return v + __int_as_float(t);
}
__device__ __forceinline__ float red16(float v) {
  v = dpp_add<0xB1>(v);
  v = dpp_add<0x4E>(v);
  v = dpp_add<0x141>(v);
  v = dpp_add<0x140>(v);
  return v;
}
__device__ __forceinline__ float red8(float v) {
  v = dpp_add<0xB1>(v); v = dpp_add<0x4E>(v); v = dpp_add<0x141>(v);
  return v;
}
__device__ __forceinline__ void glds16(const void* sbase, unsigned voff, unsigned lds) {
  asm volatile("s_mov_b32 m0, %2\n\ts_nop 0\n\tglobal_load_lds_dwordx4 %0, %1" ::"v"(voff), "s"(sbase), "s"(lds) : "memory");
}
#define WAIT_VM(n) asm volatile("s_waitcnt vmcnt(%0)" ::"n"(n) : "memory")
#define WAIT_LGKM0() asm volatile("s_waitcnt lgkmcnt(0)" ::: "memory")
#define BAR() do { asm volatile("s_waitcnt lgkmcnt(0)" ::: "memory"); __builtin_amdgcn_s_barrier(); asm volatile("" ::: "memory"); } while (0)
__device__ __forceinline__ s16x4 tr_read(LAS const char* p) {
  typedef short v4i16 __attribute__((ext_vector_type(4)));
  return __builtin_amdgcn_ds_read_tr16_b64_v4i16((LAS v4i16*)p);
}
__device__ __forceinline__ f32x4 mfma_bf(s16x8 a, s16x8 b, f32x4 c) {
  return __builtin_amdgcn_mfma_f32_16x16x32_bf16(__builtin_bit_cast(bf16x8, a), __builtin_bit_cast(bf16x8, b), c, 0, 0, 0);
}
__device__ __forceinline__ float dot2bf(unsigned a, unsigned b, float c) {
  return __builtin_amdgcn_fdot2_f32_bf16(__builtin_bit_cast(bf16x2, a), __builtin_bit_cast(bf16x2, b), c, false);
}

constexpr int K1_X = 0;
constexpr int K1_W = 57344;
constexpr int K1_SLOT = 16384;
constexpr int K1_S = K1_W + 6 * K1_SLOT;
constexpr int K1_P = K1_S;
constexpr int K1_M2 = K1_S + 2048;
constexpr int K1_R2 = K1_M2 + 256;
constexpr int K1_W1 = K1_R2 + 256;
constexpr int K1_LDS = 163840;
constexpr unsigned ONES_BF = 0x3f803f80u;
typedef float f32x16 __attribute__((ext_vector_type(16)));

__device__ __forceinline__ float xsum32(float v) {
  auto b = __builtin_amdgcn_permlane32_swap(__float_as_uint(v), __float_as_uint(v), false, false);
  return __uint_as_float(b[0]) + __uint_as_float(b[1]);
}
__device__ __forceinline__ float xsum16_32(float v) {
  auto a = __builtin_amdgcn_permlane16_swap(__float_as_uint(v), __float_as_uint(v), false, false);
  return xsum32(__uint_as_float(a[0]) + __uint_as_float(a[1]));
}
__device__ __forceinline__ f32x2 up_bf(unsigned w) { return (f32x2){bflo(w), bfhi(w)}; }
__device__ __forceinline__ unsigned rn_pair(f32x2 v) {
  unsigned r;
  asm("v_cvt_pk_bf16_f32 %0, %1, %2" : "=v"(r) : "v"(v.x), "v"(v.y));
  return r;
}
__device__ __forceinline__ f32x2 gelu2(f32x2 x) {
  const float c1 = -2.0f * 0.7978845608028654f * 1.4426950408889634f, c2 = c1 * 0.044715f;
  const f32x2 t = x * x;
  const f32x2 a = x * (t * c2 + c1);
  f32x2 d = {__builtin_amdgcn_exp2f(a.x), __builtin_amdgcn_exp2f(a.y)};
  d = d + 1.0f;
  return x * (f32x2){__builtin_amdgcn_rcpf(d.x), __builtin_amdgcn_rcpf(d.y)};
}
__device__ __forceinline__ void ln_scalars(float s, float q, float invn, float& mb, float& rr) {
  const float mean = s * invn;
  float var = q * invn - mean * mean; var = var < 0.f ? 0.f : var;
  mb = rbf(mean);
  rr = __builtin_amdgcn_rcpf(rbf(__builtin_amdgcn_sqrtf(rbf(rbf(var) + 1e-5f))));
}
__device__ __forceinline__ f32x16 mfma32_bf(s16x8 a, s16x8 b, f32x16 c) {
  return __builtin_amdgcn_mfma_f32_32x32x16_bf16(__builtin_bit_cast(bf16x8, a), __builtin_bit_cast(bf16x8, b), c, 0, 0, 0);
}
__device__ __forceinline__ void poll_granule(u32x4& gr, const char* base, unsigned voff, unsigned tag) {
  for (unsigned spins = 0;; ++spins) {
    const bool ok = (gr[1] == tag) && (gr[3] == tag);
    if (__all(ok) || spins > (1u << 22)) break;
    __builtin_amdgcn_s_sleep(1);
    asm volatile("global_load_dwordx4 %0, %1, %2 sc1\n\ts_waitcnt vmcnt(0)" : "=&v"(gr) : "v"(voff), "s"(base) : "memory");
  }
}

template <int V> struct IC { static constexpr int value = V; };
template <int A, int B, class F> __device__ __forceinline__ void static_for(F&& f) {
  if constexpr (A < B) { f(IC<A>{}); static_for<A + 1, B>(f); }
}
__device__ __forceinline__ void k1_xt(LAS char* smem, const unsigned short* __restrict__ x, char* __restrict__ ws, int v, int tid) {
  const int row = tid >> 3, c8 = tid & 7;
  const unsigned tag = __hip_atomic_load((GAS unsigned*)(ws + WS_EP + v * 128), __ATOMIC_RELAXED, __HIP_MEMORY_SCOPE_AGENT) + 1u;
  u32x4 xv[7];
#pragma unroll
  for (int m = 0; m < 7; ++m) xv[m] = *(const u32x4*)(x + (size_t)(64 * v + row) * DM + (c8 + 8 * m) * 8);
  float s = 0.f, q = 0.f;
#pragma unroll
  for (int m = 0; m < 7; ++m)
#pragma unroll
    for (int e = 0; e < 4; ++e) { const unsigned w = xv[m][e]; s = dot2bf(w, ONES_BF, s); q = dot2bf(w, w, q); }
  s = red8(s); q = red8(q);
  float mb, rr;
  ln_scalars(s, q, 1.0f / 448.0f, mb, rr);
  if (c8 == 0) {
    u32x4 gr = {__float_as_uint(mb), tag, __float_as_uint(rr), tag};
    const char* gp = ws + WS_XS1 + (size_t)v * 1024;
    const unsigned vo = row * 16;
    asm volatile("global_store_dwordx4 %0, %1, %2 sc1\n\ts_nop 1" ::"v"(vo), "v"(gr), "s"(gp) : "memory");
  }
#pragma unroll
  for (int m = 0; m < 7; ++m)
#pragma unroll
    for (int e = 0; e < 4; ++e) {
      const unsigned w = xv[m][e];
      const f32x2 n3 = up_bf(rn_pair(up_bf(rn_pair(up_bf(w) - mb)) * rr));
      const int d = (c8 + 8 * m) * 8 + 2 * e;
      *(LAS _Float16*)(smem + d * 144 + row * 2) = (_Float16)n3.x;
      *(LAS _Float16*)(smem + (d + 1) * 144 + row * 2) = (_Float16)n3.y;
    }
  __syncthreads();
#pragma unroll
  for (int m = 0; m < 7; ++m) {
    const int d = row + 64 * m;
    *(u32x4*)(ws + WS_XT + ((size_t)d * SEQ + 64 * v + 8 * c8) * 2) = *(LAS const u32x4*)(smem + d * 144 + c8 * 16);
  }
}

__global__ __launch_bounds__(512, 2) void k1_gemm1(const unsigned short* __restrict__ x, const unsigned short* __restrict__ w_in, char* __restrict__ ws) {
  extern __shared__ __attribute__((aligned(16))) char smem_raw[];
  LAS char* smem = (LAS char*)smem_raw;
  const int tid = threadIdx.x, lane = tid & 63;
  const int wave = __builtin_amdgcn_readfirstlane(tid >> 6);
  const int b = blockIdx.x;
  if (b >= 224) { k1_xt(smem, x, ws, b - 224, tid); return; }
  const int xcd = b & 7, rr = b >> 3;
  const int ti = 4 * xcd + rr / 7, tj = rr % 7, rh = wave >> 2, cg = wave & 3;
  const int h = lane >> 5, l31 = lane & 31;

  STAMP(0, 0);
  unsigned ep;
  {
    const char* epp = ws + WS_EP + ti * 128;
    unsigned zero = 0;
    asm volatile("global_load_dword %0, %1, %2 sc1" : "=v"(ep) : "v"(zero), "s"(epp) : "memory");
  }
  const int xrow = 8 * wave + (lane >> 3);
  const unsigned voffx = (unsigned)(xrow * 896 + (((lane & 7) ^ ((xrow >> 1) & 7)) * 16));
  const char* xsrc = (const char*)x + (size_t)ti * 64 * 896;
  unsigned voffw[2];
#pragma unroll
  for (int e = 0; e < 2; ++e) voffw[e] = (unsigned)((8 * wave + 4 * e + (lane >> 4)) * 3584 + (((lane & 15) ^ (4 * (lane >> 4))) * 16) + tj * 256);
#define K1_XPIECE(g) glds16(xsrc + (g) * 128, voffx, (unsigned)(K1_X + (g) * 8192 + wave * 1024))
#define K1_WPIECE(G, e) \
  glds16(w_in, voffw[e] + (unsigned)(((G) % 7) * 64 * 3584 + ((G) < 7 ? 1792 : 0)), (unsigned)(K1_W + ((G) % 6) * K1_SLOT + wave * 2048 + (e) * 1024))
#pragma unroll
  for (int g = 0; g < 3; ++g) { K1_XPIECE(g); K1_WPIECE(g, 0); K1_WPIECE(g, 1); }
  LAS const char* ab[2];
  LAS const char* xb[4];
  {
    const int g4 = lane >> 4, qq = (lane & 15) >> 2, p4 = lane & 3;
    const int lb = (8 * (g4 >> 1) + qq) * 256 + (((4 * cg + 2 * (g4 & 1) + (p4 >> 1)) ^ (4 * qq)) * 16) + (p4 & 1) * 8;
    ab[0] = smem + K1_W + lb + rh * 4096;
    ab[1] = smem + K1_W + lb + (rh ^ 1) * 4096;
    const int row = 32 * rh + l31, sw = (row >> 1) & 7;
#pragma unroll
    for (int i = 0; i < 4; ++i) xb[i] = smem + K1_X + row * 128 + (((2 * (i ^ rh) + h) ^ sw) * 16);
  }
  s16x8 xa[28], wb[8];
#define K1_FRAGS(st)                                                                                                   \
  do {                                                                                                                 \
    const int G_ = (st) >> 2, i_ = (st) & 3, so_ = (G_ % 6) * K1_SLOT + (i_ >> 1) * 8192;                              \
    if ((st) < 28) xa[(st) % 28] = *(LAS const s16x8*)(xb[i_] + G_ * 8192);                                            \
    wb[(st) & 7] = __builtin_shufflevector(tr_read(ab[i_ & 1] + so_), tr_read(ab[i_ & 1] + so_ + 1024), 0, 1, 2, 3, 4, 5, 6, 7); \
  } while (0)
  f32x16 acc2 = {}, acc1 = {};
  float w1a = 0.f, w1b = 0.f;
  u32x4 g1;
  const char* g1p = ws + WS_XS1 + (size_t)ti * 1024;
  const unsigned g1o = (unsigned)(32 * rh + l31) * 16;
  unsigned zp[8];
  float m1 = 0.f, r1 = 0.f;
  asm volatile("s_waitcnt vmcnt(3)" : "+v"(ep)::"memory");
  const unsigned tag = ep + 1u;
  BAR();
  STAMP(0, 1);
  K1_FRAGS(0); K1_FRAGS(1); K1_FRAGS(2); K1_FRAGS(3);
  auto step = [&](auto stc) __attribute__((always_inline)) {
    constexpr int st = decltype(stc)::value;
    constexpr int G = st >> 2, i = st & 3;
    if (i == 0 && G >= 1) {
      if (G == 1) WAIT_VM(6); else if (G == 2) WAIT_VM(9); else if (G == 3) WAIT_VM(8); else if (G == 4) WAIT_VM(7); else if (G <= 6) WAIT_VM(6);
      else if (G == 7) WAIT_VM(0);
      else if (G <= 9) WAIT_VM(6); else if (G == 10) WAIT_VM(4); else if (G == 11) WAIT_VM(2); else WAIT_VM(0);
      __builtin_amdgcn_s_barrier();
      asm volatile("" ::: "memory");
    }
    __builtin_amdgcn_sched_barrier(0);
    if constexpr (st < 28) acc2 = mfma32_bf(wb[st & 7], xa[st % 28], acc2); else acc1 = mfma32_bf(wb[st & 7], xa[st % 28], acc1);
    if constexpr (st + 4 < 56) K1_FRAGS(st + 4);
    if constexpr ((i & 1) == 0) {
      const u32x4 wv = __builtin_bit_cast(u32x4, wb[st & 7]);
#pragma unroll
      for (int e = 0; e < 4; ++e) { const unsigned ww = wv[e]; if (st < 28) w1a = dot2bf(ww, ONES_BF, w1a); else w1b = dot2bf(ww, ONES_BF, w1b); }
    }
    __builtin_amdgcn_sched_barrier(0);
    if constexpr ((i & 1) == 0) {
      constexpr int ks = st >> 1;
      if constexpr (ks < 4) { K1_XPIECE(ks + 3); K1_WPIECE(ks + 3, 0); K1_WPIECE(ks + 3, 1); }
      else if constexpr (ks < 18) K1_WPIECE(5 + (ks >> 1), ks & 1);
    }
    if constexpr (st == 20) asm volatile("global_load_dwordx4 %0, %1, %2 sc1" : "=v"(g1) : "v"(g1o), "s"(g1p) : "memory");
    __builtin_amdgcn_sched_barrier(0);
  };
  static_for<0, 28>(step);
    {
      STAMP(0, 3);
      const float w1s = xsum32(w1a);
      if (h == 0) *(LAS float*)(smem + K1_W1 + (rh * 128 + 32 * cg + l31) * 4) = w1s;
      asm volatile("s_waitcnt vmcnt(0)" : "+v"(g1)::"memory");
      BAR();
      poll_granule(g1, g1p, g1o, tag);
      m1 = __uint_as_float(g1[0]); r1 = __uint_as_float(g1[2]);
      float ss = 0.f, sq = 0.f;
#pragma unroll
      for (int g = 0; g < 4; ++g) {
        const f32x4 w1 = *(LAS const f32x4*)(smem + K1_W1 + (32 * cg + 8 * g + 4 * h) * 4) + *(LAS const f32x4*)(smem + K1_W1 + (128 + 32 * cg + 8 * g + 4 * h) * 4);
#pragma unroll
        for (int p = 0; p < 2; ++p) {
          const f32x2 a2 = {acc2[4 * g + 2 * p], acc2[4 * g + 2 * p + 1]}, w2 = {w1[2 * p], w1[2 * p + 1]};
          const unsigned zz = rn_pair(gelu2(up_bf(rn_pair((a2 - w2 * m1) * r1))));
          zp[2 * g + p] = zz;
          ss = dot2bf(zz, ONES_BF, ss); sq = dot2bf(zz, zz, sq);
        }
      }
      ss = xsum32(ss); sq = xsum32(sq);
      if (h == 0) *(LAS f32x2*)(smem + K1_P + ((32 * rh + l31) * 4 + cg) * 8) = (f32x2){ss, sq};
      BAR();
      if (wave == 0) {
        const f32x4 p0 = *(LAS const f32x4*)(smem + K1_P + lane * 32), p1 = *(LAS const f32x4*)(smem + K1_P + lane * 32 + 16);
        const float s = (p0[0] + p0[2]) + (p1[0] + p1[2]), q = (p0[1] + p0[3]) + (p1[1] + p1[3]);
        u32x4 gr = {__float_as_uint(s), tag, __float_as_uint(q), tag};
        const char* xp = ws + WS_XCH + (size_t)ti * 64 * 128 + tj * 16;
        const unsigned vo = lane * 128;
        asm volatile("global_store_dwordx4 %0, %1, %2 sc1\n\ts_nop 1" ::"v"(vo), "v"(gr), "s"(xp) : "memory");
      }
      STAMP(0, 4);
    }
  static_for<28, 56>(step);
#undef K1_FRAGS
#undef K1_XPIECE
#undef K1_WPIECE
  STAMP(0, 5);
  {
    const float w1s = xsum32(w1b);
    if (h == 0) *(LAS float*)(smem + K1_W1 + 1024 + (rh * 128 + 32 * cg + l31) * 4) = w1s;
  }
  BAR();
  u32x4 gr;
  const int row = tid >> 3, jj = tid & 7;
  const char* xp = ws + WS_XCH + (size_t)ti * 64 * 128;
  const unsigned vo = row * 128 + (jj < 7 ? jj : 0) * 16;
  asm volatile("global_load_dwordx4 %0, %1, %2 sc1" : "=v"(gr) : "v"(vo), "s"(xp) : "memory");
  {
    const int tt = 4 * ti + 2 * rh + (l31 >> 4);
#pragma unroll
    for (int g = 0; g < 4; ++g) {
      const f32x4 w1 = *(LAS const f32x4*)(smem + K1_W1 + 1024 + (32 * cg + 8 * g + 4 * h) * 4) + *(LAS const f32x4*)(smem + K1_W1 + 1024 + (128 + 32 * cg + 8 * g + 4 * h) * 4);
      unsigned o[2];
#pragma unroll
      for (int p = 0; p < 2; ++p) {
        const f32x2 a2 = {acc1[4 * g + 2 * p], acc1[4 * g + 2 * p + 1]}, w2 = {w1[2 * p], w1[2 * p + 1]};
        o[p] = rn_pair(gelu2(up_bf(rn_pair((a2 - w2 * m1) * r1))));
      }
      const int ft = 8 * tj + 2 * cg + (g >> 1), lp = (lane & 15) + 16 * (2 * (g & 1) + h);
      *(u32x2*)(ws + WS_Z1D + ((size_t)(tt * 56 + ft) * 64 + lp) * 8) = (u32x2){o[0], o[1]};
    }
  }
  STAMP(0, 6);
  asm volatile("s_waitcnt vmcnt(4)" : "+v"(gr)::"memory");
  poll_granule(gr, xp, vo, tag);
  {
    const float s = red8(jj < 7 ? __uint_as_float(gr[0]) : 0.f), q = red8(jj < 7 ? __uint_as_float(gr[2]) : 0.f);
    float mb, rr2;
    ln_scalars(s, q, 1.0f / 896.0f, mb, rr2);
    if (jj == 0) { *(LAS float*)(smem + K1_M2 + row * 4) = mb; *(LAS float*)(smem + K1_R2 + row * 4) = rr2; }
  }
  BAR();
  STAMP(0, 7);
  if (tj == 0 && tid == 0) __hip_atomic_store((GAS unsigned*)(ws + WS_EP + ti * 128), tag, __ATOMIC_RELAXED, __HIP_MEMORY_SCOPE_AGENT);
  {
    LAS char* tb = smem + K1_W + wave * 2304;
    const int srow = 32 * rh + l31;
    const float mb2 = *(LAS const float*)(smem + K1_M2 + srow * 4), rr2 = *(LAS const float*)(smem + K1_R2 + srow * 4);
#pragma unroll
    for (int g = 0; g < 4; ++g) {
      unsigned o[2];
#pragma unroll
      for (int p = 0; p < 2; ++p) o[p] = rn_pair(up_bf(rn_pair(up_bf(zp[2 * g + p]) - mb2)) * rr2);
      *(LAS u32x2*)(tb + l31 * 72 + (8 * g + 4 * h) * 2) = (u32x2){o[0], o[1]};
    }
    asm volatile("s_waitcnt lgkmcnt(0)" ::: "memory");
    const int q4 = lane >> 4, l15 = lane & 15;
#pragma unroll
    for (int n = 0; n < 2; ++n) {
      LAS const char* rp = tb + (8 * q4 + (l15 >> 2)) * 72 + n * 32 + (lane & 3) * 8;
      const s16x4 lo = tr_read(rp), hi = tr_read(rp + 4 * 72);
      const s16x8 o = __builtin_shufflevector(lo, hi, 0, 1, 2, 3, 4, 5, 6, 7);
      const int ft = 8 * tj + 2 * cg + n, sb = 2 * ti + rh;
      *(s16x8*)(ws + WS_Z2F + ((size_t)(ft * 64 + sb) * 64 + lane) * 16) = o;
    }
  }
  STAMP(0, 8);
}

constexpr int K2_RING = 0;
constexpr int K2_CH = 65536;
constexpr int K2_CHS = 49152;
constexpr int CH_FT = 0;
constexpr int CH_FW = 16416, CH_FS = 288;
constexpr int CH_CS = 4160;
constexpr int CH_U = 33280;
constexpr int CH_X = CH_U + 4608;
constexpr int CH_T = CH_X + 4224;
constexpr int CH_CNT = CH_T + 6144;
constexpr int K2_LDS = 163840;

__device__ __forceinline__ void lds_sync(LAS char* cntp, unsigned target, int lane) {
  LAS unsigned* cnt = (LAS unsigned*)cntp;
  asm volatile("s_waitcnt lgkmcnt(0)" ::: "memory");
  if (lane == 0) __hip_atomic_fetch_add(cnt, 1u, __ATOMIC_RELAXED, __HIP_MEMORY_SCOPE_WORKGROUP);
  while ((unsigned)__builtin_amdgcn_readfirstlane(*(volatile LAS unsigned*)cnt) < target) __builtin_amdgcn_s_sleep(1);
  asm volatile("" ::: "memory");
}
__device__ __forceinline__ __amdgpu_buffer_rsrc_t mk_rsrc(const void* p, unsigned bytes) {
  return __builtin_amdgcn_make_buffer_rsrc((void*)p, (short)0, (int)bytes, 0x00020000);
}
__device__ __forceinline__ void bglds16(__amdgpu_buffer_rsrc_t rs, unsigned voff, unsigned soff, unsigned lds) {
  asm volatile("s_mov_b32 m0, %3\n\ts_nop 0\n\tbuffer_load_dwordx4 %0, %1, %2 offen lds" ::"v"(voff), "s"(rs), "s"(soff), "s"(lds) : "memory");
}
__device__ __forceinline__ void bload16(u32x4& dst, __amdgpu_buffer_rsrc_t rs, unsigned voff, unsigned soff) {
  asm volatile("buffer_load_dwordx4 %0, %1, %2, %3 offen" : "=v"(dst) : "v"(voff), "s"(rs), "s"(soff) : "memory");
}

template <int W>
__device__ __forceinline__ void k2_epi(LAS char* smem, char* __restrict__ ws, f32x4 (&acc)[7][4], const u32x2 (&z1v)[7], float bs, int lane, int tt, int J) {
  LAS char* cnt = smem + K2_CH + CH_CNT + 64;
  LAS char* mine = smem + K2_RING + W * 16384 + lane * 16;
  LAS char* other = smem + K2_RING + (W ^ 2) * 16384 + lane * 16;
  LAS char* o2 = smem + K2_RING + (W ^ 3) * 16384 + lane * 16;
  constexpr int K0 = W & 2, S0 = K0 ^ 2;
#pragma unroll
  for (int n = 0; n < 7; ++n) { *(LAS f32x4*)(mine + n * 1024) = acc[n][S0]; *(LAS f32x4*)(mine + (7 + n) * 1024) = acc[n][S0 + 1]; }
  lds_sync(cnt, 4, lane);
#pragma unroll
  for (int n = 0; n < 7; ++n) { acc[n][K0] += *(LAS const f32x4*)(other + n * 1024); acc[n][K0 + 1] += *(LAS const f32x4*)(other + (7 + n) * 1024); }
  asm volatile("s_waitcnt lgkmcnt(0)" ::: "memory");
#pragma unroll
  for (int n = 0; n < 7; ++n) *(LAS f32x4*)(other + n * 1024) = acc[n][W ^ 1];
  lds_sync(cnt, 8, lane);
  STAMP(1, 2);
  unsigned hp[7][2];
#pragma unroll
  for (int n = 0; n < 7; ++n) {
    const f32x4 v = acc[n][W] + *(LAS const f32x4*)(o2 + n * 1024);
#pragma unroll
    for (int p = 0; p < 2; ++p) {
      const f32x2 g0 = up_bf(pk_bf16(v[2 * p], v[2 * p + 1])) + bs;
      const unsigned zw = z1v[n][p];
      const f32x2 h = up_bf(pk_bf16(g0.x, g0.y)) * up_bf(zw);
      hp[n][p] = pk_bf16(h.x, h.y);
    }
  }
  char* hb = ws + WS_HF + ((size_t)(tt * 28 + (7 * J >> 1)) * 64 + lane) * 16;
  if (!(J & 1)) {
#pragma unroll
    for (int p = 0; p < 3; ++p) *(u32x4*)(hb + p * 1024) = (u32x4){hp[2 * p][0], hp[2 * p][1], hp[2 * p + 1][0], hp[2 * p + 1][1]};
    *(u32x2*)(hb + 3 * 1024) = (u32x2){hp[6][0], hp[6][1]};
  } else {
    *(u32x2*)(hb + 8) = (u32x2){hp[0][0], hp[0][1]};
#pragma unroll
    for (int p = 0; p < 3; ++p) *(u32x4*)(hb + (p + 1) * 1024) = (u32x4){hp[2 * p + 1][0], hp[2 * p + 1][1], hp[2 * p + 2][0], hp[2 * p + 2][1]};
  }
}

__device__ __forceinline__ void k2_gemm(LAS char* smem, const unsigned short* __restrict__ w_sp, const unsigned short* __restrict__ b_sp, char* __restrict__ ws,
                                        int wave, int lane, int T, int J, int rot) {
  const __amdgpu_buffer_rsrc_t zs = mk_rsrc(ws + WS_Z2F + (size_t)(7 * J * 64 + 16 * wave) * 1024, 7 * 65536 - 16 * wave * 1024);
  const __amdgpu_buffer_rsrc_t wr = mk_rsrc((const char*)w_sp + (size_t)T * 64 * 4096 + wave * 1024, 64 * 4096 - wave * 1024);
  const unsigned voffz = lane * 16;
  const unsigned voffw0 = (lane >> 3) * 4096 + (((lane & 7) ^ ((lane >> 4) & 3)) * 16);
  const unsigned voffw1 = (8 + (lane >> 3)) * 4096 + (((lane & 7) ^ (4 + ((lane >> 4) & 3))) * 16);
  const unsigned ring = K2_RING + wave * 16384;
  LAS const char* wrd0 = smem + ring + (lane & 15) * 128 + (((lane >> 4) ^ ((lane >> 1) & 7)) * 16);
  LAS const char* wrd1 = smem + ring + (lane & 15) * 128 + (((4 + (lane >> 4)) ^ ((lane >> 1) & 7)) * 16);
  u32x4 z[3][7];
  f32x4 acc[7][4];
#pragma unroll
  for (int n = 0; n < 7; ++n)
#pragma unroll
    for (int m = 0; m < 4; ++m) acc[n][m] = (f32x4){0.f, 0.f, 0.f, 0.f};
#define K2_ISSUE_W(pp)                                                                                      \
  do {                                                                                                      \
    const unsigned kw_ = (unsigned)((2 * (pp) + rot) & 15) * 64;                                            \
    _Pragma("unroll") for (int m = 0; m < 4; ++m) {                                                         \
      bglds16(wr, voffw0, m * 65536 + kw_, ring + ((pp) & 1) * 8192 + m * 2048);                            \
      bglds16(wr, voffw1, m * 65536 + kw_, ring + ((pp) & 1) * 8192 + m * 2048 + 1024);                     \
    }                                                                                                       \
  } while (0)
#define K2_ISSUE_Z(it)                                                                                      \
  do {                                                                                                      \
    const unsigned kz_ = (unsigned)(((it) + rot) & 15) * 1024;                                              \
    _Pragma("unroll") for (int n = 0; n < 7; ++n) bload16(z[(it) % 3][n], zs, voffz, n * 65536 + kz_);      \
  } while (0)
  STAMP(1, 0);
  K2_ISSUE_W(0); K2_ISSUE_Z(0); K2_ISSUE_Z(1); K2_ISSUE_W(1); K2_ISSUE_Z(2);
  const int tt = 4 * T + wave;
  u32x2 z1v[7];
  unsigned bsw;
#define K2_WAITZ(N) asm volatile("s_waitcnt vmcnt(" #N ")" : "+v"(zc[0]), "+v"(zc[1]), "+v"(zc[2]), "+v"(zc[3]), "+v"(zc[4]), "+v"(zc[5]), "+v"(zc[6])::"memory")
#pragma unroll
  for (int it = 0; it < 16; ++it) {
    u32x4(&zc)[7] = z[it % 3];
    if (it <= 13) K2_WAITZ(22); else if (it == 14) K2_WAITZ(15); else K2_WAITZ(8);
    s16x8 wf[4];
#pragma unroll
    for (int m = 0; m < 4; ++m) wf[m] = *(LAS const s16x8*)(((it & 1) ? wrd1 : wrd0) + ((it >> 1) & 1) * 8192 + m * 2048);
    const unsigned kz = (unsigned)((it + 3 + rot) & 15) * 1024, kw = (unsigned)((it + 3 + rot) & 15) * 64;
#pragma unroll
    for (int n = 0; n < 7; ++n) {
#pragma unroll
      for (int m = 0; m < 4; ++m) acc[n][m] = mfma_bf(__builtin_bit_cast(s16x8, zc[n]), wf[m], acc[n][m]);
      __builtin_amdgcn_sched_barrier(0);
      if (it + 3 < 16) {
        bload16(zc[n], zs, voffz, n * 65536 + kz);
        if ((it & 1) && n >= 1 && n <= 4) {
          const int m = n - 1;
          bglds16(wr, voffw0, m * 65536 + kw, ring + (((it + 3) >> 1) & 1) * 8192 + m * 2048);
          bglds16(wr, voffw1, m * 65536 + kw, ring + (((it + 3) >> 1) & 1) * 8192 + m * 2048 + 1024);
        }
      }
      if (it == 13) {
        const char* zb = ws + WS_Z1D + (size_t)(tt * 56 + 7 * J + n) * 512;
        const unsigned vo = lane * 8;
        asm volatile("global_load_dwordx2 %0, %1, %2" : "=v"(z1v[n]) : "v"(vo), "s"(zb) : "memory");
        if (n == 6) {
          const unsigned vb = (unsigned)(16 * tt + (lane & 15)) * 2u;
          asm volatile("global_load_ushort %0, %1, %2" : "=v"(bsw) : "v"(vb), "s"(b_sp) : "memory");
        }
      }
      __builtin_amdgcn_sched_barrier(0);
    }
  }
#undef K2_WAITZ
#undef K2_ISSUE_W
#undef K2_ISSUE_Z
  STAMP(1, 1);
  asm volatile("s_waitcnt vmcnt(0)" : "+v"(z1v[0]), "+v"(z1v[1]), "+v"(z1v[2]), "+v"(z1v[3]), "+v"(z1v[4]), "+v"(z1v[5]), "+v"(z1v[6]), "+v"(bsw)::"memory");
  const float bs = bf2f((unsigned short)bsw);
  if (wave == 0) k2_epi<0>(smem, ws, acc, z1v, bs, lane, tt, J);
  else if (wave == 1) k2_epi<1>(smem, ws, acc, z1v, bs, lane, tt, J);
  else if (wave == 2) k2_epi<2>(smem, ws, acc, z1v, bs, lane, tt, J);
  else k2_epi<3>(smem, ws, acc, z1v, bs, lane, tt, J);
  STAMP(1, 3);
}

__device__ __forceinline__ f32x2 cmul(f32x2 a, f32x2 b) {
  f32x2 t, r;
  asm("v_pk_mul_f32 %0, %1, %2 op_sel:[0,0] op_sel_hi:[0,1]" : "=v"(t) : "v"(a), "v"(b));
  asm("v_pk_fma_f32 %0, %1, %2, %3 op_sel:[1,1,0] op_sel_hi:[1,0,1] neg_lo:[1,0,0]" : "=v"(r) : "v"(a), "v"(b), "v"(t));
  return r;
}
__device__ __forceinline__ unsigned hmul(unsigned a, unsigned b) {
  unsigned t, r;
  asm("v_pk_mul_f16 %0, %1, %2 op_sel:[0,0] op_sel_hi:[0,1]" : "=v"(t) : "v"(a), "v"(b));
  asm("v_pk_fma_f16 %0, %1, %2, %3 op_sel:[1,1,0] op_sel_hi:[1,0,1] neg_lo:[1,0,0]" : "=v"(r) : "v"(a), "v"(b), "v"(t));
  return r;
}
__device__ __forceinline__ unsigned hcv(f32x2 v) { return pk_f16(v.x, v.y); }
__device__ __forceinline__ f32x2 cexp_p(float al, float be, float p) {
  const float e = __builtin_amdgcn_exp2f(al * p), th = __builtin_amdgcn_fractf(be * p);
  return (f32x2){__builtin_amdgcn_cosf(th), __builtin_amdgcn_sinf(th)} * e;
}
__device__ __forceinline__ f32x4 mfma_h(u32x4 a, u32x4 b, f32x4 c) {
  return __builtin_amdgcn_mfma_f32_16x16x32_f16(__builtin_bit_cast(f16x8, a), __builtin_bit_cast(f16x8, b), c, 0, 0, 0);
}
struct PosTab { unsigned short v[2048]; };
constexpr int pos_bf16(int j) {
  if (j < 256) return j;
  int sh = 1; while ((j >> sh) >= 256) ++sh;
  const int q = j >> sh, r = j & ((1 << sh) - 1), h = 1 << (sh - 1);
  return (r > h || (r == h && (q & 1)) ? q + 1 : q) << sh;
}
constexpr PosTab make_postab() {
  PosTab t = {};
  for (int x = 0; x < 2048; ++x) {
    const int p = pos_bf16(2047 - x);
    int col = 0, a = 0;
    if (p < 256) { a = p >> 4; col = p & 15; }
    else if (p < 512) { const int r = p - 256; a = r >> 4; col = 16 + ((r & 15) >> 1); }
    else if (p < 1024) { const int r = p - 512; a = (r & 255) >> 4; col = 24 + 4 * (r >> 8) + ((r & 15) >> 2); }
    else if (p < 2048) { const int r = p - 1024; a = (r & 255) >> 4; col = 32 + 2 * (r >> 8) + ((r & 15) >> 3); }
    else { a = 0; col = 40; }
    t.v[x] = (unsigned short)((col * 16 + a) * 4);
  }
  return t;
}
__device__ const PosTab POSTAB = make_postab();

template <int HW>
__device__ __forceinline__ void s4d_conv(LAS char* cb, char* __restrict__ ws, int d, int lane, float iscale, float dsk) {
  constexpr int NF = HW ? 7 : 8;
  f32x4 acc[8] = {};
  const int r = lane & 15, h = lane >> 4;
  LAS const char* ab = cb + CH_FT + ((7 - r) & 7) * CH_CS + (255 - (r >> 3) - 2 * (h >> 1) + (h & 1)) * 16 - (64 * 7 + 512 * 7);
  LAS const char* bb = cb + CH_U + 512 + 32 * r - 32 * (h >> 1) + 16 * (h & 1) - 64 * 7;
  u32x4 A[2][8], B[2][8];
#define CONV_LOAD(ar, buf)                                                                                   \
  _Pragma("unroll") for (int k = 0; k < NF; ++k) {                                                           \
    B[buf][k] = *(LAS const u32x4*)(bb + 512 * k + 64 * (7 - (ar)));                                         \
    A[buf][k] = *(LAS const u32x4*)(ab + 512 * (7 - k) + 64 * (7 - (ar)));                                   \
  }
  CONV_LOAD(0, 0);
#pragma unroll
  for (int ar = 0; ar < 8; ++ar) {
    __builtin_amdgcn_sched_barrier(0);
    if (ar < 7) { CONV_LOAD(ar + 1, (ar + 1) & 1); }
#pragma unroll
    for (int al = 0; al < NF; ++al)
#pragma unroll
      for (int be = 0; be < NF; ++be) {
        const int tau = al + be;
        if (HW ? (tau >= 4 && tau <= 6) : (tau <= 3 || tau == 7)) acc[tau] = mfma_h(A[ar & 1][al], B[ar & 1][be], acc[tau]);
      }
#pragma unroll
    for (int i = 0; i < 16; ++i) { __builtin_amdgcn_sched_group_barrier(0x008, 1, 0); __builtin_amdgcn_sched_group_barrier(0x100, 1, 0); }
    __builtin_amdgcn_sched_barrier(0);
  }
#undef CONV_LOAD
  STAMP(1, 4);
  const int q = lane >> 4;
#pragma unroll
  for (int t = 0; t < 8; ++t) {
    if (HW ? (t >= 4 && t <= 6) : (t <= 3 || t == 7)) {
      const f32x4 v = acc[t];
      const int l0 = 256 * t + 16 * r + 4 * q;
      const u32x2 uw = *(LAS const u32x2*)(cb + CH_U + 512 + l0 * 2);
      const unsigned uw0 = uw[0], uw1 = uw[1];
      const f16x2 ua = __builtin_bit_cast(f16x2, uw0), ub = __builtin_bit_cast(f16x2, uw1);
      const float uu[4] = {(float)ua[0], (float)ua[1], (float)ub[0], (float)ub[1]};
      float sv[4];
#pragma unroll
      for (int j = 0; j < 4; ++j) sv[j] = rbf(v[j] * iscale) + rbf(uu[j] * dsk);
      *(u32x2*)(ws + WS_ST + ((size_t)d * SEQ + l0) * 2) = (u32x2){pk_bf16(sv[0], sv[1]), pk_bf16(sv[2], sv[3])};
    }
  }
}

__device__ __forceinline__ void k2_s4d(LAS char* cb, const unsigned short* __restrict__ logA, const unsigned short* __restrict__ Aim, const unsigned short* __restrict__ Cre,
                                       const unsigned short* __restrict__ Cim, const unsigned short* __restrict__ logdt, const unsigned short* __restrict__ Dsk,
                                       char* __restrict__ ws, int d, int hw, int lane) {
  LAS char* cnt = cb + CH_CNT;
  unsigned la_[2], ai_[2], cr_[4], ci_[4], ldt_, dsk_;
  u32x4 u0, u1, pt0, pt1;
  {
    const unsigned vo = (unsigned)(d * NST + 128 * hw + lane) * 2u, vo2 = (unsigned)(d * NST + ((128 * hw + 128 + lane) & 255)) * 2u, vd = (unsigned)d * 2u;
    asm volatile("global_load_ushort %0, %1, %2" : "=v"(la_[0]) : "v"(vo), "s"(logA) : "memory");
    asm volatile("global_load_ushort %0, %1, %2 offset:128" : "=v"(la_[1]) : "v"(vo), "s"(logA) : "memory");
    asm volatile("global_load_ushort %0, %1, %2" : "=v"(ai_[0]) : "v"(vo), "s"(Aim) : "memory");
    asm volatile("global_load_ushort %0, %1, %2 offset:128" : "=v"(ai_[1]) : "v"(vo), "s"(Aim) : "memory");
    asm volatile("global_load_ushort %0, %1, %2" : "=v"(cr_[0]) : "v"(vo), "s"(Cre) : "memory");
    asm volatile("global_load_ushort %0, %1, %2 offset:128" : "=v"(cr_[1]) : "v"(vo), "s"(Cre) : "memory");
    asm volatile("global_load_ushort %0, %1, %2" : "=v"(cr_[2]) : "v"(vo2), "s"(Cre) : "memory");
    asm volatile("global_load_ushort %0, %1, %2 offset:128" : "=v"(cr_[3]) : "v"(vo2), "s"(Cre) : "memory");
    asm volatile("global_load_ushort %0, %1, %2" : "=v"(ci_[0]) : "v"(vo), "s"(Cim) : "memory");
    asm volatile("global_load_ushort %0, %1, %2 offset:128" : "=v"(ci_[1]) : "v"(vo), "s"(Cim) : "memory");
    asm volatile("global_load_ushort %0, %1, %2" : "=v"(ci_[2]) : "v"(vo2), "s"(Cim) : "memory");
    asm volatile("global_load_ushort %0, %1, %2 offset:128" : "=v"(ci_[3]) : "v"(vo2), "s"(Cim) : "memory");
    asm volatile("global_load_ushort %0, %1, %2" : "=v"(ldt_) : "v"(vd), "s"(logdt) : "memory");
    asm volatile("global_load_ushort %0, %1, %2" : "=v"(dsk_) : "v"(vd), "s"(Dsk) : "memory");
    const char* ub = ws + WS_XT + (size_t)d * SEQ * 2 + hw * 2048;
    const unsigned vu = lane * 16;
    asm volatile("global_load_dwordx4 %0, %1, %2" : "=v"(u0) : "v"(vu), "s"(ub) : "memory");
    asm volatile("global_load_dwordx4 %0, %1, %2 offset:1024" : "=v"(u1) : "v"(vu), "s"(ub) : "memory");
    const char* pb = (const char*)POSTAB.v + hw * 2048;
    const unsigned vp = lane * 32;
    asm volatile("global_load_dwordx4 %0, %1, %2" : "=v"(pt0) : "v"(vp), "s"(pb) : "memory");
    asm volatile("global_load_dwordx4 %0, %1, %2 offset:16" : "=v"(pt1) : "v"(vp), "s"(pb) : "memory");
    if (hw == 0 && lane < 32) *(LAS u32x4*)(cb + CH_U + lane * 16) = (u32x4){0u, 0u, 0u, 0u};
    if (hw == 1 && lane < 4) *(LAS u32x4*)(cb + CH_X + 4096 + lane * 16) = (u32x4){0u, 0u, 0u, 0u};
  }
  BAR();
#define K2_PRIO 3
  __builtin_amdgcn_s_setprio(K2_PRIO);
  STAMP(1, 0);
  asm volatile("s_waitcnt vmcnt(0)" : "+v"(la_[0]), "+v"(la_[1]), "+v"(ai_[0]), "+v"(ai_[1]), "+v"(cr_[0]), "+v"(cr_[1]), "+v"(cr_[2]), "+v"(cr_[3]), "+v"(ci_[0]), "+v"(ci_[1]),
               "+v"(ci_[2]), "+v"(ci_[3]), "+v"(ldt_), "+v"(dsk_), "+v"(u0), "+v"(u1), "+v"(pt0), "+v"(pt1)::"memory");
  float cr[4], ci[4];
#pragma unroll
  for (int p = 0; p < 4; ++p) { cr[p] = bf2f((unsigned short)cr_[p]); ci[p] = bf2f((unsigned short)ci_[p]); }
  const float dt = rbf(expf(bf2f((unsigned short)ldt_))), dsk = bf2f((unsigned short)dsk_);
  *(LAS u32x4*)(cb + CH_U + 512 + hw * 2048 + lane * 16) = u0;
  *(LAS u32x4*)(cb + CH_U + 512 + hw * 2048 + 1024 + lane * 16) = u1;
  float sabs = 0.f;
#pragma unroll
  for (int p = 0; p < 4; ++p) sabs += __builtin_fabsf(cr[p]) + __builtin_fabsf(ci[p]);
  sabs = xsum16_32(red16(sabs));
  sabs *= 2.0f * dt;
  int ex = (int)((__float_as_uint(sabs) >> 23) & 0xff) - 126;
  ex = ex < -100 ? -100 : (ex > 100 ? 100 : ex);
  const float scale = __uint_as_float((unsigned)(127 + 15 - ex) << 23), iscale = __uint_as_float((unsigned)(127 - 15 + ex) << 23);
  STAMP(1, 1);
  f32x4 kacc[3] = {};
  LAS char* fw = cb + CH_FT + hw * CH_FW;
#pragma unroll 1
  for (int p = 0; p < 2; ++p) {
    const float lap = bf2f((unsigned short)(p ? la_[1] : la_[0])), aip = bf2f((unsigned short)(p ? ai_[1] : ai_[0]));
    const float crp = p ? cr[1] : cr[0], cip = p ? ci[1] : ci[0];
    const float lre = -rbf(expf(lap)) * dt, lim = aip * dt;
    const float al = lre * 1.4426950408889634f, be = lim * 0.15915494309189535f;
    LAS char* wp = fw + lane * 4;
#define PUTH(row, v) *(LAS unsigned*)(wp + (row) * CH_FS) = (v)
    {
      const f32x2 w = cexp_p(al, be, 16.f);
      const float cs = 2.0f * dt * scale;
      const f32x2 c0 = {crp * cs, cip * cs};
      const f32x2 w2 = cmul(w, w), c1 = cmul(c0, w), w4 = cmul(w2, w2), c2 = cmul(c0, w2), c3 = cmul(c1, w2), w8 = cmul(w4, w4), w12 = cmul(w8, w4);
      const unsigned hc[4] = {hcv(c0), hcv(c1), hcv(c2), hcv(c3)}, hw4[3] = {hcv(w4), hcv(w8), hcv(w12)};
#pragma unroll
      for (int j = 0; j < 4; ++j) {
        PUTH(j, hc[j]);
#pragma unroll
        for (int k = 0; k < 3; ++k) PUTH(4 * (k + 1) + j, hmul(hc[j], hw4[k]));
      }
    }
    {
      const f32x2 e1 = cexp_p(al, -be, 1.f);
      const f32x2 e2 = cmul(e1, e1), e3 = cmul(e2, e1), e4 = cmul(e2, e2), e8 = cmul(e4, e4), e12 = cmul(e8, e4);
      unsigned E[16];
      E[0] = 0x00003c00u; E[1] = hcv(e1); E[2] = hcv(e2); E[3] = hcv(e3); E[4] = hcv(e4); E[8] = hcv(e8); E[12] = hcv(e12);
#pragma unroll
      for (int m = 1; m < 4; ++m)
#pragma unroll
        for (int j = 1; j < 4; ++j) E[4 * m + j] = hmul(E[4 * m], E[j]);
#pragma unroll
      for (int k = 0; k < 16; ++k) PUTH(16 + k, E[k]);
      const f32x2 z256 = cexp_p(al, -be, 256.f), z512 = cexp_p(al, -be, 512.f), z1024 = cexp_p(al, -be, 1024.f), z2048 = cexp_p(al, -be, 2048.f);
      const f32x2 z768 = cmul(z512, z256);
      const unsigned h256 = hcv(z256), h512 = hcv(z512), h768 = hcv(z768);
      PUTH(32, h256);
#pragma unroll
      for (int k = 1; k < 8; ++k) PUTH(32 + k, hmul(h256, E[2 * k]));
      PUTH(40, h512); PUTH(44, h768);
#pragma unroll
      for (int k = 1; k < 4; ++k) { PUTH(40 + k, hmul(h512, E[4 * k])); PUTH(44 + k, hmul(h768, E[4 * k])); }
      const unsigned hb[4] = {hcv(z1024), hcv(cmul(z1024, z256)), hcv(cmul(z1024, z512)), hcv(cmul(z1024, z768))};
#pragma unroll
      for (int k = 0; k < 4; ++k) { PUTH(48 + 2 * k, hb[k]); PUTH(49 + 2 * k, hmul(hb[k], E[8])); }
      PUTH(56, hcv(z2048));
    }
#undef PUTH
    asm volatile("s_waitcnt lgkmcnt(0)" ::: "memory");
    LAS const char* rp = fw + (lane & 15) * CH_FS + (lane >> 4) * 16;
    u32x4 fa[4], fb[4][3];
#pragma unroll
    for (int kk = 0; kk < 4; ++kk) {
      fa[kk] = *(LAS const u32x4*)(rp + kk * 64);
#pragma unroll
      for (int t = 0; t < 3; ++t) fb[kk][t] = *(LAS const u32x4*)(rp + (16 + 16 * t) * CH_FS + kk * 64);
    }
#pragma unroll
    for (int kk = 0; kk < 4; ++kk)
#pragma unroll
      for (int t = 0; t < 3; ++t) kacc[t] = mfma_h(fa[kk], fb[kk][t], kacc[t]);
    asm volatile("" ::: "memory");
  }
  STAMP(1, 2);
#pragma unroll
  for (int t = 0; t < 3; ++t) *(LAS f32x4*)(cb + CH_T + hw * 3072 + (16 * t + (lane & 15)) * 64 + (lane >> 4) * 16) = kacc[t];
  lds_sync(cnt, 2, lane);
  {
    const int x0 = 1024 * hw + 16 * lane;
    const unsigned po[8] = {pt0[0], pt0[1], pt0[2], pt0[3], pt1[0], pt1[1], pt1[2], pt1[3]};
    unsigned w[8];
#pragma unroll
    for (int k = 0; k < 8; ++k) {
      LAS const char* a0 = cb + CH_T + (po[k] & 0xffffu);
      LAS const char* a1 = cb + CH_T + (po[k] >> 16);
      w[k] = pk_f16(*(LAS const float*)a0 + *(LAS const float*)(a0 + 3072), *(LAS const float*)a1 + *(LAS const float*)(a1 + 3072));
    }
    *(LAS u32x4*)(cb + CH_X + x0 * 2) = (u32x4){w[0], w[1], w[2], w[3]};
    *(LAS u32x4*)(cb + CH_X + x0 * 2 + 16) = (u32x4){w[4], w[5], w[6], w[7]};
  }
  lds_sync(cnt, 4, lane);
#pragma unroll
  for (int it = 0; it < 3; ++it) {
    const int y = hw * 64 + lane + 128 * it;
    if (it < 2 || y <= 256) {
      const u32x4 r0 = *(LAS const u32x4*)(cb + CH_X + y * 16), r1 = *(LAS const u32x4*)(cb + CH_X + y * 16 + 16);
      unsigned W[8] = {r0[0], r0[1], r0[2], r0[3], r1[0], r1[1], r1[2], r1[3]}, V[7];
#pragma unroll
      for (int i = 0; i < 7; ++i) V[i] = __builtin_amdgcn_alignbit(W[i + 1], W[i], 16);
#pragma unroll
      for (int m = 0; m < 4; ++m) {
        *(LAS u32x4*)(cb + CH_FT + (2 * m) * CH_CS + y * 16) = (u32x4){W[m], W[m + 1], W[m + 2], W[m + 3]};
        *(LAS u32x4*)(cb + CH_FT + (2 * m + 1) * CH_CS + y * 16) = (u32x4){V[m], V[m + 1], V[m + 2], V[m + 3]};
      }
    }
  }
  lds_sync(cnt, 6, lane);
  STAMP(1, 3);
  if (hw) s4d_conv<1>(cb, ws, d, lane, iscale, dsk); else s4d_conv<0>(cb, ws, d, lane, iscale, dsk);
  STAMP(1, 5);
}

__global__ __launch_bounds__(512, 2) void k2_mix(const unsigned short* __restrict__ w_sp, const unsigned short* __restrict__ b_sp, const unsigned short* __restrict__ logA,
                                                 const unsigned short* __restrict__ Aim, const unsigned short* __restrict__ Cre, const unsigned short* __restrict__ Cim,
                                                 const unsigned short* __restrict__ logdt, const unsigned short* __restrict__ Dsk, const unsigned short* __restrict__ w_out,
                                                 char* __restrict__ ws) {
  extern __shared__ __attribute__((aligned(16))) char smem_raw[];
  LAS char* smem = (LAS char*)smem_raw;
  const int tid = threadIdx.x, lane = tid & 63;
  const int wave = __builtin_amdgcn_readfirstlane(tid >> 6);
  const int b = blockIdx.x, xcd = b & 7, rr = b >> 3;
  if (tid < 64) {
    if (lane < 32) *(LAS unsigned*)(smem + K2_CH + CH_CNT + lane * 4) = 0u;
    else *(LAS unsigned*)(smem + K2_CH + K2_CHS + CH_CNT + (lane - 32) * 4) = 0u;
  }
  if (wave < 4) {
    BAR();
    const int T = 8 * (xcd >> 1) + (rr & 7), J = 4 * (xcd & 1) + (rr >> 3);
#define K2_ROT 0
    k2_gemm(smem, w_sp, b_sp, ws, wave, lane, T, J, K2_ROT == 1 ? (2 * (rr & 7) + 4 * (rr >> 3)) & 15 : K2_ROT == 2 ? 2 * (rr & 1) : 0);
  } else {
    const int sw = wave - 4;
    if (b < 224) {
      k2_s4d(smem + K2_CH + (sw >> 1) * K2_CHS, logA, Aim, Cre, Cim, logdt, Dsk, ws, 2 * b + (sw >> 1), sw & 1, lane);
    } else {
      BAR();
      for (int fr = (b - 224) * 4 + sw; fr < 784; fr += 128) {
        const int dt = fr / 28, fb = fr - dt * 28;
        const int dcol = 16 * dt + (lane & 15), q = lane >> 4;
        unsigned short v[8];
#pragma unroll
        for (int jj = 0; jj < 8; ++jj) v[jj] = w_out[(size_t)(32 * fb + 16 * (jj >> 2) + 4 * q + (jj & 3)) * DM + dcol];
        u32x4 o = {(unsigned)v[0] | ((unsigned)v[1] << 16), (unsigned)v[2] | ((unsigned)v[3] << 16), (unsigned)v[4] | ((unsigned)v[5] << 16), (unsigned)v[6] | ((unsigned)v[7] << 16)};
        *(u32x4*)(ws + WS_WOF + ((size_t)fr * 64 + lane) * 16) = o;
      }
    }
  }
}

constexpr int K3_LDS = 49152 + 8448;
__global__ __launch_bounds__(256, 1) void k3_out(const unsigned short* __restrict__ x, char* __restrict__ ws, unsigned short* __restrict__ out) {
  extern __shared__ __attribute__((aligned(16))) char smem_raw[];
  LAS char* smem = (LAS char*)smem_raw;
  const int tid = threadIdx.x, lane = tid & 63;
  const int wave = __builtin_amdgcn_readfirstlane(tid >> 6);
  const int b = blockIdx.x, xcd = b & 7, rr = b >> 3;
  const int T = 4 * xcd + rr / 7, Dt = rr % 7;
  STAMP(2, 0);
  const u32x4* hp = (const u32x4*)(ws + WS_HF + ((size_t)(4 * T * 28 + 7 * wave) * 64 + lane) * 16);
  const u32x4* wp = (const u32x4*)(ws + WS_WOF + ((size_t)(4 * Dt * 28 + 7 * wave) * 64 + lane) * 16);
  u32x4 hf[7][4], wf[7][4];
#pragma unroll
  for (int k = 0; k < 7; ++k)
#pragma unroll
    for (int m = 0; m < 4; ++m) { hf[k][m] = hp[(m * 28 + k) * 64]; wf[k][m] = wp[(m * 28 + k) * 64]; }
  const int erow = tid >> 2, ec = tid & 3;
  const u32x4 st0 = *(const u32x4*)(ws + WS_ST + ((size_t)(64 * Dt + erow) * SEQ + 64 * T + 16 * ec) * 2);
  const u32x4 st1 = *(const u32x4*)(ws + WS_ST + ((size_t)(64 * Dt + erow) * SEQ + 64 * T + 16 * ec + 8) * 2);
  const u32x4 x0 = *(const u32x4*)(x + (size_t)(64 * T + erow) * DM + 64 * Dt + 16 * ec);
  const u32x4 x1 = *(const u32x4*)(x + (size_t)(64 * T + erow) * DM + 64 * Dt + 16 * ec + 8);
  __builtin_amdgcn_sched_barrier(0);
  f32x4 acc[4][4];
#pragma unroll
  for (int n = 0; n < 4; ++n)
#pragma unroll
    for (int m = 0; m < 4; ++m) acc[n][m] = (f32x4){0.f, 0.f, 0.f, 0.f};
#pragma unroll
  for (int k = 0; k < 7; ++k)
#pragma unroll
    for (int n = 0; n < 4; ++n)
#pragma unroll
      for (int m = 0; m < 4; ++m) acc[n][m] = mfma_bf(__builtin_bit_cast(s16x8, wf[k][n]), __builtin_bit_cast(s16x8, hf[k][m]), acc[n][m]);
  STAMP(2, 1);
  LAS char* sl = smem + 49152;
  {
    LAS unsigned* p = (LAS unsigned*)(sl + erow * 132 + ec * 32);
#pragma unroll
    for (int e = 0; e < 4; ++e) { p[e] = st0[e]; p[4 + e] = st1[e]; }
  }
#pragma unroll
  for (int m = 0; m < 4; ++m) {
    if (m != wave) {
      const int slot = m > wave ? m - 1 : m;
#pragma unroll
      for (int n = 0; n < 4; ++n) *(LAS f32x4*)(smem + ((wave * 3 + slot) * 4 + n) * 1024 + lane * 16) = acc[n][m];
    }
  }
  __syncthreads();
  f32x4 o[4];
  if (wave == 0) { for (int n = 0; n < 4; ++n) o[n] = acc[n][0]; }
  else if (wave == 1) { for (int n = 0; n < 4; ++n) o[n] = acc[n][1]; }
  else if (wave == 2) { for (int n = 0; n < 4; ++n) o[n] = acc[n][2]; }
  else { for (int n = 0; n < 4; ++n) o[n] = acc[n][3]; }
#pragma unroll
  for (int sw = 0; sw < 4; ++sw) {
    if (sw != wave) {
      const int slot = wave > sw ? wave - 1 : wave;
#pragma unroll
      for (int n = 0; n < 4; ++n) o[n] += *(LAS const f32x4*)(smem + ((sw * 3 + slot) * 4 + n) * 1024 + lane * 16);
    }
  }
  __syncthreads();
  {
    const int t = 16 * wave + (lane & 15), q = lane >> 4;
#pragma unroll
    for (int n = 0; n < 4; ++n) {
      f32x4 g;
#pragma unroll
      for (int j = 0; j < 4; ++j) {
        const float v = rbf(o[n][j]);
        g[j] = rbf(v * __builtin_amdgcn_rcpf(1.0f + __builtin_amdgcn_exp2f(-1.4426950408889634f * v)));
      }
      *(LAS f32x4*)(smem + t * 272 + (16 * n + 4 * q) * 4) = g;
    }
  }
  __syncthreads();
  {
    unsigned ow[8];
#pragma unroll
    for (int k = 0; k < 4; ++k) {
      const f32x4 g = *(LAS const f32x4*)(smem + erow * 272 + (16 * ec + 4 * k) * 4);
      float r[4];
#pragma unroll
      for (int j = 0; j < 4; ++j) {
        const int dl = 16 * ec + 4 * k + j;
        const float sv = bf2f(*(LAS const unsigned short*)(sl + dl * 132 + erow * 2));
        const unsigned xw = (k < 2 ? x0 : x1)[((4 * k + j) >> 1) & 3];
        const float xv = (j & 1) ? bfhi(xw) : bflo(xw);
        r[j] = xv + rbf(sv * g[j]);
      }
      ow[2 * k] = pk_bf16(r[0], r[1]); ow[2 * k + 1] = pk_bf16(r[2], r[3]);
    }
    u32x4* op = (u32x4*)(out + (size_t)(64 * T + erow) * DM + 64 * Dt + 16 * ec);
    op[0] = (u32x4){ow[0], ow[1], ow[2], ow[3]};
    op[1] = (u32x4){ow[4], ow[5], ow[6], ow[7]};
  }
  STAMP(2, 2);
}

extern "C" void kernel_launch(void* const* d_in, const int* in_sizes, int n_in, void* d_out, int out_size, void* d_ws, size_t ws_size, hipStream_t stream) {
  const unsigned short* x = (const unsigned short*)d_in[0];
  const unsigned short* logA = (const unsigned short*)d_in[1];
  const unsigned short* Aim = (const unsigned short*)d_in[2];
  const unsigned short* Cre = (const unsigned short*)d_in[3];
  const unsigned short* Cim = (const unsigned short*)d_in[4];
  const unsigned short* logdt = (const unsigned short*)d_in[5];
  const unsigned short* Dsk = (const unsigned short*)d_in[6];
  const unsigned short* w_in = (const unsigned short*)d_in[8];
  const unsigned short* w_sp = (const unsigned short*)d_in[9];
  const unsigned short* b_sp = (const unsigned short*)d_in[10];
  const unsigned short* w_out = (const unsigned short*)d_in[11];
  char* ws = (char*)d_ws;
  (void)hipFuncSetAttribute((const void*)k1_gemm1, hipFuncAttributeMaxDynamicSharedMemorySize, K1_LDS);
  (void)hipFuncSetAttribute((const void*)k2_mix, hipFuncAttributeMaxDynamicSharedMemorySize, K2_LDS);
  (void)hipFuncSetAttribute((const void*)k3_out, hipFuncAttributeMaxDynamicSharedMemorySize, K3_LDS);
  k1_gemm1<<<256, 512, K1_LDS, stream>>>(x, w_in, ws);
  k2_mix<<<256, 512, K2_LDS, stream>>>(w_sp, b_sp, logA, Aim, Cre, Cim, logdt, Dsk, w_out, ws);
  k3_out<<<224, 256, K3_LDS, stream>>>(x, ws, (unsigned short*)d_out);
}
```

```cpp
#include <hip/hip_runtime.h>
#include <hip/hip_bf16.h>
#include <stdint.h>

#define LAS __attribute__((address_space(3)))
#define GAS __attribute__((address_space(1)))
typedef float f32x2 __attribute__((ext_vector_type(2)));
typedef float f32x4 __attribute__((ext_vector_type(4)));
typedef unsigned u32x2 __attribute__((ext_vector_type(2)));
typedef unsigned u32x4 __attribute__((ext_vector_type(4)));
typedef short s16x4 __attribute__((ext_vector_type(4)));
typedef short s16x8 __attribute__((ext_vector_type(8)));
typedef __bf16 bf16x2 __attribute__((ext_vector_type(2)));
typedef __bf16 bf16x8 __attribute__((ext_vector_type(8)));
typedef _Float16 f16x2 __attribute__((ext_vector_type(2)));
typedef _Float16 f16x8 __attribute__((ext_vector_type(8)));

constexpr int SEQ = 2048, DM = 448, FH = 896, FF = 1792, NST = 256;

constexpr size_t WS_Z1D = 0;
constexpr size_t WS_Z2F = WS_Z1D + (size_t)SEQ * FH * 2;
constexpr size_t WS_HF  = WS_Z2F + (size_t)SEQ * FH * 2;
constexpr size_t WS_XT  = WS_HF + (size_t)SEQ * FH * 2;
constexpr size_t WS_ST  = WS_XT + (size_t)SEQ * DM * 2;
constexpr size_t WS_WOF = WS_ST + (size_t)SEQ * DM * 2;
constexpr size_t WS_XCH = WS_WOF + (size_t)FH * DM * 2;
constexpr size_t WS_EP  = WS_XCH + 32 * 64 * 8 * 16;
constexpr size_t WS_XS1 = WS_EP + 32 * 128;
constexpr size_t WS_END = WS_XS1 + 32 * 1024;

constexpr size_t WS_DBG = 32u << 20;
#define STAMP(kern, slot) do {} while (0)
#define MARK_DECL do {} while (0)
#define MARK(i) do {} while (0)
#define MARK_DUMP(kern) do {} while (0)
__device__ __forceinline__ float bf2f(unsigned short h) { return __uint_as_float((unsigned)h << 16); }
__device__ __forceinline__ float bflo(unsigned w) { return __uint_as_float(w << 16); }
__device__ __forceinline__ float bfhi(unsigned w) { return __uint_as_float(w & 0xffff0000u); }
__device__ __forceinline__ unsigned pk_bf16(float lo, float hi) {
  bf16x2 v = {(__bf16)lo, (__bf16)hi};
  return __builtin_bit_cast(unsigned, v);
}
__device__ __forceinline__ float rbf(float x) { return (float)(__bf16)x; }
__device__ __forceinline__ unsigned pk_f16(float lo, float hi) {
  f16x2 v = {(_Float16)lo, (_Float16)hi};
  return __builtin_bit_cast(unsigned, v);
}
__device__ __forceinline__ float gelu_tanh(float x) {
  const float c1 = -2.0f * 0.7978845608028654f * 1.4426950408889634f, c2 = c1 * 0.044715f;
  float t = x * x;
  float a = x * __builtin_fmaf(t, c2, c1);
  float e = __builtin_amdgcn_exp2f(a);
  return x * __builtin_amdgcn_rcpf(1.0f + e);
}
template <int CTRL> __device__ __forceinline__ float dpp_add(float v) {
  int t = __builtin_amdgcn_update_dpp(0, __float_as_int(v), CTRL, 0xf, 0xf, true);
  return v + __int_as_float(t);
}
__device__ __forceinline__ float red16(float v) {
  v = dpp_add<0xB1>(v);
  v = dpp_add<0x4E>(v);
  v = dpp_add<0x141>(v);
  v = dpp_add<0x140>(v);
  return v;
}
__device__ __forceinline__ float red8(float v) {
  v = dpp_add<0xB1>(v); v = dpp_add<0x4E>(v); v = dpp_add<0x141>(v);
  return v;
}
__device__ __forceinline__ void glds16(const void* sbase, unsigned voff, unsigned lds) {
  asm volatile("s_mov_b32 m0, %2\n\ts_nop 0\n\tglobal_load_lds_dwordx4 %0, %1" ::"v"(voff), "s"(sbase), "s"(lds) : "memory");
}
#define WAIT_VM(n) asm volatile("s_waitcnt vmcnt(%0)" ::"n"(n) : "memory")
#define WAIT_LGKM0() asm volatile("s_waitcnt lgkmcnt(0)" ::: "memory")
#define BAR() do { asm volatile("s_waitcnt lgkmcnt(0)" ::: "memory"); __builtin_amdgcn_s_barrier(); asm volatile("" ::: "memory"); } while (0)
__device__ __forceinline__ s16x4 tr_read(LAS const char* p) {
  typedef short v4i16 __attribute__((ext_vector_type(4)));
  return __builtin_amdgcn_ds_read_tr16_b64_v4i16((LAS v4i16*)p);
}
__device__ __forceinline__ f32x4 mfma_bf(s16x8 a, s16x8 b, f32x4 c) {
  return __builtin_amdgcn_mfma_f32_16x16x32_bf16(__builtin_bit_cast(bf16x8, a), __builtin_bit_cast(bf16x8, b), c, 0, 0, 0);
}
__device__ __forceinline__ float dot2bf(unsigned a, unsigned b, float c) {
  return __builtin_amdgcn_fdot2_f32_bf16(__builtin_bit_cast(bf16x2, a), __builtin_bit_cast(bf16x2, b), c, false);
}

constexpr int K1_X = 0;
constexpr int K1_W = 57344;
constexpr int K1_SLOT = 16384;
constexpr int K1_S = K1_W + 6 * K1_SLOT;
constexpr int K1_P = K1_S;
constexpr int K1_M2 = K1_S + 2048;
constexpr int K1_R2 = K1_M2 + 256;
constexpr int K1_W1 = K1_R2 + 256;
constexpr int K1_LDS = 163840;
constexpr unsigned ONES_BF = 0x3f803f80u;
typedef float f32x16 __attribute__((ext_vector_type(16)));

__device__ __forceinline__ float xsum32(float v) {
  auto b = __builtin_amdgcn_permlane32_swap(__float_as_uint(v), __float_as_uint(v), false, false);
  return __uint_as_float(b[0]) + __uint_as_float(b[1]);
}
__device__ __forceinline__ float xsum16_32(float v) {
  auto a = __builtin_amdgcn_permlane16_swap(__float_as_uint(v), __float_as_uint(v), false, false);
  return xsum32(__uint_as_float(a[0]) + __uint_as_float(a[1]));
}
__device__ __forceinline__ f32x2 up_bf(unsigned w) { return (f32x2){bflo(w), bfhi(w)}; }
__device__ __forceinline__ unsigned rn_pair(f32x2 v) {
  unsigned r;
  asm("v_cvt_pk_bf16_f32 %0, %1, %2" : "=v"(r) : "v"(v.x), "v"(v.y));
  return r;
}
__device__ __forceinline__ f32x2 gelu2(f32x2 x) {
  const float c1 = -2.0f * 0.7978845608028654f * 1.4426950408889634f, c2 = c1 * 0.044715f;
  const f32x2 t = x * x;
  const f32x2 a = x * (t * c2 + c1);
  f32x2 d = {__builtin_amdgcn_exp2f(a.x), __builtin_amdgcn_exp2f(a.y)};
  d = d + 1.0f;
  return x * (f32x2){__builtin_amdgcn_rcpf(d.x), __builtin_amdgcn_rcpf(d.y)};
}
__device__ __forceinline__ void ln_scalars(float s, float q, float invn, float& mb, float& rr) {
  const float mean = s * invn;
  float var = q * invn - mean * mean; var = var < 0.f ? 0.f : var;
  mb = rbf(mean);
  rr = __builtin_amdgcn_rcpf(rbf(__builtin_amdgcn_sqrtf(rbf(rbf(var) + 1e-5f))));
}
__device__ __forceinline__ f32x16 mfma32_bf(s16x8 a, s16x8 b, f32x16 c) {
  return __builtin_amdgcn_mfma_f32_32x32x16_bf16(__builtin_bit_cast(bf16x8, a), __builtin_bit_cast(bf16x8, b), c, 0, 0, 0);
}
__device__ __forceinline__ void poll_granule(u32x4& gr, const char* base, unsigned voff, unsigned tag) {
  for (unsigned spins = 0;; ++spins) {
    const bool ok = (gr[1] == tag) && (gr[3] == tag);
    if (__all(ok) || spins > (1u << 22)) break;
    __builtin_amdgcn_s_sleep(1);
    asm volatile("global_load_dwordx4 %0, %1, %2 sc1\n\ts_waitcnt vmcnt(0)" : "=&v"(gr) : "v"(voff), "s"(base) : "memory");
  }
}

template <int V> struct IC { static constexpr int value = V; };
template <int A, int B, class F> __device__ __forceinline__ void static_for(F&& f) {
  if constexpr (A < B) { f(IC<A>{}); static_for<A + 1, B>(f); }
}
#define K1_PREFETCH 1
__device__ __forceinline__ void k1_xt(LAS char* smem, const unsigned short* __restrict__ x, const unsigned short* __restrict__ w_sp, char* __restrict__ ws, int v, int tid) {
  const int row = tid >> 3, c8 = tid & 7;
  const unsigned tag = __hip_atomic_load((GAS unsigned*)(ws + WS_EP + v * 128), __ATOMIC_RELAXED, __HIP_MEMORY_SCOPE_AGENT) + 1u;
  u32x4 xv[7];
#pragma unroll
  for (int m = 0; m < 7; ++m) xv[m] = *(const u32x4*)(x + (size_t)(64 * v + row) * DM + (c8 + 8 * m) * 8);
  float s = 0.f, q = 0.f;
#pragma unroll
  for (int m = 0; m < 7; ++m)
#pragma unroll
    for (int e = 0; e < 4; ++e) { const unsigned w = xv[m][e]; s = dot2bf(w, ONES_BF, s); q = dot2bf(w, w, q); }
  s = red8(s); q = red8(q);
  float mb, rr;
  ln_scalars(s, q, 1.0f / 448.0f, mb, rr);
  if (c8 == 0) {
    u32x4 gr = {__float_as_uint(mb), tag, __float_as_uint(rr), tag};
    const char* gp = ws + WS_XS1 + (size_t)v * 1024;
    const unsigned vo = row * 16;
    asm volatile("global_store_dwordx4 %0, %1, %2 sc1\n\ts_nop 1" ::"v"(vo), "v"(gr), "s"(gp) : "memory");
  }
#pragma unroll
  for (int m = 0; m < 7; ++m)
#pragma unroll
    for (int e = 0; e < 4; ++e) {
      const unsigned w = xv[m][e];
      const f32x2 n3 = up_bf(rn_pair(up_bf(rn_pair(up_bf(w) - mb)) * rr));
      const int d = (c8 + 8 * m) * 8 + 2 * e;
      *(LAS _Float16*)(smem + d * 144 + row * 2) = (_Float16)n3.x;
      *(LAS _Float16*)(smem + (d + 1) * 144 + row * 2) = (_Float16)n3.y;
    }
  __syncthreads();
#pragma unroll
  for (int m = 0; m < 7; ++m) {
    const int d = row + 64 * m;
    *(u32x4*)(ws + WS_XT + ((size_t)d * SEQ + 64 * v + 8 * c8) * 2) = *(LAS const u32x4*)(smem + d * 144 + c8 * 16);
  }
  {
    const u32x4* p = (const u32x4*)((const char*)w_sp + (size_t)v * 64 * 4096) + tid;
    u32x4 a = {0u, 0u, 0u, 0u};
#pragma unroll
    for (int o = 0; o < 2; ++o) {
      u32x4 t[16];
#pragma unroll
      for (int m = 0; m < 16; ++m) t[m] = p[(o * 16 + m) * 512];
#pragma unroll
      for (int m = 0; m < 16; ++m) a ^= t[m];
    }
    if (a[0] == 0x9e3779b9u && a[1] == 0x7f4a7c15u && a[2] == 0x85ebca6bu && a[3] == 0xc2b2ae35u) *(unsigned*)(ws + WS_END) = a[0];
  }
}

__global__ __launch_bounds__(512, 2) void k1_gemm1(const unsigned short* __restrict__ x, const unsigned short* __restrict__ w_in, const unsigned short* __restrict__ w_sp, char* __restrict__ ws) {
  extern __shared__ __attribute__((aligned(16))) char smem_raw[];
  LAS char* smem = (LAS char*)smem_raw;
  const int tid = threadIdx.x, lane = tid & 63;
  const int wave = __builtin_amdgcn_readfirstlane(tid >> 6);
  const int b = blockIdx.x;
  if (b >= 224) { k1_xt(smem, x, w_sp, ws, b - 224, tid); return; }
  const int xcd = b & 7, rr = b >> 3;
  const int ti = 4 * xcd + rr / 7, tj = rr % 7, rh = wave >> 2, cg = wave & 3;
  const int h = lane >> 5, l31 = lane & 31;

  STAMP(0, 0);
  unsigned ep;
  {
    const char* epp = ws + WS_EP + ti * 128;
    unsigned zero = 0;
    asm volatile("global_load_dword %0, %1, %2 sc1" : "=v"(ep) : "v"(zero), "s"(epp) : "memory");
  }
  const int xrow = 8 * wave + (lane >> 3);
  const unsigned voffx = (unsigned)(xrow * 896 + (((lane & 7) ^ ((xrow >> 1) & 7)) * 16));
  const char* xsrc = (const char*)x + (size_t)ti * 64 * 896;
  unsigned voffw[2];
#pragma unroll
  for (int e = 0; e < 2; ++e) voffw[e] = (unsigned)((8 * wave + 4 * e + (lane >> 4)) * 3584 + (((lane & 15) ^ (4 * (lane >> 4))) * 16) + tj * 256);
#define K1_XPIECE(g) glds16(xsrc + (g) * 128, voffx, (unsigned)(K1_X + (g) * 8192 + wave * 1024))
#define K1_WPIECE(G, e) \
  glds16(w_in, voffw[e] + (unsigned)(((G) % 7) * 64 * 3584 + ((G) < 7 ? 1792 : 0)), (unsigned)(K1_W + ((G) % 6) * K1_SLOT + wave * 2048 + (e) * 1024))
#pragma unroll
  for (int g = 0; g < 3; ++g) { K1_XPIECE(g); K1_WPIECE(g, 0); K1_WPIECE(g, 1); }
  LAS const char* ab[2];
  LAS const char* xb[4];
  {
    const int g4 = lane >> 4, qq = (lane & 15) >> 2, p4 = lane & 3;
    const int lb = (8 * (g4 >> 1) + qq) * 256 + (((4 * cg + 2 * (g4 & 1) + (p4 >> 1)) ^ (4 * qq)) * 16) + (p4 & 1) * 8;
    ab[0] = smem + K1_W + lb + rh * 4096;
    ab[1] = smem + K1_W + lb + (rh ^ 1) * 4096;
    const int row = 32 * rh + l31, sw = (row >> 1) & 7;
#pragma unroll
    for (int i = 0; i < 4; ++i) xb[i] = smem + K1_X + row * 128 + (((2 * (i ^ rh) + h) ^ sw) * 16);
  }
  s16x8 xa[28], wb[8];
#define K1_FRAGS(st)                                                                                                   \
  do {                                                                                                                 \
    const int G_ = (st) >> 2, i_ = (st) & 3, so_ = (G_ % 6) * K1_SLOT + (i_ >> 1) * 8192;                              \
    if ((st) < 28) xa[(st) % 28] = *(LAS const s16x8*)(xb[i_] + G_ * 8192);                                            \
    wb[(st) & 7] = __builtin_shufflevector(tr_read(ab[i_ & 1] + so_), tr_read(ab[i_ & 1] + so_ + 1024), 0, 1, 2, 3, 4, 5, 6, 7); \
  } while (0)
  f32x16 acc2 = {}, acc1 = {};
  float w1a = 0.f, w1b = 0.f;
  u32x4 g1;
  const char* g1p = ws + WS_XS1 + (size_t)ti * 1024;
  const unsigned g1o = (unsigned)(32 * rh + l31) * 16;
  unsigned zp[8];
  float m1 = 0.f, r1 = 0.f;
  asm volatile("s_waitcnt vmcnt(3)" : "+v"(ep)::"memory");
  const unsigned tag = ep + 1u;
  BAR();
  STAMP(0, 1);
  K1_FRAGS(0); K1_FRAGS(1); K1_FRAGS(2); K1_FRAGS(3);
  auto step = [&](auto stc) __attribute__((always_inline)) {
    constexpr int st = decltype(stc)::value;
    constexpr int G = st >> 2, i = st & 3;
    if (i == 0 && G >= 1) {
      if (G == 1) WAIT_VM(6); else if (G == 2) WAIT_VM(9); else if (G == 3) WAIT_VM(8); else if (G == 4) WAIT_VM(7); else if (G <= 6) WAIT_VM(6);
      else if (G == 7) WAIT_VM(0);
      else if (G <= 9) WAIT_VM(6); else if (G == 10) WAIT_VM(4); else if (G == 11) WAIT_VM(2); else WAIT_VM(0);
      __builtin_amdgcn_s_barrier();
      asm volatile("" ::: "memory");
    }
    __builtin_amdgcn_sched_barrier(0);
    if constexpr (st < 28) acc2 = mfma32_bf(wb[st & 7], xa[st % 28], acc2); else acc1 = mfma32_bf(wb[st & 7], xa[st % 28], acc1);
    if constexpr (st + 4 < 56) K1_FRAGS(st + 4);
    if constexpr ((i & 1) == 0) {
      const u32x4 wv = __builtin_bit_cast(u32x4, wb[st & 7]);
#pragma unroll
      for (int e = 0; e < 4; ++e) { const unsigned ww = wv[e]; if (st < 28) w1a = dot2bf(ww, ONES_BF, w1a); else w1b = dot2bf(ww, ONES_BF, w1b); }
    }
    __builtin_amdgcn_sched_barrier(0);
    if constexpr ((i & 1) == 0) {
      constexpr int ks = st >> 1;
      if constexpr (ks < 4) { K1_XPIECE(ks + 3); K1_WPIECE(ks + 3, 0); K1_WPIECE(ks + 3, 1); }
      else if constexpr (ks < 18) K1_WPIECE(5 + (ks >> 1), ks & 1);
    }
    if constexpr (st == 20) asm volatile("global_load_dwordx4 %0, %1, %2 sc1" : "=v"(g1) : "v"(g1o), "s"(g1p) : "memory");
    __builtin_amdgcn_sched_barrier(0);
  };
  static_for<0, 28>(step);
    {
      STAMP(0, 3);
      const float w1s = xsum32(w1a);
      if (h == 0) *(LAS float*)(smem + K1_W1 + (rh * 128 + 32 * cg + l31) * 4) = w1s;
      asm volatile("s_waitcnt vmcnt(0)" : "+v"(g1)::"memory");
      BAR();
      poll_granule(g1, g1p, g1o, tag);
      m1 = __uint_as_float(g1[0]); r1 = __uint_as_float(g1[2]);
      float ss = 0.f, sq = 0.f;
#pragma unroll
      for (int g = 0; g < 4; ++g) {
        const f32x4 w1 = *(LAS const f32x4*)(smem + K1_W1 + (32 * cg + 8 * g + 4 * h) * 4) + *(LAS const f32x4*)(smem + K1_W1 + (128 + 32 * cg + 8 * g + 4 * h) * 4);
#pragma unroll
        for (int p = 0; p < 2; ++p) {
          const f32x2 a2 = {acc2[4 * g + 2 * p], acc2[4 * g + 2 * p + 1]}, w2 = {w1[2 * p], w1[2 * p + 1]};
          const unsigned zz = rn_pair(gelu2(up_bf(rn_pair((a2 - w2 * m1) * r1))));
          zp[2 * g + p] = zz;
          ss = dot2bf(zz, ONES_BF, ss); sq = dot2bf(zz, zz, sq);
        }
      }
      ss = xsum32(ss); sq = xsum32(sq);
      if (h == 0) *(LAS f32x2*)(smem + K1_P + ((32 * rh + l31) * 4 + cg) * 8) = (f32x2){ss, sq};
      BAR();
      if (wave == 0) {
        const f32x4 p0 = *(LAS const f32x4*)(smem + K1_P + lane * 32), p1 = *(LAS const f32x4*)(smem + K1_P + lane * 32 + 16);
        const float s = (p0[0] + p0[2]) + (p1[0] + p1[2]), q = (p0[1] + p0[3]) + (p1[1] + p1[3]);
        u32x4 gr = {__float_as_uint(s), tag, __float_as_uint(q), tag};
        const char* xp = ws + WS_XCH + (size_t)ti * 64 * 128 + tj * 16;
        const unsigned vo = lane * 128;
        asm volatile("global_store_dwordx4 %0, %1, %2 sc1\n\ts_nop 1" ::"v"(vo), "v"(gr), "s"(xp) : "memory");
      }
      STAMP(0, 4);
    }
  static_for<28, 56>(step);
#undef K1_FRAGS
#undef K1_XPIECE
#undef K1_WPIECE
  STAMP(0, 5);
  {
    const float w1s = xsum32(w1b);
    if (h == 0) *(LAS float*)(smem + K1_W1 + 1024 + (rh * 128 + 32 * cg + l31) * 4) = w1s;
  }
  BAR();
  u32x4 gr;
  const int row = tid >> 3, jj = tid & 7;
  const char* xp = ws + WS_XCH + (size_t)ti * 64 * 128;
  const unsigned vo = row * 128 + (jj < 7 ? jj : 0) * 16;
  asm volatile("global_load_dwordx4 %0, %1, %2 sc1" : "=v"(gr) : "v"(vo), "s"(xp) : "memory");
  {
    const int tt = 4 * ti + 2 * rh + (l31 >> 4);
#pragma unroll
    for (int g = 0; g < 4; ++g) {
      const f32x4 w1 = *(LAS const f32x4*)(smem + K1_W1 + 1024 + (32 * cg + 8 * g + 4 * h) * 4) + *(LAS const f32x4*)(smem + K1_W1 + 1024 + (128 + 32 * cg + 8 * g + 4 * h) * 4);
      unsigned o[2];
#pragma unroll
      for (int p = 0; p < 2; ++p) {
        const f32x2 a2 = {acc1[4 * g + 2 * p], acc1[4 * g + 2 * p + 1]}, w2 = {w1[2 * p], w1[2 * p + 1]};
        o[p] = rn_pair(gelu2(up_bf(rn_pair((a2 - w2 * m1) * r1))));
      }
      const int ft = 8 * tj + 2 * cg + (g >> 1), lp = (lane & 15) + 16 * (2 * (g & 1) + h);
      *(u32x2*)(ws + WS_Z1D + ((size_t)(tt * 56 + ft) * 64 + lp) * 8) = (u32x2){o[0], o[1]};
    }
  }
  STAMP(0, 6);
  asm volatile("s_waitcnt vmcnt(4)" : "+v"(gr)::"memory");
  poll_granule(gr, xp, vo, tag);
  {
    const float s = red8(jj < 7 ? __uint_as_float(gr[0]) : 0.f), q = red8(jj < 7 ? __uint_as_float(gr[2]) : 0.f);
    float mb, rr2;
    ln_scalars(s, q, 1.0f / 896.0f, mb, rr2);
    if (jj == 0) { *(LAS float*)(smem + K1_M2 + row * 4) = mb; *(LAS float*)(smem + K1_R2 + row * 4) = rr2; }
  }
  BAR();
  STAMP(0, 7);
  if (tj == 0 && tid == 0) __hip_atomic_store((GAS unsigned*)(ws + WS_EP + ti * 128), tag, __ATOMIC_RELAXED, __HIP_MEMORY_SCOPE_AGENT);
  {
    LAS char* tb = smem + K1_W + wave * 2304;
    const int srow = 32 * rh + l31;
    const float mb2 = *(LAS const float*)(smem + K1_M2 + srow * 4), rr2 = *(LAS const float*)(smem + K1_R2 + srow * 4);
#pragma unroll
    for (int g = 0; g < 4; ++g) {
      unsigned o[2];
#pragma unroll
      for (int p = 0; p < 2; ++p) o[p] = rn_pair(up_bf(rn_pair(up_bf(zp[2 * g + p]) - mb2)) * rr2);
      *(LAS u32x2*)(tb + l31 * 72 + (8 * g + 4 * h) * 2) = (u32x2){o[0], o[1]};
    }
    asm volatile("s_waitcnt lgkmcnt(0)" ::: "memory");
    const int q4 = lane >> 4, l15 = lane & 15;
#pragma unroll
    for (int n = 0; n < 2; ++n) {
      LAS const char* rp = tb + (8 * q4 + (l15 >> 2)) * 72 + n * 32 + (lane & 3) * 8;
      const s16x4 lo = tr_read(rp), hi = tr_read(rp + 4 * 72);
      const s16x8 o = __builtin_shufflevector(lo, hi, 0, 1, 2, 3, 4, 5, 6, 7);
      const int ft = 8 * tj + 2 * cg + n, sb = 2 * ti + rh;
      *(s16x8*)(ws + WS_Z2F + ((size_t)(ft * 64 + sb) * 64 + lane) * 16) = o;
    }
  }
  STAMP(0, 8);
}

constexpr int K2_RING = 0;
constexpr int K2_CH = 65536;
constexpr int K2_CHS = 49152;
constexpr int CH_FT = 0;
constexpr int CH_FW = 16416, CH_FS = 288;
constexpr int CH_CS = 4160;
constexpr int CH_U = 33280;
constexpr int CH_X = CH_U + 4608;
constexpr int CH_T = CH_X + 4224;
constexpr int CH_CNT = CH_T + 6144;
constexpr int K2_LDS = 163840;

__device__ __forceinline__ void lds_sync(LAS char* cntp, unsigned target, int lane) {
  LAS unsigned* cnt = (LAS unsigned*)cntp;
  asm volatile("s_waitcnt lgkmcnt(0)" ::: "memory");
  if (lane == 0) __hip_atomic_fetch_add(cnt, 1u, __ATOMIC_RELAXED, __HIP_MEMORY_SCOPE_WORKGROUP);
  while ((unsigned)__builtin_amdgcn_readfirstlane(*(volatile LAS unsigned*)cnt) < target) __builtin_amdgcn_s_sleep(1);
  asm volatile("" ::: "memory");
}
__device__ __forceinline__ __amdgpu_buffer_rsrc_t mk_rsrc(const void* p, unsigned bytes) {
  return __builtin_amdgcn_make_buffer_rsrc((void*)p, (short)0, (int)bytes, 0x00020000);
}
__device__ __forceinline__ void bglds16(__amdgpu_buffer_rsrc_t rs, unsigned voff, unsigned soff, unsigned lds) {
  asm volatile("s_mov_b32 m0, %3\n\ts_nop 0\n\tbuffer_load_dwordx4 %0, %1, %2 offen lds" ::"v"(voff), "s"(rs), "s"(soff), "s"(lds) : "memory");
}
__device__ __forceinline__ void bload16(u32x4& dst, __amdgpu_buffer_rsrc_t rs, unsigned voff, unsigned soff) {
  asm volatile("buffer_load_dwordx4 %0, %1, %2, %3 offen" : "=v"(dst) : "v"(voff), "s"(rs), "s"(soff) : "memory");
}

template <int W>
__device__ __forceinline__ void k2_epi(LAS char* smem, char* __restrict__ ws, f32x4 (&acc)[7][4], const u32x2 (&z1v)[7], float bs, int lane, int tt, int J) {
  LAS char* cnt = smem + K2_CH + CH_CNT + 64;
  LAS char* mine = smem + K2_RING + W * 16384 + lane * 16;
  LAS char* other = smem + K2_RING + (W ^ 2) * 16384 + lane * 16;
  LAS char* o2 = smem + K2_RING + (W ^ 3) * 16384 + lane * 16;
  constexpr int K0 = W & 2, S0 = K0 ^ 2;
#pragma unroll
  for (int n = 0; n < 7; ++n) { *(LAS f32x4*)(mine + n * 1024) = acc[n][S0]; *(LAS f32x4*)(mine + (7 + n) * 1024) = acc[n][S0 + 1]; }
  lds_sync(cnt, 4, lane);
#pragma unroll
  for (int n = 0; n < 7; ++n) { acc[n][K0] += *(LAS const f32x4*)(other + n * 1024); acc[n][K0 + 1] += *(LAS const f32x4*)(other + (7 + n) * 1024); }
  asm volatile("s_waitcnt lgkmcnt(0)" ::: "memory");
#pragma unroll
  for (int n = 0; n < 7; ++n) *(LAS f32x4*)(other + n * 1024) = acc[n][W ^ 1];
  lds_sync(cnt, 8, lane);
  STAMP(1, 2);
  unsigned hp[7][2];
#pragma unroll
  for (int n = 0; n < 7; ++n) {
    const f32x4 v = acc[n][W] + *(LAS const f32x4*)(o2 + n * 1024);
#pragma unroll
    for (int p = 0; p < 2; ++p) {
      const f32x2 g0 = up_bf(pk_bf16(v[2 * p], v[2 * p + 1])) + bs;
      const unsigned zw = z1v[n][p];
      const f32x2 h = up_bf(pk_bf16(g0.x, g0.y)) * up_bf(zw);
      hp[n][p] = pk_bf16(h.x, h.y);
    }
  }
  char* hb = ws + WS_HF + ((size_t)(tt * 28 + (7 * J >> 1)) * 64 + lane) * 16;
  if (!(J & 1)) {
#pragma unroll
    for (int p = 0; p < 3; ++p) *(u32x4*)(hb + p * 1024) = (u32x4){hp[2 * p][0], hp[2 * p][1], hp[2 * p + 1][0], hp[2 * p + 1][1]};
    *(u32x2*)(hb + 3 * 1024) = (u32x2){hp[6][0], hp[6][1]};
  } else {
    *(u32x2*)(hb + 8) = (u32x2){hp[0][0], hp[0][1]};
#pragma unroll
    for (int p = 0; p < 3; ++p) *(u32x4*)(hb + (p + 1) * 1024) = (u32x4){hp[2 * p + 1][0], hp[2 * p + 1][1], hp[2 * p + 2][0], hp[2 * p + 2][1]};
  }
}

__device__ __forceinline__ void k2_gemm(LAS char* smem, const unsigned short* __restrict__ w_sp, const unsigned short* __restrict__ b_sp, char* __restrict__ ws,
                                        int wave, int lane, int T, int J, int rot) {
  const __amdgpu_buffer_rsrc_t zs = mk_rsrc(ws + WS_Z2F + (size_t)(7 * J * 64 + 16 * wave) * 1024, 7 * 65536 - 16 * wave * 1024);
  const __amdgpu_buffer_rsrc_t wr = mk_rsrc((const char*)w_sp + (size_t)T * 64 * 4096 + wave * 1024, 64 * 4096 - wave * 1024);
  const unsigned voffz = lane * 16;
  const unsigned voffw0 = (lane >> 3) * 4096 + (((lane & 7) ^ ((lane >> 4) & 3)) * 16);
  const unsigned voffw1 = (8 + (lane >> 3)) * 4096 + (((lane & 7) ^ (4 + ((lane >> 4) & 3))) * 16);
  const unsigned ring = K2_RING + wave * 16384;
  LAS const char* wrd0 = smem + ring + (lane & 15) * 128 + (((lane >> 4) ^ ((lane >> 1) & 7)) * 16);
  LAS const char* wrd1 = smem + ring + (lane & 15) * 128 + (((4 + (lane >> 4)) ^ ((lane >> 1) & 7)) * 16);
  u32x4 z[3][7];
  f32x4 acc[7][4];
#pragma unroll
  for (int n = 0; n < 7; ++n)
#pragma unroll
    for (int m = 0; m < 4; ++m) acc[n][m] = (f32x4){0.f, 0.f, 0.f, 0.f};
#define K2_ISSUE_W(pp)                                                                                      \
  do {                                                                                                      \
    const unsigned kw_ = (unsigned)((2 * (pp) + rot) & 15) * 64;                                            \
    _Pragma("unroll") for (int m = 0; m < 4; ++m) {                                                         \
      bglds16(wr, voffw0, m * 65536 + kw_, ring + ((pp) & 1) * 8192 + m * 2048);                            \
      bglds16(wr, voffw1, m * 65536 + kw_, ring + ((pp) & 1) * 8192 + m * 2048 + 1024);                     \
    }                                                                                                       \
  } while (0)
#define K2_ISSUE_Z(it)                                                                                      \
  do {                                                                                                      \
    const unsigned kz_ = (unsigned)(((it) + rot) & 15) * 1024;                                              \
    _Pragma("unroll") for (int n = 0; n < 7; ++n) bload16(z[(it) % 3][n], zs, voffz, n * 65536 + kz_);      \
  } while (0)
  STAMP(1, 0);
  K2_ISSUE_W(0); K2_ISSUE_Z(0); K2_ISSUE_Z(1); K2_ISSUE_W(1); K2_ISSUE_Z(2);
  const int tt = 4 * T + wave;
  u32x2 z1v[7];
  unsigned bsw;
#define K2_WAITZ(N) asm volatile("s_waitcnt vmcnt(" #N ")" : "+v"(zc[0]), "+v"(zc[1]), "+v"(zc[2]), "+v"(zc[3]), "+v"(zc[4]), "+v"(zc[5]), "+v"(zc[6])::"memory")
#pragma unroll
  for (int it = 0; it < 16; ++it) {
    u32x4(&zc)[7] = z[it % 3];
    if (it <= 13) K2_WAITZ(22); else if (it == 14) K2_WAITZ(15); else K2_WAITZ(8);
    s16x8 wf[4];
#pragma unroll
    for (int m = 0; m < 4; ++m) wf[m] = *(LAS const s16x8*)(((it & 1) ? wrd1 : wrd0) + ((it >> 1) & 1) * 8192 + m * 2048);
    const unsigned kz = (unsigned)((it + 3 + rot) & 15) * 1024, kw = (unsigned)((it + 3 + rot) & 15) * 64;
#pragma unroll
    for (int n = 0; n < 7; ++n) {
#pragma unroll
      for (int m = 0; m < 4; ++m) acc[n][m] = mfma_bf(__builtin_bit_cast(s16x8, zc[n]), wf[m], acc[n][m]);
      __builtin_amdgcn_sched_barrier(0);
      if (it + 3 < 16) {
        bload16(zc[n], zs, voffz, n * 65536 + kz);
        if ((it & 1) && n >= 1 && n <= 4) {
          const int m = n - 1;
          bglds16(wr, voffw0, m * 65536 + kw, ring + (((it + 3) >> 1) & 1) * 8192 + m * 2048);
          bglds16(wr, voffw1, m * 65536 + kw, ring + (((it + 3) >> 1) & 1) * 8192 + m * 2048 + 1024);
        }
      }
      if (it == 13) {
        const char* zb = ws + WS_Z1D + (size_t)(tt * 56 + 7 * J + n) * 512;
        const unsigned vo = lane * 8;
        asm volatile("global_load_dwordx2 %0, %1, %2" : "=v"(z1v[n]) : "v"(vo), "s"(zb) : "memory");
        if (n == 6) {
          const unsigned vb = (unsigned)(16 * tt + (lane & 15)) * 2u;
          asm volatile("global_load_ushort %0, %1, %2" : "=v"(bsw) : "v"(vb), "s"(b_sp) : "memory");
        }
      }
      __builtin_amdgcn_sched_barrier(0);
    }
  }
#undef K2_WAITZ
#undef K2_ISSUE_W
#undef K2_ISSUE_Z
  STAMP(1, 1);
  asm volatile("s_waitcnt vmcnt(0)" : "+v"(z1v[0]), "+v"(z1v[1]), "+v"(z1v[2]), "+v"(z1v[3]), "+v"(z1v[4]), "+v"(z1v[5]), "+v"(z1v[6]), "+v"(bsw)::"memory");
  const float bs = bf2f((unsigned short)bsw);
  if (wave == 0) k2_epi<0>(smem, ws, acc, z1v, bs, lane, tt, J);
  else if (wave == 1) k2_epi<1>(smem, ws, acc, z1v, bs, lane, tt, J);
  else if (wave == 2) k2_epi<2>(smem, ws, acc, z1v, bs, lane, tt, J);
  else k2_epi<3>(smem, ws, acc, z1v, bs, lane, tt, J);
  STAMP(1, 3);
}

__device__ __forceinline__ f32x2 cmul(f32x2 a, f32x2 b) {
  f32x2 t, r;
  asm("v_pk_mul_f32 %0, %1, %2 op_sel:[0,0] op_sel_hi:[0,1]" : "=v"(t) : "v"(a), "v"(b));
  asm("v_pk_fma_f32 %0, %1, %2, %3 op_sel:[1,1,0] op_sel_hi:[1,0,1] neg_lo:[1,0,0]" : "=v"(r) : "v"(a), "v"(b), "v"(t));
  return r;
}
__device__ __forceinline__ unsigned hmul(unsigned a, unsigned b) {
  unsigned t, r;
  asm("v_pk_mul_f16 %0, %1, %2 op_sel:[0,0] op_sel_hi:[0,1]" : "=v"(t) : "v"(a), "v"(b));
  asm("v_pk_fma_f16 %0, %1, %2, %3 op_sel:[1,1,0] op_sel_hi:[1,0,1] neg_lo:[1,0,0]" : "=v"(r) : "v"(a), "v"(b), "v"(t));
  return r;
}
__device__ __forceinline__ unsigned hcv(f32x2 v) { return pk_f16(v.x, v.y); }
__device__ __forceinline__ f32x2 cexp_p(float al, float be, float p) {
  const float e = __builtin_amdgcn_exp2f(al * p), th = __builtin_amdgcn_fractf(be * p);
  return (f32x2){__builtin_amdgcn_cosf(th), __builtin_amdgcn_sinf(th)} * e;
}
__device__ __forceinline__ f32x4 mfma_h(u32x4 a, u32x4 b, f32x4 c) {
  return __builtin_amdgcn_mfma_f32_16x16x32_f16(__builtin_bit_cast(f16x8, a), __builtin_bit_cast(f16x8, b), c, 0, 0, 0);
}
struct PosTab { unsigned short v[2048]; };
constexpr int pos_bf16(int j) {
  if (j < 256) return j;
  int sh = 1; while ((j >> sh) >= 256) ++sh;
  const int q = j >> sh, r = j & ((1 << sh) - 1), h = 1 << (sh - 1);
  return (r > h || (r == h && (q & 1)) ? q + 1 : q) << sh;
}
constexpr PosTab make_postab() {
  PosTab t = {};
  for (int x = 0; x < 2048; ++x) {
    const int p = pos_bf16(2047 - x);
    int col = 0, a = 0;
    if (p < 256) { a = p >> 4; col = p & 15; }
    else if (p < 512) { const int r = p - 256; a = r >> 4; col = 16 + ((r & 15) >> 1); }
    else if (p < 1024) { const int r = p - 512; a = (r & 255) >> 4; col = 24 + 4 * (r >> 8) + ((r & 15) >> 2); }
    else if (p < 2048) { const int r = p - 1024; a = (r & 255) >> 4; col = 32 + 2 * (r >> 8) + ((r & 15) >> 3); }
    else { a = 0; col = 40; }
    t.v[x] = (unsigned short)((col * 16 + a) * 4);
  }
  return t;
}
__device__ const PosTab POSTAB = make_postab();

template <int HW>
__device__ __forceinline__ void s4d_conv(LAS char* cb, char* __restrict__ ws, int d, int lane, float iscale, float dsk) {
  constexpr int NF = HW ? 7 : 8;
  f32x4 acc[8] = {};
  const int r = lane & 15, h = lane >> 4;
  LAS const char* ab = cb + CH_FT + ((7 - r) & 7) * CH_CS + (255 - (r >> 3) - 2 * (h >> 1) + (h & 1)) * 16 - (64 * 7 + 512 * 7);
  LAS const char* bb = cb + CH_U + 512 + 32 * r - 32 * (h >> 1) + 16 * (h & 1) - 64 * 7;
  u32x4 A[2][8], B[2][8];
#define CONV_LOAD(ar, buf)                                                                                   \
  _Pragma("unroll") for (int k = 0; k < NF; ++k) {                                                           \
    B[buf][k] = *(LAS const u32x4*)(bb + 512 * k + 64 * (7 - (ar)));                                         \
    A[buf][k] = *(LAS const u32x4*)(ab + 512 * (7 - k) + 64 * (7 - (ar)));                                   \
  }
  CONV_LOAD(0, 0);
#pragma unroll
  for (int ar = 0; ar < 8; ++ar) {
    __builtin_amdgcn_sched_barrier(0);
    if (ar < 7) { CONV_LOAD(ar + 1, (ar + 1) & 1); }
#pragma unroll
    for (int al = 0; al < NF; ++al)
#pragma unroll
      for (int be = 0; be < NF; ++be) {
        const int tau = al + be;
        if (HW ? (tau >= 4 && tau <= 6) : (tau <= 3 || tau == 7)) acc[tau] = mfma_h(A[ar & 1][al], B[ar & 1][be], acc[tau]);
      }
#pragma unroll
    for (int i = 0; i < 16; ++i) { __builtin_amdgcn_sched_group_barrier(0x008, 1, 0); __builtin_amdgcn_sched_group_barrier(0x100, 1, 0); }
    __builtin_amdgcn_sched_barrier(0);
  }
#undef CONV_LOAD
  STAMP(1, 4);
  const int q = lane >> 4;
#pragma unroll
  for (int t = 0; t < 8; ++t) {
    if (HW ? (t >= 4 && t <= 6) : (t <= 3 || t == 7)) {
      const f32x4 v = acc[t];
      const int l0 = 256 * t + 16 * r + 4 * q;
      const u32x2 uw = *(LAS const u32x2*)(cb + CH_U + 512 + l0 * 2);
      const unsigned uw0 = uw[0], uw1 = uw[1];
      const f16x2 ua = __builtin_bit_cast(f16x2, uw0), ub = __builtin_bit_cast(f16x2, uw1);
      const float uu[4] = {(float)ua[0], (float)ua[1], (float)ub[0], (float)ub[1]};
      float sv[4];
#pragma unroll
      for (int j = 0; j < 4; ++j) sv[j] = rbf(v[j] * iscale) + rbf(uu[j] * dsk);
      *(u32x2*)(ws + WS_ST + ((size_t)d * SEQ + l0) * 2) = (u32x2){pk_bf16(sv[0], sv[1]), pk_bf16(sv[2], sv[3])};
    }
  }
}

__device__ __forceinline__ void k2_s4d(LAS char* cb, const unsigned short* __restrict__ logA, const unsigned short* __restrict__ Aim, const unsigned short* __restrict__ Cre,
                                       const unsigned short* __restrict__ Cim, const unsigned short* __restrict__ logdt, const unsigned short* __restrict__ Dsk,
                                       char* __restrict__ ws, int d, int hw, int lane) {
  LAS char* cnt = cb + CH_CNT;
  unsigned la_[2], ai_[2], cr_[4], ci_[4], ldt_, dsk_;
  u32x4 u0, u1, pt0, pt1;
  {
    const unsigned vo = (unsigned)(d * NST + 128 * hw + lane) * 2u, vo2 = (unsigned)(d * NST + ((128 * hw + 128 + lane) & 255)) * 2u, vd = (unsigned)d * 2u;
    asm volatile("global_load_ushort %0, %1, %2" : "=v"(la_[0]) : "v"(vo), "s"(logA) : "memory");
    asm volatile("global_load_ushort %0, %1, %2 offset:128" : "=v"(la_[1]) : "v"(vo), "s"(logA) : "memory");
    asm volatile("global_load_ushort %0, %1, %2" : "=v"(ai_[0]) : "v"(vo), "s"(Aim) : "memory");
    asm volatile("global_load_ushort %0, %1, %2 offset:128" : "=v"(ai_[1]) : "v"(vo), "s"(Aim) : "memory");
    asm volatile("global_load_ushort %0, %1, %2" : "=v"(cr_[0]) : "v"(vo), "s"(Cre) : "memory");
    asm volatile("global_load_ushort %0, %1, %2 offset:128" : "=v"(cr_[1]) : "v"(vo), "s"(Cre) : "memory");
    asm volatile("global_load_ushort %0, %1, %2" : "=v"(cr_[2]) : "v"(vo2), "s"(Cre) : "memory");
    asm volatile("global_load_ushort %0, %1, %2 offset:128" : "=v"(cr_[3]) : "v"(vo2), "s"(Cre) : "memory");
    asm volatile("global_load_ushort %0, %1, %2" : "=v"(ci_[0]) : "v"(vo), "s"(Cim) : "memory");
    asm volatile("global_load_ushort %0, %1, %2 offset:128" : "=v"(ci_[1]) : "v"(vo), "s"(Cim) : "memory");
    asm volatile("global_load_ushort %0, %1, %2" : "=v"(ci_[2]) : "v"(vo2), "s"(Cim) : "memory");
    asm volatile("global_load_ushort %0, %1, %2 offset:128" : "=v"(ci_[3]) : "v"(vo2), "s"(Cim) : "memory");
    asm volatile("global_load_ushort %0, %1, %2" : "=v"(ldt_) : "v"(vd), "s"(logdt) : "memory");
    asm volatile("global_load_ushort %0, %1, %2" : "=v"(dsk_) : "v"(vd), "s"(Dsk) : "memory");
    const char* ub = ws + WS_XT + (size_t)d * SEQ * 2 + hw * 2048;
    const unsigned vu = lane * 16;
    asm volatile("global_load_dwordx4 %0, %1, %2" : "=v"(u0) : "v"(vu), "s"(ub) : "memory");
    asm volatile("global_load_dwordx4 %0, %1, %2 offset:1024" : "=v"(u1) : "v"(vu), "s"(ub) : "memory");
    const char* pb = (const char*)POSTAB.v + hw * 2048;
    const unsigned vp = lane * 32;
    asm volatile("global_load_dwordx4 %0, %1, %2" : "=v"(pt0) : "v"(vp), "s"(pb) : "memory");
    asm volatile("global_load_dwordx4 %0, %1, %2 offset:16" : "=v"(pt1) : "v"(vp), "s"(pb) : "memory");
    if (hw == 0 && lane < 32) *(LAS u32x4*)(cb + CH_U + lane * 16) = (u32x4){0u, 0u, 0u, 0u};
    if (hw == 1 && lane < 4) *(LAS u32x4*)(cb + CH_X + 4096 + lane * 16) = (u32x4){0u, 0u, 0u, 0u};
  }
  BAR();
#define K2_PRIO 3
  __builtin_amdgcn_s_setprio(K2_PRIO);
  STAMP(1, 0);
  asm volatile("s_waitcnt vmcnt(0)" : "+v"(la_[0]), "+v"(la_[1]), "+v"(ai_[0]), "+v"(ai_[1]), "+v"(cr_[0]), "+v"(cr_[1]), "+v"(cr_[2]), "+v"(cr_[3]), "+v"(ci_[0]), "+v"(ci_[1]),
               "+v"(ci_[2]), "+v"(ci_[3]), "+v"(ldt_), "+v"(dsk_), "+v"(u0), "+v"(u1), "+v"(pt0), "+v"(pt1)::"memory");
  float cr[4], ci[4];
#pragma unroll
  for (int p = 0; p < 4; ++p) { cr[p] = bf2f((unsigned short)cr_[p]); ci[p] = bf2f((unsigned short)ci_[p]); }
  const float dt = rbf(expf(bf2f((unsigned short)ldt_))), dsk = bf2f((unsigned short)dsk_);
  *(LAS u32x4*)(cb + CH_U + 512 + hw * 2048 + lane * 16) = u0;
  *(LAS u32x4*)(cb + CH_U + 512 + hw * 2048 + 1024 + lane * 16) = u1;
  float sabs = 0.f;
#pragma unroll
  for (int p = 0; p < 4; ++p) sabs += __builtin_fabsf(cr[p]) + __builtin_fabsf(ci[p]);
  sabs = xsum16_32(red16(sabs));
  sabs *= 2.0f * dt;
  int ex = (int)((__float_as_uint(sabs) >> 23) & 0xff) - 126;
  ex = ex < -100 ? -100 : (ex > 100 ? 100 : ex);
  const float scale = __uint_as_float((unsigned)(127 + 15 - ex) << 23), iscale = __uint_as_float((unsigned)(127 - 15 + ex) << 23);
  STAMP(1, 1);
  f32x4 kacc[3] = {};
  LAS char* fw = cb + CH_FT + hw * CH_FW;
#pragma unroll 1
  for (int p = 0; p < 2; ++p) {
    const float lap = bf2f((unsigned short)(p ? la_[1] : la_[0])), aip = bf2f((unsigned short)(p ? ai_[1] : ai_[0]));
    const float crp = p ? cr[1] : cr[0], cip = p ? ci[1] : ci[0];
    const float lre = -rbf(expf(lap)) * dt, lim = aip * dt;
    const float al = lre * 1.4426950408889634f, be = lim * 0.15915494309189535f;
    LAS char* wp = fw + lane * 4;
#define PUTH(row, v) *(LAS unsigned*)(wp + (row) * CH_FS) = (v)
    {
      const f32x2 w = cexp_p(al, be, 16.f);
      const float cs = 2.0f * dt * scale;
      const f32x2 c0 = {crp * cs, cip * cs};
      const f32x2 w2 = cmul(w, w), c1 = cmul(c0, w), w4 = cmul(w2, w2), c2 = cmul(c0, w2), c3 = cmul(c1, w2), w8 = cmul(w4, w4), w12 = cmul(w8, w4);
      const unsigned hc[4] = {hcv(c0), hcv(c1), hcv(c2), hcv(c3)}, hw4[3] = {hcv(w4), hcv(w8), hcv(w12)};
#pragma unroll
      for (int j = 0; j < 4; ++j) {
        PUTH(j, hc[j]);
#pragma unroll
        for (int k = 0; k < 3; ++k) PUTH(4 * (k + 1) + j, hmul(hc[j], hw4[k]));
      }
    }
    {
      const f32x2 e1 = cexp_p(al, -be, 1.f);
      const f32x2 e2 = cmul(e1, e1), e3 = cmul(e2, e1), e4 = cmul(e2, e2), e8 = cmul(e4, e4), e12 = cmul(e8, e4);
      unsigned E[16];
      E[0] = 0x00003c00u; E[1] = hcv(e1); E[2] = hcv(e2); E[3] = hcv(e3); E[4] = hcv(e4); E[8] = hcv(e8); E[12] = hcv(e12);
#pragma unroll
      for (int m = 1; m < 4; ++m)
#pragma unroll
        for (int j = 1; j < 4; ++j) E[4 * m + j] = hmul(E[4 * m], E[j]);
#pragma unroll
      for (int k = 0; k < 16; ++k) PUTH(16 + k, E[k]);
      const f32x2 z256 = cexp_p(al, -be, 256.f), z512 = cexp_p(al, -be, 512.f), z1024 = cexp_p(al, -be, 1024.f), z2048 = cexp_p(al, -be, 2048.f);
      const f32x2 z768 = cmul(z512, z256);
      const unsigned h256 = hcv(z256), h512 = hcv(z512), h768 = hcv(z768);
      PUTH(32, h256);
#pragma unroll
      for (int k = 1; k < 8; ++k) PUTH(32 + k, hmul(h256, E[2 * k]));
      PUTH(40, h512); PUTH(44, h768);
#pragma unroll
      for (int k = 1; k < 4; ++k) { PUTH(40 + k, hmul(h512, E[4 * k])); PUTH(44 + k, hmul(h768, E[4 * k])); }
      const unsigned hb[4] = {hcv(z1024), hcv(cmul(z1024, z256)), hcv(cmul(z1024, z512)), hcv(cmul(z1024, z768))};
#pragma unroll
      for (int k = 0; k < 4; ++k) { PUTH(48 + 2 * k, hb[k]); PUTH(49 + 2 * k, hmul(hb[k], E[8])); }
      PUTH(56, hcv(z2048));
    }
#undef PUTH
    asm volatile("s_waitcnt lgkmcnt(0)" ::: "memory");
    LAS const char* rp = fw + (lane & 15) * CH_FS + (lane >> 4) * 16;
    u32x4 fa[4], fb[4][3];
#pragma unroll
    for (int kk = 0; kk < 4; ++kk) {
      fa[kk] = *(LAS const u32x4*)(rp + kk * 64);
#pragma unroll
      for (int t = 0; t < 3; ++t) fb[kk][t] = *(LAS const u32x4*)(rp + (16 + 16 * t) * CH_FS + kk * 64);
    }
#pragma unroll
    for (int kk = 0; kk < 4; ++kk)
#pragma unroll
      for (int t = 0; t < 3; ++t) kacc[t] = mfma_h(fa[kk], fb[kk][t], kacc[t]);
    asm volatile("" ::: "memory");
  }
  STAMP(1, 2);
#pragma unroll
  for (int t = 0; t < 3; ++t) *(LAS f32x4*)(cb + CH_T + hw * 3072 + (16 * t + (lane & 15)) * 64 + (lane >> 4) * 16) = kacc[t];
  lds_sync(cnt, 2, lane);
  {
    const int x0 = 1024 * hw + 16 * lane;
    const unsigned po[8] = {pt0[0], pt0[1], pt0[2], pt0[3], pt1[0], pt1[1], pt1[2], pt1[3]};
    unsigned w[8];
#pragma unroll
    for (int k = 0; k < 8; ++k) {
      LAS const char* a0 = cb + CH_T + (po[k] & 0xffffu);
      LAS const char* a1 = cb + CH_T + (po[k] >> 16);
      w[k] = pk_f16(*(LAS const float*)a0 + *(LAS const float*)(a0 + 3072), *(LAS const float*)a1 + *(LAS const float*)(a1 + 3072));
    }
    *(LAS u32x4*)(cb + CH_X + x0 * 2) = (u32x4){w[0], w[1], w[2], w[3]};
    *(LAS u32x4*)(cb + CH_X + x0 * 2 + 16) = (u32x4){w[4], w[5], w[6], w[7]};
  }
  lds_sync(cnt, 4, lane);
#pragma unroll
  for (int it = 0; it < 3; ++it) {
    const int y = hw * 64 + lane + 128 * it;
    if (it < 2 || y <= 256) {
      const u32x4 r0 = *(LAS const u32x4*)(cb + CH_X + y * 16), r1 = *(LAS const u32x4*)(cb + CH_X + y * 16 + 16);
      unsigned W[8] = {r0[0], r0[1], r0[2], r0[3], r1[0], r1[1], r1[2], r1[3]}, V[7];
#pragma unroll
      for (int i = 0; i < 7; ++i) V[i] = __builtin_amdgcn_alignbit(W[i + 1], W[i], 16);
#pragma unroll
      for (int m = 0; m < 4; ++m) {
        *(LAS u32x4*)(cb + CH_FT + (2 * m) * CH_CS + y * 16) = (u32x4){W[m], W[m + 1], W[m + 2], W[m + 3]};
        *(LAS u32x4*)(cb + CH_FT + (2 * m + 1) * CH_CS + y * 16) = (u32x4){V[m], V[m + 1], V[m + 2], V[m + 3]};
      }
    }
  }
  lds_sync(cnt, 6, lane);
  STAMP(1, 3);
  if (hw) s4d_conv<1>(cb, ws, d, lane, iscale, dsk); else s4d_conv<0>(cb, ws, d, lane, iscale, dsk);
  STAMP(1, 5);
}

__global__ __launch_bounds__(512, 2) void k2_mix(const unsigned short* __restrict__ w_sp, const unsigned short* __restrict__ b_sp, const unsigned short* __restrict__ logA,
                                                 const unsigned short* __restrict__ Aim, const unsigned short* __restrict__ Cre, const unsigned short* __restrict__ Cim,
                                                 const unsigned short* __restrict__ logdt, const unsigned short* __restrict__ Dsk, const unsigned short* __restrict__ w_out,
                                                 char* __restrict__ ws) {
  extern __shared__ __attribute__((aligned(16))) char smem_raw[];
  LAS char* smem = (LAS char*)smem_raw;
  const int tid = threadIdx.x, lane = tid & 63;
  const int wave = __builtin_amdgcn_readfirstlane(tid >> 6);
  const int b = blockIdx.x, xcd = b & 7, rr = b >> 3;
  if (tid < 64) {
    if (lane < 32) *(LAS unsigned*)(smem + K2_CH + CH_CNT + lane * 4) = 0u;
    else *(LAS unsigned*)(smem + K2_CH + K2_CHS + CH_CNT + (lane - 32) * 4) = 0u;
  }
  if (wave < 4) {
    BAR();
    const int T = 8 * (xcd >> 1) + (rr & 7), J = 4 * (xcd & 1) + (rr >> 3);
#define K2_ROT 0
    k2_gemm(smem, w_sp, b_sp, ws, wave, lane, T, J, K2_ROT == 1 ? (2 * (rr & 7) + 4 * (rr >> 3)) & 15 : K2_ROT == 2 ? 2 * (rr & 1) : 0);
  } else {
    const int sw = wave - 4;
    if (b < 224) {
      k2_s4d(smem + K2_CH + (sw >> 1) * K2_CHS, logA, Aim, Cre, Cim, logdt, Dsk, ws, 2 * b + (sw >> 1), sw & 1, lane);
    } else {
      BAR();
      for (int fr = (b - 224) * 4 + sw; fr < 784; fr += 128) {
        const int dt = fr / 28, fb = fr - dt * 28;
        const int dcol = 16 * dt + (lane & 15), q = lane >> 4;
        unsigned short v[8];
#pragma unroll
        for (int jj = 0; jj < 8; ++jj) v[jj] = w_out[(size_t)(32 * fb + 16 * (jj >> 2) + 4 * q + (jj & 3)) * DM + dcol];
        u32x4 o = {(unsigned)v[0] | ((unsigned)v[1] << 16), (unsigned)v[2] | ((unsigned)v[3] << 16), (unsigned)v[4] | ((unsigned)v[5] << 16), (unsigned)v[6] | ((unsigned)v[7] << 16)};
        *(u32x4*)(ws + WS_WOF + ((size_t)fr * 64 + lane) * 16) = o;
      }
    }
  }
}

constexpr int K3_LDS = 49152 + 8448;
__global__ __launch_bounds__(256, 1) void k3_out(const unsigned short* __restrict__ x, char* __restrict__ ws, unsigned short* __restrict__ out) {
  extern __shared__ __attribute__((aligned(16))) char smem_raw[];
  LAS char* smem = (LAS char*)smem_raw;
  const int tid = threadIdx.x, lane = tid & 63;
  const int wave = __builtin_amdgcn_readfirstlane(tid >> 6);
  const int b = blockIdx.x, xcd = b & 7, rr = b >> 3;
  const int T = 4 * xcd + rr / 7, Dt = rr % 7;
  STAMP(2, 0);
  const u32x4* hp = (const u32x4*)(ws + WS_HF + ((size_t)(4 * T * 28 + 7 * wave) * 64 + lane) * 16);
  const u32x4* wp = (const u32x4*)(ws + WS_WOF + ((size_t)(4 * Dt * 28 + 7 * wave) * 64 + lane) * 16);
  u32x4 hf[7][4], wf[7][4];
#pragma unroll
  for (int k = 0; k < 7; ++k)
#pragma unroll
    for (int m = 0; m < 4; ++m) { hf[k][m] = hp[(m * 28 + k) * 64]; wf[k][m] = wp[(m * 28 + k) * 64]; }
  const int erow = tid >> 2, ec = tid & 3;
  const u32x4 st0 = *(const u32x4*)(ws + WS_ST + ((size_t)(64 * Dt + erow) * SEQ + 64 * T + 16 * ec) * 2);
  const u32x4 st1 = *(const u32x4*)(ws + WS_ST + ((size_t)(64 * Dt + erow) * SEQ + 64 * T + 16 * ec + 8) * 2);
  const u32x4 x0 = *(const u32x4*)(x + (size_t)(64 * T + erow) * DM + 64 * Dt + 16 * ec);
  const u32x4 x1 = *(const u32x4*)(x + (size_t)(64 * T + erow) * DM + 64 * Dt + 16 * ec + 8);
  __builtin_amdgcn_sched_barrier(0);
  f32x4 acc[4][4];
#pragma unroll
  for (int n = 0; n < 4; ++n)
#pragma unroll
    for (int m = 0; m < 4; ++m) acc[n][m] = (f32x4){0.f, 0.f, 0.f, 0.f};
#pragma unroll
  for (int k = 0; k < 7; ++k)
#pragma unroll
    for (int n = 0; n < 4; ++n)
#pragma unroll
      for (int m = 0; m < 4; ++m) acc[n][m] = mfma_bf(__builtin_bit_cast(s16x8, wf[k][n]), __builtin_bit_cast(s16x8, hf[k][m]), acc[n][m]);
  STAMP(2, 1);
  LAS char* sl = smem + 49152;
  {
    LAS unsigned* p = (LAS unsigned*)(sl + erow * 132 + ec * 32);
#pragma unroll
    for (int e = 0; e < 4; ++e) { p[e] = st0[e]; p[4 + e] = st1[e]; }
  }
#pragma unroll
  for (int m = 0; m < 4; ++m) {
    if (m != wave) {
      const int slot = m > wave ? m - 1 : m;
#pragma unroll
      for (int n = 0; n < 4; ++n) *(LAS f32x4*)(smem + ((wave * 3 + slot) * 4 + n) * 1024 + lane * 16) = acc[n][m];
    }
  }
  __syncthreads();
  f32x4 o[4];
  if (wave == 0) { for (int n = 0; n < 4; ++n) o[n] = acc[n][0]; }
  else if (wave == 1) { for (int n = 0; n < 4; ++n) o[n] = acc[n][1]; }
  else if (wave == 2) { for (int n = 0; n < 4; ++n) o[n] = acc[n][2]; }
  else { for (int n = 0; n < 4; ++n) o[n] = acc[n][3]; }
#pragma unroll
  for (int sw = 0; sw < 4; ++sw) {
    if (sw != wave) {
      const int slot = wave > sw ? wave - 1 : wave;
#pragma unroll
      for (int n = 0; n < 4; ++n) o[n] += *(LAS const f32x4*)(smem + ((sw * 3 + slot) * 4 + n) * 1024 + lane * 16);
    }
  }
  __syncthreads();
  {
    const int t = 16 * wave + (lane & 15), q = lane >> 4;
#pragma unroll
    for (int n = 0; n < 4; ++n) {
      f32x4 g;
#pragma unroll
      for (int j = 0; j < 4; ++j) {
        const float v = rbf(o[n][j]);
        g[j] = rbf(v * __builtin_amdgcn_rcpf(1.0f + __builtin_amdgcn_exp2f(-1.4426950408889634f * v)));
      }
      *(LAS f32x4*)(smem + t * 272 + (16 * n + 4 * q) * 4) = g;
    }
  }
  __syncthreads();
  {
    unsigned ow[8];
#pragma unroll
    for (int k = 0; k < 4; ++k) {
      const f32x4 g = *(LAS const f32x4*)(smem + erow * 272 + (16 * ec + 4 * k) * 4);
      float r[4];
#pragma unroll
      for (int j = 0; j < 4; ++j) {
        const int dl = 16 * ec + 4 * k + j;
        const float sv = bf2f(*(LAS const unsigned short*)(sl + dl * 132 + erow * 2));
        const unsigned xw = (k < 2 ? x0 : x1)[((4 * k + j) >> 1) & 3];
        const float xv = (j & 1) ? bfhi(xw) : bflo(xw);
        r[j] = xv + rbf(sv * g[j]);
      }
      ow[2 * k] = pk_bf16(r[0], r[1]); ow[2 * k + 1] = pk_bf16(r[2], r[3]);
    }
    u32x4* op = (u32x4*)(out + (size_t)(64 * T + erow) * DM + 64 * Dt + 16 * ec);
    op[0] = (u32x4){ow[0], ow[1], ow[2], ow[3]};
    op[1] = (u32x4){ow[4], ow[5], ow[6], ow[7]};
  }
  STAMP(2, 2);
}

extern "C" void kernel_launch(void* const* d_in, const int* in_sizes, int n_in, void* d_out, int out_size, void* d_ws, size_t ws_size, hipStream_t stream) {
  const unsigned short* x = (const unsigned short*)d_in[0];
  const unsigned short* logA = (const unsigned short*)d_in[1];
  const unsigned short* Aim = (const unsigned short*)d_in[2];
  const unsigned short* Cre = (const unsigned short*)d_in[3];
  const unsigned short* Cim = (const unsigned short*)d_in[4];
  const unsigned short* logdt = (const unsigned short*)d_in[5];
  const unsigned short* Dsk = (const unsigned short*)d_in[6];
  const unsigned short* w_in = (const unsigned short*)d_in[8];
  const unsigned short* w_sp = (const unsigned short*)d_in[9];
  const unsigned short* b_sp = (const unsigned short*)d_in[10];
  const unsigned short* w_out = (const unsigned short*)d_in[11];
  char* ws = (char*)d_ws;
  (void)hipFuncSetAttribute((const void*)k1_gemm1, hipFuncAttributeMaxDynamicSharedMemorySize, K1_LDS);
  (void)hipFuncSetAttribute((const void*)k2_mix, hipFuncAttributeMaxDynamicSharedMemorySize, K2_LDS);
  (void)hipFuncSetAttribute((const void*)k3_out, hipFuncAttributeMaxDynamicSharedMemorySize, K3_LDS);
  k1_gemm1<<<256, 512, K1_LDS, stream>>>(x, w_in, w_sp, ws);
  k2_mix<<<256, 512, K2_LDS, stream>>>(w_sp, b_sp, logA, Aim, Cre, Cim, logdt, Dsk, w_out, ws);
  k3_out<<<224, 256, K3_LDS, stream>>>(x, ws, (unsigned short*)d_out);
}
```

```cpp
#include <hip/hip_runtime.h>
#include <hip/hip_bf16.h>
#include <stdint.h>

#define LAS __attribute__((address_space(3)))
#define GAS __attribute__((address_space(1)))
typedef float f32x2 __attribute__((ext_vector_type(2)));
typedef float f32x4 __attribute__((ext_vector_type(4)));
typedef unsigned u32x2 __attribute__((ext_vector_type(2)));
typedef unsigned u32x4 __attribute__((ext_vector_type(4)));
typedef short s16x4 __attribute__((ext_vector_type(4)));
typedef short s16x8 __attribute__((ext_vector_type(8)));
typedef __bf16 bf16x2 __attribute__((ext_vector_type(2)));
typedef __bf16 bf16x8 __attribute__((ext_vector_type(8)));
typedef _Float16 f16x2 __attribute__((ext_vector_type(2)));
typedef _Float16 f16x8 __attribute__((ext_vector_type(8)));

constexpr int SEQ = 2048, DM = 448, FH = 896, FF = 1792, NST = 256;

constexpr size_t WS_Z1D = 0;
constexpr size_t WS_Z2F = WS_Z1D + (size_t)SEQ * FH * 2;
constexpr size_t WS_HF  = WS_Z2F + (size_t)SEQ * FH * 2;
constexpr size_t WS_XT  = WS_HF + (size_t)SEQ * FH * 2;
constexpr size_t WS_ST  = WS_XT + (size_t)SEQ * DM * 2;
constexpr size_t WS_WOF = WS_ST + (size_t)SEQ * DM * 2;
constexpr size_t WS_XCH = WS_WOF + (size_t)FH * DM * 2;
constexpr size_t WS_EP  = WS_XCH + 32 * 64 * 8 * 16;
constexpr size_t WS_XS1 = WS_EP + 32 * 128;
constexpr size_t WS_END = WS_XS1 + 32 * 1024;

constexpr size_t WS_DBG = 32u << 20;
#define STAMP(kern, slot) do {} while (0)
#define MARK_DECL do {} while (0)
#define MARK(i) do {} while (0)
#define MARK_DUMP(kern) do {} while (0)
__device__ __forceinline__ float bf2f(unsigned short h) { return __uint_as_float((unsigned)h << 16); }
__device__ __forceinline__ float bflo(unsigned w) { return __uint_as_float(w << 16); }
__device__ __forceinline__ float bfhi(unsigned w) { return __uint_as_float(w & 0xffff0000u); }
__device__ __forceinline__ unsigned pk_bf16(float lo, float hi) {
  bf16x2 v = {(__bf16)lo, (__bf16)hi};
  return __builtin_bit_cast(unsigned, v);
}
__device__ __forceinline__ float rbf(float x) { return (float)(__bf16)x; }
__device__ __forceinline__ unsigned pk_f16(float lo, float hi) {
  f16x2 v = {(_Float16)lo, (_Float16)hi};
  return __builtin_bit_cast(unsigned, v);
}
__device__ __forceinline__ float gelu_tanh(float x) {
  const float c1 = -2.0f * 0.7978845608028654f * 1.4426950408889634f, c2 = c1 * 0.044715f;
  float t = x * x;
  float a = x * __builtin_fmaf(t, c2, c1);
  float e = __builtin_amdgcn_exp2f(a);
  return x * __builtin_amdgcn_rcpf(1.0f + e);
}
template <int CTRL> __device__ __forceinline__ float dpp_add(float v) {
  int t = __builtin_amdgcn_update_dpp(0, __float_as_int(v), CTRL, 0xf, 0xf, true);
  return v + __int_as_float(t);
}
__device__ __forceinline__ float red16(float v) {
  v = dpp_add<0xB1>(v);
  v = dpp_add<0x4E>(v);
  v = dpp_add<0x141>(v);
  v = dpp_add<0x140>(v);
  return v;
}
__device__ __forceinline__ float red8(float v) {
  v = dpp_add<0xB1>(v); v = dpp_add<0x4E>(v); v = dpp_add<0x141>(v);
  return v;
}
__device__ __forceinline__ void glds16(const void* sbase, unsigned voff, unsigned lds) {
  asm volatile("s_mov_b32 m0, %2\n\ts_nop 0\n\tglobal_load_lds_dwordx4 %0, %1" ::"v"(voff), "s"(sbase), "s"(lds) : "memory");
}
#define WAIT_VM(n) asm volatile("s_waitcnt vmcnt(%0)" ::"n"(n) : "memory")
#define WAIT_LGKM0() asm volatile("s_waitcnt lgkmcnt(0)" ::: "memory")
#define BAR() do { asm volatile("s_waitcnt lgkmcnt(0)" ::: "memory"); __builtin_amdgcn_s_barrier(); asm volatile("" ::: "memory"); } while (0)
__device__ __forceinline__ s16x4 tr_read(LAS const char* p) {
  typedef short v4i16 __attribute__((ext_vector_type(4)));
  return __builtin_amdgcn_ds_read_tr16_b64_v4i16((LAS v4i16*)p);
}
__device__ __forceinline__ f32x4 mfma_bf(s16x8 a, s16x8 b, f32x4 c) {
  return __builtin_amdgcn_mfma_f32_16x16x32_bf16(__builtin_bit_cast(bf16x8, a), __builtin_bit_cast(bf16x8, b), c, 0, 0, 0);
}
__device__ __forceinline__ float dot2bf(unsigned a, unsigned b, float c) {
  return __builtin_amdgcn_fdot2_f32_bf16(__builtin_bit_cast(bf16x2, a), __builtin_bit_cast(bf16x2, b), c, false);
}

constexpr int K1_X = 0;
constexpr int K1_W = 57344;
constexpr int K1_SLOT = 16384;
constexpr int K1_S = K1_W + 6 * K1_SLOT;
constexpr int K1_P = K1_S;
constexpr int K1_M2 = K1_S + 2048;
constexpr int K1_R2 = K1_M2 + 256;
constexpr int K1_W1 = K1_R2 + 256;
constexpr int K1_LDS = 163840;
constexpr unsigned ONES_BF = 0x3f803f80u;
typedef float f32x16 __attribute__((ext_vector_type(16)));

__device__ __forceinline__ float xsum32(float v) {
  auto b = __builtin_amdgcn_permlane32_swap(__float_as_uint(v), __float_as_uint(v), false, false);
  return __uint_as_float(b[0]) + __uint_as_float(b[1]);
}
__device__ __forceinline__ float xsum16_32(float v) {
  auto a = __builtin_amdgcn_permlane16_swap(__float_as_uint(v), __float_as_uint(v), false, false);
  return xsum32(__uint_as_float(a[0]) + __uint_as_float(a[1]));
}
__device__ __forceinline__ f32x2 up_bf(unsigned w) { return (f32x2){bflo(w), bfhi(w)}; }
__device__ __forceinline__ unsigned rn_pair(f32x2 v) {
  unsigned r;
  asm("v_cvt_pk_bf16_f32 %0, %1, %2" : "=v"(r) : "v"(v.x), "v"(v.y));
  return r;
}
__device__ __forceinline__ f32x2 gelu2(f32x2 x) {
  const float c1 = -2.0f * 0.7978845608028654f * 1.4426950408889634f, c2 = c1 * 0.044715f;
  const f32x2 t = x * x;
  const f32x2 a = x * (t * c2 + c1);
  f32x2 d = {__builtin_amdgcn_exp2f(a.x), __builtin_amdgcn_exp2f(a.y)};
  d = d + 1.0f;
  return x * (f32x2){__builtin_amdgcn_rcpf(d.x), __builtin_amdgcn_rcpf(d.y)};
}
__device__ __forceinline__ void ln_scalars(float s, float q, float invn, float& mb, float& rr) {
  const float mean = s * invn;
  float var = q * invn - mean * mean; var = var < 0.f ? 0.f : var;
  mb = rbf(mean);
  rr = __builtin_amdgcn_rcpf(rbf(__builtin_amdgcn_sqrtf(rbf(rbf(var) + 1e-5f))));
}
__device__ __forceinline__ f32x16 mfma32_bf(s16x8 a, s16x8 b, f32x16 c) {
  return __builtin_amdgcn_mfma_f32_32x32x16_bf16(__builtin_bit_cast(bf16x8, a), __builtin_bit_cast(bf16x8, b), c, 0, 0, 0);
}
__device__ __forceinline__ void poll_granule(u32x4& gr, const char* base, unsigned voff, unsigned tag) {
  for (unsigned spins = 0;; ++spins) {
    const bool ok = (gr[1] == tag) && (gr[3] == tag);
    if (__all(ok) || spins > (1u << 22)) break;
    __builtin_amdgcn_s_sleep(1);
    asm volatile("global_load_dwordx4 %0, %1, %2 sc1\n\ts_waitcnt vmcnt(0)" : "=&v"(gr) : "v"(voff), "s"(base) : "memory");
  }
}

template <int V> struct IC { static constexpr int value = V; };
template <int A, int B, class F> __device__ __forceinline__ void static_for(F&& f) {
  if constexpr (A < B) { f(IC<A>{}); static_for<A + 1, B>(f); }
}
#define K1_PREFETCH 0
__device__ __forceinline__ void k1_xt(LAS char* smem, const unsigned short* __restrict__ x, const unsigned short* __restrict__ w_sp, char* __restrict__ ws, int v, int tid) {
  const int row = tid >> 3, c8 = tid & 7;
  const unsigned tag = __hip_atomic_load((GAS unsigned*)(ws + WS_EP + v * 128), __ATOMIC_RELAXED, __HIP_MEMORY_SCOPE_AGENT) + 1u;
  u32x4 xv[7];
#pragma unroll
  for (int m = 0; m < 7; ++m) xv[m] = *(const u32x4*)(x + (size_t)(64 * v + row) * DM + (c8 + 8 * m) * 8);
  float s = 0.f, q = 0.f;
#pragma unroll
  for (int m = 0; m < 7; ++m)
#pragma unroll
    for (int e = 0; e < 4; ++e) { const unsigned w = xv[m][e]; s = dot2bf(w, ONES_BF, s); q = dot2bf(w, w, q); }
  s = red8(s); q = red8(q);
  float mb, rr;
  ln_scalars(s, q, 1.0f / 448.0f, mb, rr);
  if (c8 == 0) {
    u32x4 gr = {__float_as_uint(mb), tag, __float_as_uint(rr), tag};
    const char* gp = ws + WS_XS1 + (size_t)v * 1024;
    const unsigned vo = row * 16;
    asm volatile("global_store_dwordx4 %0, %1, %2 sc1\n\ts_nop 1" ::"v"(vo), "v"(gr), "s"(gp) : "memory");
  }
#pragma unroll
  for (int m = 0; m < 7; ++m)
#pragma unroll
    for (int e = 0; e < 4; ++e) {
      const unsigned w = xv[m][e];
      const f32x2 n3 = up_bf(rn_pair(up_bf(rn_pair(up_bf(w) - mb)) * rr));
      const int d = (c8 + 8 * m) * 8 + 2 * e;
      *(LAS _Float16*)(smem + d * 144 + row * 2) = (_Float16)n3.x;
      *(LAS _Float16*)(smem + (d + 1) * 144 + row * 2) = (_Float16)n3.y;
    }
  __syncthreads();
#pragma unroll
  for (int m = 0; m < 7; ++m) {
    const int d = row + 64 * m;
    *(u32x4*)(ws + WS_XT + ((size_t)d * SEQ + 64 * v + 8 * c8) * 2) = *(LAS const u32x4*)(smem + d * 144 + c8 * 16);
  }
}

__global__ __launch_bounds__(512, 2) void k1_gemm1(const unsigned short* __restrict__ x, const unsigned short* __restrict__ w_in, const unsigned short* __restrict__ w_sp, char* __restrict__ ws) {
  extern __shared__ __attribute__((aligned(16))) char smem_raw[];
  LAS char* smem = (LAS char*)smem_raw;
  const int tid = threadIdx.x, lane = tid & 63;
  const int wave = __builtin_amdgcn_readfirstlane(tid >> 6);
  const int b = blockIdx.x;
  if (b >= 224) { k1_xt(smem, x, w_sp, ws, b - 224, tid); return; }
  const int xcd = b & 7, rr = b >> 3;
  const int ti = 4 * xcd + rr / 7, tj = rr % 7, rh = wave >> 2, cg = wave & 3;
  const int h = lane >> 5, l31 = lane & 31;

  STAMP(0, 0);
  unsigned ep;
  {
    const char* epp = ws + WS_EP + ti * 128;
    unsigned zero = 0;
    asm volatile("global_load_dword %0, %1, %2 sc1" : "=v"(ep) : "v"(zero), "s"(epp) : "memory");
  }
  const int xrow = 8 * wave + (lane >> 3);
  const unsigned voffx = (unsigned)(xrow * 896 + (((lane & 7) ^ ((xrow >> 1) & 7)) * 16));
  const char* xsrc = (const char*)x + (size_t)ti * 64 * 896;
  unsigned voffw[2];
#pragma unroll
  for (int e = 0; e < 2; ++e) voffw[e] = (unsigned)((8 * wave + 4 * e + (lane >> 4)) * 3584 + (((lane & 15) ^ (4 * (lane >> 4))) * 16) + tj * 256);
#define K1_XPIECE(g) glds16(xsrc + (g) * 128, voffx, (unsigned)(K1_X + (g) * 8192 + wave * 1024))
#define K1_WPIECE(G, e) \
  glds16(w_in, voffw[e] + (unsigned)(((G) % 7) * 64 * 3584 + ((G) < 7 ? 1792 : 0)), (unsigned)(K1_W + ((G) % 6) * K1_SLOT + wave * 2048 + (e) * 1024))
#pragma unroll
  for (int g = 0; g < 3; ++g) { K1_XPIECE(g); K1_WPIECE(g, 0); K1_WPIECE(g, 1); }
  LAS const char* ab[2];
  LAS const char* xb[4];
  {
    const int g4 = lane >> 4, qq = (lane & 15) >> 2, p4 = lane & 3;
    const int lb = (8 * (g4 >> 1) + qq) * 256 + (((4 * cg + 2 * (g4 & 1) + (p4 >> 1)) ^ (4 * qq)) * 16) + (p4 & 1) * 8;
    ab[0] = smem + K1_W + lb + rh * 4096;
    ab[1] = smem + K1_W + lb + (rh ^ 1) * 4096;
    const int row = 32 * rh + l31, sw = (row >> 1) & 7;
#pragma unroll
    for (int i = 0; i < 4; ++i) xb[i] = smem + K1_X + row * 128 + (((2 * (i ^ rh) + h) ^ sw) * 16);
  }
  s16x8 xa[28], wb[8];
#define K1_FRAGS(st)                                                                                                   \
  do {                                                                                                                 \
    const int G_ = (st) >> 2, i_ = (st) & 3, so_ = (G_ % 6) * K1_SLOT + (i_ >> 1) * 8192;                              \
    if ((st) < 28) xa[(st) % 28] = *(LAS const s16x8*)(xb[i_] + G_ * 8192);                                            \
    wb[(st) & 7] = __builtin_shufflevector(tr_read(ab[i_ & 1] + so_), tr_read(ab[i_ & 1] + so_ + 1024), 0, 1, 2, 3, 4, 5, 6, 7); \
  } while (0)
  f32x16 acc2 = {}, acc1 = {};
  float w1a = 0.f, w1b = 0.f;
  u32x4 g1;
  const char* g1p = ws + WS_XS1 + (size_t)ti * 1024;
  const unsigned g1o = (unsigned)(32 * rh + l31) * 16;
  unsigned zp[8];
  float m1 = 0.f, r1 = 0.f;
  asm volatile("s_waitcnt vmcnt(3)" : "+v"(ep)::"memory");
  const unsigned tag = ep + 1u;
  BAR();
  STAMP(0, 1);
  K1_FRAGS(0); K1_FRAGS(1); K1_FRAGS(2); K1_FRAGS(3);
  auto step = [&](auto stc) __attribute__((always_inline)) {
    constexpr int st = decltype(stc)::value;
    constexpr int G = st >> 2, i = st & 3;
    if (i == 0 && G >= 1) {
      if (G == 1) WAIT_VM(6); else if (G == 2) WAIT_VM(9); else if (G == 3) WAIT_VM(8); else if (G == 4) WAIT_VM(7); else if (G <= 6) WAIT_VM(6);
      else if (G == 7) WAIT_VM(0);
      else if (G <= 9) WAIT_VM(6); else if (G == 10) WAIT_VM(4); else if (G == 11) WAIT_VM(2); else WAIT_VM(0);
      __builtin_amdgcn_s_barrier();
      asm volatile("" ::: "memory");
    }
    __builtin_amdgcn_sched_barrier(0);
    if constexpr (st < 28) acc2 = mfma32_bf(wb[st & 7], xa[st % 28], acc2); else acc1 = mfma32_bf(wb[st & 7], xa[st % 28], acc1);
    if constexpr (st + 4 < 56) K1_FRAGS(st + 4);
    if constexpr ((i & 1) == 0) {
      const u32x4 wv = __builtin_bit_cast(u32x4, wb[st & 7]);
#pragma unroll
      for (int e = 0; e < 4; ++e) { const unsigned ww = wv[e]; if (st < 28) w1a = dot2bf(ww, ONES_BF, w1a); else w1b = dot2bf(ww, ONES_BF, w1b); }
    }
    __builtin_amdgcn_sched_barrier(0);
    if constexpr ((i & 1) == 0) {
      constexpr int ks = st >> 1;
      if constexpr (ks < 4) { K1_XPIECE(ks + 3); K1_WPIECE(ks + 3, 0); K1_WPIECE(ks + 3, 1); }
      else if constexpr (ks < 18) K1_WPIECE(5 + (ks >> 1), ks & 1);
    }
    if constexpr (st == 20) asm volatile("global_load_dwordx4 %0, %1, %2 sc1" : "=v"(g1) : "v"(g1o), "s"(g1p) : "memory");
    __builtin_amdgcn_sched_barrier(0);
  };
  static_for<0, 28>(step);
    {
      STAMP(0, 3);
      const float w1s = xsum32(w1a);
      if (h == 0) *(LAS float*)(smem + K1_W1 + (rh * 128 + 32 * cg + l31) * 4) = w1s;
      asm volatile("s_waitcnt vmcnt(0)" : "+v"(g1)::"memory");
      BAR();
      poll_granule(g1, g1p, g1o, tag);
      m1 = __uint_as_float(g1[0]); r1 = __uint_as_float(g1[2]);
      float ss = 0.f, sq = 0.f;
#pragma unroll
      for (int g = 0; g < 4; ++g) {
        const f32x4 w1 = *(LAS const f32x4*)(smem + K1_W1 + (32 * cg + 8 * g + 4 * h) * 4) + *(LAS const f32x4*)(smem + K1_W1 + (128 + 32 * cg + 8 * g + 4 * h) * 4);
#pragma unroll
        for (int p = 0; p < 2; ++p) {
          const f32x2 a2 = {acc2[4 * g + 2 * p], acc2[4 * g + 2 * p + 1]}, w2 = {w1[2 * p], w1[2 * p + 1]};
          const unsigned zz = rn_pair(gelu2(up_bf(rn_pair((a2 - w2 * m1) * r1))));
          zp[2 * g + p] = zz;
          ss = dot2bf(zz, ONES_BF, ss); sq = dot2bf(zz, zz, sq);
        }
      }
      ss = xsum32(ss); sq = xsum32(sq);
      if (h == 0) *(LAS f32x2*)(smem + K1_P + ((32 * rh + l31) * 4 + cg) * 8) = (f32x2){ss, sq};
      BAR();
      if (wave == 0) {
        const f32x4 p0 = *(LAS const f32x4*)(smem + K1_P + lane * 32), p1 = *(LAS const f32x4*)(smem + K1_P + lane * 32 + 16);
        const float s = (p0[0] + p0[2]) + (p1[0] + p1[2]), q = (p0[1] + p0[3]) + (p1[1] + p1[3]);
        u32x4 gr = {__float_as_uint(s), tag, __float_as_uint(q), tag};
        const char* xp = ws + WS_XCH + (size_t)ti * 64 * 128 + tj * 16;
        const unsigned vo = lane * 128;
        asm volatile("global_store_dwordx4 %0, %1, %2 sc1\n\ts_nop 1" ::"v"(vo), "v"(gr), "s"(xp) : "memory");
      }
      STAMP(0, 4);
    }
  static_for<28, 56>(step);
#undef K1_FRAGS
#undef K1_XPIECE
#undef K1_WPIECE
  STAMP(0, 5);
  {
    const float w1s = xsum32(w1b);
    if (h == 0) *(LAS float*)(smem + K1_W1 + 1024 + (rh * 128 + 32 * cg + l31) * 4) = w1s;
  }
  BAR();
  u32x4 gr;
  const int row = tid >> 3, jj = tid & 7;
  const char* xp = ws + WS_XCH + (size_t)ti * 64 * 128;
  const unsigned vo = row * 128 + (jj < 7 ? jj : 0) * 16;
  asm volatile("global_load_dwordx4 %0, %1, %2 sc1" : "=v"(gr) : "v"(vo), "s"(xp) : "memory");
  {
    const int tt = 4 * ti + 2 * rh + (l31 >> 4);
#pragma unroll
    for (int g = 0; g < 4; ++g) {
      const f32x4 w1 = *(LAS const f32x4*)(smem + K1_W1 + 1024 + (32 * cg + 8 * g + 4 * h) * 4) + *(LAS const f32x4*)(smem + K1_W1 + 1024 + (128 + 32 * cg + 8 * g + 4 * h) * 4);
      unsigned o[2];
#pragma unroll
      for (int p = 0; p < 2; ++p) {
        const f32x2 a2 = {acc1[4 * g + 2 * p], acc1[4 * g + 2 * p + 1]}, w2 = {w1[2 * p], w1[2 * p + 1]};
        o[p] = rn_pair(gelu2(up_bf(rn_pair((a2 - w2 * m1) * r1))));
      }
      const int ft = 8 * tj + 2 * cg + (g >> 1), lp = (lane & 15) + 16 * (2 * (g & 1) + h);
      *(u32x2*)(ws + WS_Z1D + ((size_t)(tt * 56 + ft) * 64 + lp) * 8) = (u32x2){o[0], o[1]};
    }
  }
  STAMP(0, 6);
  asm volatile("s_waitcnt vmcnt(4)" : "+v"(gr)::"memory");
  poll_granule(gr, xp, vo, tag);
  {
    const float s = red8(jj < 7 ? __uint_as_float(gr[0]) : 0.f), q = red8(jj < 7 ? __uint_as_float(gr[2]) : 0.f);
    float mb, rr2;
    ln_scalars(s, q, 1.0f / 896.0f, mb, rr2);
    if (jj == 0) { *(LAS float*)(smem + K1_M2 + row * 4) = mb; *(LAS float*)(smem + K1_R2 + row * 4) = rr2; }
  }
  BAR();
  STAMP(0, 7);
  if (tj == 0 && tid == 0) __hip_atomic_store((GAS unsigned*)(ws + WS_EP + ti * 128), tag, __ATOMIC_RELAXED, __HIP_MEMORY_SCOPE_AGENT);
  {
    LAS char* tb = smem + K1_W + wave * 2304;
    const int srow = 32 * rh + l31;
    const float mb2 = *(LAS const float*)(smem + K1_M2 + srow * 4), rr2 = *(LAS const float*)(smem + K1_R2 + srow * 4);
#pragma unroll
    for (int g = 0; g < 4; ++g) {
      unsigned o[2];
#pragma unroll
      for (int p = 0; p < 2; ++p) o[p] = rn_pair(up_bf(rn_pair(up_bf(zp[2 * g + p]) - mb2)) * rr2);
      *(LAS u32x2*)(tb + l31 * 72 + (8 * g + 4 * h) * 2) = (u32x2){o[0], o[1]};
    }
    asm volatile("s_waitcnt lgkmcnt(0)" ::: "memory");
    const int q4 = lane >> 4, l15 = lane & 15;
#pragma unroll
    for (int n = 0; n < 2; ++n) {
      LAS const char* rp = tb + (8 * q4 + (l15 >> 2)) * 72 + n * 32 + (lane & 3) * 8;
      const s16x4 lo = tr_read(rp), hi = tr_read(rp + 4 * 72);
      const s16x8 o = __builtin_shufflevector(lo, hi, 0, 1, 2, 3, 4, 5, 6, 7);
      const int ft = 8 * tj + 2 * cg + n, sb = 2 * ti + rh;
      *(s16x8*)(ws + WS_Z2F + ((size_t)(ft * 64 + sb) * 64 + lane) * 16) = o;
    }
  }
  STAMP(0, 8);
}

constexpr int K2_RING = 0;
constexpr int K2_CH = 65536;
constexpr int K2_CHS = 49152;
constexpr int CH_FT = 0;
constexpr int CH_FW = 16416, CH_FS = 288;
constexpr int CH_CS = 4160;
constexpr int CH_U = 33280;
constexpr int CH_X = CH_U + 4608;
constexpr int CH_T = CH_X + 4224;
constexpr int CH_CNT = CH_T + 6144;
constexpr int K2_LDS = 163840;

__device__ __forceinline__ void lds_sync(LAS char* cntp, unsigned target, int lane) {
  LAS unsigned* cnt = (LAS unsigned*)cntp;
  asm volatile("s_waitcnt lgkmcnt(0)" ::: "memory");
  if (lane == 0) __hip_atomic_fetch_add(cnt, 1u, __ATOMIC_RELAXED, __HIP_MEMORY_SCOPE_WORKGROUP);
  while ((unsigned)__builtin_amdgcn_readfirstlane(*(volatile LAS unsigned*)cnt) < target) __builtin_amdgcn_s_sleep(1);
  asm volatile("" ::: "memory");
}
__device__ __forceinline__ __amdgpu_buffer_rsrc_t mk_rsrc(const void* p, unsigned bytes) {
  return __builtin_amdgcn_make_buffer_rsrc((void*)p, (short)0, (int)bytes, 0x00020000);
}
__device__ __forceinline__ void bglds16(__amdgpu_buffer_rsrc_t rs, unsigned voff, unsigned soff, unsigned lds) {
  asm volatile("s_mov_b32 m0, %3\n\ts_nop 0\n\tbuffer_load_dwordx4 %0, %1, %2 offen lds" ::"v"(voff), "s"(rs), "s"(soff), "s"(lds) : "memory");
}
__device__ __forceinline__ void bload16(u32x4& dst, __amdgpu_buffer_rsrc_t rs, unsigned voff, unsigned soff) {
  asm volatile("buffer_load_dwordx4 %0, %1, %2, %3 offen" : "=v"(dst) : "v"(voff), "s"(rs), "s"(soff) : "memory");
}

template <int W>
__device__ __forceinline__ void k2_epi(LAS char* smem, char* __restrict__ ws, f32x4 (&acc)[7][4], const u32x2 (&z1v)[7], float bs, int lane, int tt, int J) {
  LAS char* cnt = smem + K2_CH + CH_CNT + 64;
  LAS char* mine = smem + K2_RING + W * 16384 + lane * 16;
  LAS char* other = smem + K2_RING + (W ^ 2) * 16384 + lane * 16;
  LAS char* o2 = smem + K2_RING + (W ^ 3) * 16384 + lane * 16;
  constexpr int K0 = W & 2, S0 = K0 ^ 2;
#pragma unroll
  for (int n = 0; n < 7; ++n) { *(LAS f32x4*)(mine + n * 1024) = acc[n][S0]; *(LAS f32x4*)(mine + (7 + n) * 1024) = acc[n][S0 + 1]; }
  lds_sync(cnt, 4, lane);
#pragma unroll
  for (int n = 0; n < 7; ++n) { acc[n][K0] += *(LAS const f32x4*)(other + n * 1024); acc[n][K0 + 1] += *(LAS const f32x4*)(other + (7 + n) * 1024); }
  asm volatile("s_waitcnt lgkmcnt(0)" ::: "memory");
#pragma unroll
  for (int n = 0; n < 7; ++n) *(LAS f32x4*)(other + n * 1024) = acc[n][W ^ 1];
  lds_sync(cnt, 8, lane);
  STAMP(1, 2);
  unsigned hp[7][2];
#pragma unroll
  for (int n = 0; n < 7; ++n) {
    const f32x4 v = acc[n][W] + *(LAS const f32x4*)(o2 + n * 1024);
#pragma unroll
    for (int p = 0; p < 2; ++p) {
      const f32x2 g0 = up_bf(pk_bf16(v[2 * p], v[2 * p + 1])) + bs;
      const unsigned zw = z1v[n][p];
      const f32x2 h = up_bf(pk_bf16(g0.x, g0.y)) * up_bf(zw);
      hp[n][p] = pk_bf16(h.x, h.y);
    }
  }
  char* hb = ws + WS_HF + ((size_t)(tt * 28 + (7 * J >> 1)) * 64 + lane) * 16;
  if (!(J & 1)) {
#pragma unroll
    for (int p = 0; p < 3; ++p) *(u32x4*)(hb + p * 1024) = (u32x4){hp[2 * p][0], hp[2 * p][1], hp[2 * p + 1][0], hp[2 * p + 1][1]};
    *(u32x2*)(hb + 3 * 1024) = (u32x2){hp[6][0], hp[6][1]};
  } else {
    *(u32x2*)(hb + 8) = (u32x2){hp[0][0], hp[0][1]};
#pragma unroll
    for (int p = 0; p < 3; ++p) *(u32x4*)(hb + (p + 1) * 1024) = (u32x4){hp[2 * p + 1][0], hp[2 * p + 1][1], hp[2 * p + 2][0], hp[2 * p + 2][1]};
  }
}

__device__ __forceinline__ void k2_gemm(LAS char* smem, const unsigned short* __restrict__ w_sp, const unsigned short* __restrict__ b_sp, char* __restrict__ ws,
                                        int wave, int lane, int T, int J, int rot) {
  const __amdgpu_buffer_rsrc_t zs = mk_rsrc(ws + WS_Z2F + (size_t)(7 * J * 64 + 16 * wave) * 1024, 7 * 65536 - 16 * wave * 1024);
  const __amdgpu_buffer_rsrc_t wr = mk_rsrc((const char*)w_sp + (size_t)T * 64 * 4096 + wave * 1024, 64 * 4096 - wave * 1024);
  const unsigned voffz = lane * 16;
  const unsigned voffw0 = (lane >> 3) * 4096 + (((lane & 7) ^ ((lane >> 4) & 3)) * 16);
  const unsigned voffw1 = (8 + (lane >> 3)) * 4096 + (((lane & 7) ^ (4 + ((lane >> 4) & 3))) * 16);
  const unsigned ring = K2_RING + wave * 16384;
  LAS const char* wrd0 = smem + ring + (lane & 15) * 128 + (((lane >> 4) ^ ((lane >> 1) & 7)) * 16);
  LAS const char* wrd1 = smem + ring + (lane & 15) * 128 + (((4 + (lane >> 4)) ^ ((lane >> 1) & 7)) * 16);
  u32x4 z[3][7];
  f32x4 acc[7][4];
#pragma unroll
  for (int n = 0; n < 7; ++n)
#pragma unroll
    for (int m = 0; m < 4; ++m) acc[n][m] = (f32x4){0.f, 0.f, 0.f, 0.f};
#define K2_ISSUE_W(pp)                                                                                      \
  do {                                                                                                      \
    const unsigned kw_ = (unsigned)((2 * (pp) + rot) & 15) * 64;                                            \
    _Pragma("unroll") for (int m = 0; m < 4; ++m) {                                                         \
      bglds16(wr, voffw0, m * 65536 + kw_, ring + ((pp) & 1) * 8192 + m * 2048);                            \
      bglds16(wr, voffw1, m * 65536 + kw_, ring + ((pp) & 1) * 8192 + m * 2048 + 1024);                     \
    }                                                                                                       \
  } while (0)
#define K2_ISSUE_Z(it)                                                                                      \
  do {                                                                                                      \
    const unsigned kz_ = (unsigned)(((it) + rot) & 15) * 1024;                                              \
    _Pragma("unroll") for (int n = 0; n < 7; ++n) bload16(z[(it) % 3][n], zs, voffz, n * 65536 + kz_);      \
  } while (0)
  STAMP(1, 0);
  K2_ISSUE_W(0); K2_ISSUE_Z(0); K2_ISSUE_Z(1); K2_ISSUE_W(1); K2_ISSUE_Z(2);
  const int tt = 4 * T + wave;
  u32x2 z1v[7];
  unsigned bsw;
#define K2_WAITZ(N) asm volatile("s_waitcnt vmcnt(" #N ")" : "+v"(zc[0]), "+v"(zc[1]), "+v"(zc[2]), "+v"(zc[3]), "+v"(zc[4]), "+v"(zc[5]), "+v"(zc[6])::"memory")
#pragma unroll
  for (int it = 0; it < 16; ++it) {
    u32x4(&zc)[7] = z[it % 3];
    if (it <= 13) K2_WAITZ(22); else if (it == 14) K2_WAITZ(15); else K2_WAITZ(8);
    s16x8 wf[4];
#pragma unroll
    for (int m = 0; m < 4; ++m) wf[m] = *(LAS const s16x8*)(((it & 1) ? wrd1 : wrd0) + ((it >> 1) & 1) * 8192 + m * 2048);
    const unsigned kz = (unsigned)((it + 3 + rot) & 15) * 1024, kw = (unsigned)((it + 3 + rot) & 15) * 64;
#pragma unroll
    for (int n = 0; n < 7; ++n) {
#pragma unroll
      for (int m = 0; m < 4; ++m) acc[n][m] = mfma_bf(__builtin_bit_cast(s16x8, zc[n]), wf[m], acc[n][m]);
      __builtin_amdgcn_sched_barrier(0);
      if (it + 3 < 16) {
        bload16(zc[n], zs, voffz, n * 65536 + kz);
        if ((it & 1) && n >= 1 && n <= 4) {
          const int m = n - 1;
          bglds16(wr, voffw0, m * 65536 + kw, ring + (((it + 3) >> 1) & 1) * 8192 + m * 2048);
          bglds16(wr, voffw1, m * 65536 + kw, ring + (((it + 3) >> 1) & 1) * 8192 + m * 2048 + 1024);
        }
      }
      if (it == 13) {
        const char* zb = ws + WS_Z1D + (size_t)(tt * 56 + 7 * J + n) * 512;
        const unsigned vo = lane * 8;
        asm volatile("global_load_dwordx2 %0, %1, %2" : "=v"(z1v[n]) : "v"(vo), "s"(zb) : "memory");
        if (n == 6) {
          const unsigned vb = (unsigned)(16 * tt + (lane & 15)) * 2u;
          asm volatile("global_load_ushort %0, %1, %2" : "=v"(bsw) : "v"(vb), "s"(b_sp) : "memory");
        }
      }
      __builtin_amdgcn_sched_barrier(0);
    }
  }
#undef K2_WAITZ
#undef K2_ISSUE_W
#undef K2_ISSUE_Z
  STAMP(1, 1);
  asm volatile("s_waitcnt vmcnt(0)" : "+v"(z1v[0]), "+v"(z1v[1]), "+v"(z1v[2]), "+v"(z1v[3]), "+v"(z1v[4]), "+v"(z1v[5]), "+v"(z1v[6]), "+v"(bsw)::"memory");
  const float bs = bf2f((unsigned short)bsw);
  if (wave == 0) k2_epi<0>(smem, ws, acc, z1v, bs, lane, tt, J);
  else if (wave == 1) k2_epi<1>(smem, ws, acc, z1v, bs, lane, tt, J);
  else if (wave == 2) k2_epi<2>(smem, ws, acc, z1v, bs, lane, tt, J);
  else k2_epi<3>(smem, ws, acc, z1v, bs, lane, tt, J);
  STAMP(1, 3);
}

__device__ __forceinline__ f32x2 cmul(f32x2 a, f32x2 b) {
  f32x2 t, r;
  asm("v_pk_mul_f32 %0, %1, %2 op_sel:[0,0] op_sel_hi:[0,1]" : "=v"(t) : "v"(a), "v"(b));
  asm("v_pk_fma_f32 %0, %1, %2, %3 op_sel:[1,1,0] op_sel_hi:[1,0,1] neg_lo:[1,0,0]" : "=v"(r) : "v"(a), "v"(b), "v"(t));
  return r;
}
__device__ __forceinline__ unsigned hmul(unsigned a, unsigned b) {
  unsigned t, r;
  asm("v_pk_mul_f16 %0, %1, %2 op_sel:[0,0] op_sel_hi:[0,1]" : "=v"(t) : "v"(a), "v"(b));
  asm("v_pk_fma_f16 %0, %1, %2, %3 op_sel:[1,1,0] op_sel_hi:[1,0,1] neg_lo:[1,0,0]" : "=v"(r) : "v"(a), "v"(b), "v"(t));
  return r;
}
__device__ __forceinline__ unsigned hcv(f32x2 v) { return pk_f16(v.x, v.y); }
__device__ __forceinline__ f32x2 cexp_p(float al, float be, float p) {
  const float e = __builtin_amdgcn_exp2f(al * p), th = __builtin_amdgcn_fractf(be * p);
  return (f32x2){__builtin_amdgcn_cosf(th), __builtin_amdgcn_sinf(th)} * e;
}
__device__ __forceinline__ f32x4 mfma_h(u32x4 a, u32x4 b, f32x4 c) {
  return __builtin_amdgcn_mfma_f32_16x16x32_f16(__builtin_bit_cast(f16x8, a), __builtin_bit_cast(f16x8, b), c, 0, 0, 0);
}
struct PosTab { unsigned short v[2048]; };
constexpr int pos_bf16(int j) {
  if (j < 256) return j;
  int sh = 1; while ((j >> sh) >= 256) ++sh;
  const int q = j >> sh, r = j & ((1 << sh) - 1), h = 1 << (sh - 1);
  return (r > h || (r == h && (q & 1)) ? q + 1 : q) << sh;
}
constexpr PosTab make_postab() {
  PosTab t = {};
  for (int x = 0; x < 2048; ++x) {
    const int p = pos_bf16(2047 - x);
    int col = 0, a = 0;
    if (p < 256) { a = p >> 4; col = p & 15; }
    else if (p < 512) { const int r = p - 256; a = r >> 4; col = 16 + ((r & 15) >> 1); }
    else if (p < 1024) { const int r = p - 512; a = (r & 255) >> 4; col = 24 + 4 * (r >> 8) + ((r & 15) >> 2); }
    else if (p < 2048) { const int r = p - 1024; a = (r & 255) >> 4; col = 32 + 2 * (r >> 8) + ((r & 15) >> 3); }
    else { a = 0; col = 40; }
    t.v[x] = (unsigned short)((col * 16 + a) * 4);
  }
  return t;
}
__device__ const PosTab POSTAB = make_postab();

template <int HW>
__device__ __forceinline__ void s4d_conv(LAS char* cb, char* __restrict__ ws, int d, int lane, float iscale, float dsk) {
  constexpr int NF = HW ? 7 : 8;
  f32x4 acc[8] = {};
  const int r = lane & 15, h = lane >> 4;
  LAS const char* ab = cb + CH_FT + ((7 - r) & 7) * CH_CS + (255 - (r >> 3) - 2 * (h >> 1) + (h & 1)) * 16 - (64 * 7 + 512 * 7);
  LAS const char* bb = cb + CH_U + 512 + 32 * r - 32 * (h >> 1) + 16 * (h & 1) - 64 * 7;
  u32x4 A[2][8], B[2][8];
#define CONV_LOAD(ar, buf)                                                                                   \
  _Pragma("unroll") for (int k = 0; k < NF; ++k) {                                                           \
    B[buf][k] = *(LAS const u32x4*)(bb + 512 * k + 64 * (7 - (ar)));                                         \
    A[buf][k] = *(LAS const u32x4*)(ab + 512 * (7 - k) + 64 * (7 - (ar)));                                   \
  }
  CONV_LOAD(0, 0);
#pragma unroll
  for (int ar = 0; ar < 8; ++ar) {
    __builtin_amdgcn_sched_barrier(0);
    if (ar < 7) { CONV_LOAD(ar + 1, (ar + 1) & 1); }
#pragma unroll
    for (int al = 0; al < NF; ++al)
#pragma unroll
      for (int be = 0; be < NF; ++be) {
        const int tau = al + be;
        if (HW ? (tau >= 4 && tau <= 6) : (tau <= 3 || tau == 7)) acc[tau] = mfma_h(A[ar & 1][al], B[ar & 1][be], acc[tau]);
      }
#pragma unroll
    for (int i = 0; i < 16; ++i) { __builtin_amdgcn_sched_group_barrier(0x008, 1, 0); __builtin_amdgcn_sched_group_barrier(0x100, 1, 0); }
    __builtin_amdgcn_sched_barrier(0);
  }
#undef CONV_LOAD
  STAMP(1, 4);
  const int q = lane >> 4;
#pragma unroll
  for (int t = 0; t < 8; ++t) {
    if (HW ? (t >= 4 && t <= 6) : (t <= 3 || t == 7)) {
      const f32x4 v = acc[t];
      const int l0 = 256 * t + 16 * r + 4 * q;
      const u32x2 uw = *(LAS const u32x2*)(cb + CH_U + 512 + l0 * 2);
      const unsigned uw0 = uw[0], uw1 = uw[1];
      const f16x2 ua = __builtin_bit_cast(f16x2, uw0), ub = __builtin_bit_cast(f16x2, uw1);
      const float uu[4] = {(float)ua[0], (float)ua[1], (float)ub[0], (float)ub[1]};
      float sv[4];
#pragma unroll
      for (int j = 0; j < 4; ++j) sv[j] = rbf(v[j] * iscale) + rbf(uu[j] * dsk);
      *(u32x2*)(ws + WS_ST + ((size_t)d * SEQ + l0) * 2) = (u32x2){pk_bf16(sv[0], sv[1]), pk_bf16(sv[2], sv[3])};
    }
  }
}

__device__ __forceinline__ void k2_s4d(LAS char* cb, const unsigned short* __restrict__ logA, const unsigned short* __restrict__ Aim, const unsigned short* __restrict__ Cre,
                                       const unsigned short* __restrict__ Cim, const unsigned short* __restrict__ logdt, const unsigned short* __restrict__ Dsk,
                                       char* __restrict__ ws, int d, int hw, int lane) {
  LAS char* cnt = cb + CH_CNT;
  unsigned la_[2], ai_[2], cr_[4], ci_[4], ldt_, dsk_;
  u32x4 u0, u1, pt0, pt1;
  {
    const unsigned vo = (unsigned)(d * NST + 128 * hw + lane) * 2u, vo2 = (unsigned)(d * NST + ((128 * hw + 128 + lane) & 255)) * 2u, vd = (unsigned)d * 2u;
    asm volatile("global_load_ushort %0, %1, %2" : "=v"(la_[0]) : "v"(vo), "s"(logA) : "memory");
    asm volatile("global_load_ushort %0, %1, %2 offset:128" : "=v"(la_[1]) : "v"(vo), "s"(logA) : "memory");
    asm volatile("global_load_ushort %0, %1, %2" : "=v"(ai_[0]) : "v"(vo), "s"(Aim) : "memory");
    asm volatile("global_load_ushort %0, %1, %2 offset:128" : "=v"(ai_[1]) : "v"(vo), "s"(Aim) : "memory");
    asm volatile("global_load_ushort %0, %1, %2" : "=v"(cr_[0]) : "v"(vo), "s"(Cre) : "memory");
    asm volatile("global_load_ushort %0, %1, %2 offset:128" : "=v"(cr_[1]) : "v"(vo), "s"(Cre) : "memory");
    asm volatile("global_load_ushort %0, %1, %2" : "=v"(cr_[2]) : "v"(vo2), "s"(Cre) : "memory");
    asm volatile("global_load_ushort %0, %1, %2 offset:128" : "=v"(cr_[3]) : "v"(vo2), "s"(Cre) : "memory");
    asm volatile("global_load_ushort %0, %1, %2" : "=v"(ci_[0]) : "v"(vo), "s"(Cim) : "memory");
    asm volatile("global_load_ushort %0, %1, %2 offset:128" : "=v"(ci_[1]) : "v"(vo), "s"(Cim) : "memory");
    asm volatile("global_load_ushort %0, %1, %2" : "=v"(ci_[2]) : "v"(vo2), "s"(Cim) : "memory");
    asm volatile("global_load_ushort %0, %1, %2 offset:128" : "=v"(ci_[3]) : "v"(vo2), "s"(Cim) : "memory");
    asm volatile("global_load_ushort %0, %1, %2" : "=v"(ldt_) : "v"(vd), "s"(logdt) : "memory");
    asm volatile("global_load_ushort %0, %1, %2" : "=v"(dsk_) : "v"(vd), "s"(Dsk) : "memory");
    const char* ub = ws + WS_XT + (size_t)d * SEQ * 2 + hw * 2048;
    const unsigned vu = lane * 16;
    asm volatile("global_load_dwordx4 %0, %1, %2" : "=v"(u0) : "v"(vu), "s"(ub) : "memory");
    asm volatile("global_load_dwordx4 %0, %1, %2 offset:1024" : "=v"(u1) : "v"(vu), "s"(ub) : "memory");
    const char* pb = (const char*)POSTAB.v + hw * 2048;
    const unsigned vp = lane * 32;
    asm volatile("global_load_dwordx4 %0, %1, %2" : "=v"(pt0) : "v"(vp), "s"(pb) : "memory");
    asm volatile("global_load_dwordx4 %0, %1, %2 offset:16" : "=v"(pt1) : "v"(vp), "s"(pb) : "memory");
    if (hw == 0 && lane < 32) *(LAS u32x4*)(cb + CH_U + lane * 16) = (u32x4){0u, 0u, 0u, 0u};
    if (hw == 1 && lane < 4) *(LAS u32x4*)(cb + CH_X + 4096 + lane * 16) = (u32x4){0u, 0u, 0u, 0u};
  }
  BAR();
#define K2_PRIO 3
  __builtin_amdgcn_s_setprio(K2_PRIO);
  STAMP(1, 0);
  asm volatile("s_waitcnt vmcnt(0)" : "+v"(la_[0]), "+v"(la_[1]), "+v"(ai_[0]), "+v"(ai_[1]), "+v"(cr_[0]), "+v"(cr_[1]), "+v"(cr_[2]), "+v"(cr_[3]), "+v"(ci_[0]), "+v"(ci_[1]),
               "+v"(ci_[2]), "+v"(ci_[3]), "+v"(ldt_), "+v"(dsk_), "+v"(u0), "+v"(u1), "+v"(pt0), "+v"(pt1)::"memory");
  float cr[4], ci[4];
#pragma unroll
  for (int p = 0; p < 4; ++p) { cr[p] = bf2f((unsigned short)cr_[p]); ci[p] = bf2f((unsigned short)ci_[p]); }
  const float dt = rbf(expf(bf2f((unsigned short)ldt_))), dsk = bf2f((unsigned short)dsk_);
  *(LAS u32x4*)(cb + CH_U + 512 + hw * 2048 + lane * 16) = u0;
  *(LAS u32x4*)(cb + CH_U + 512 + hw * 2048 + 1024 + lane * 16) = u1;
  float sabs = 0.f;
#pragma unroll
  for (int p = 0; p < 4; ++p) sabs += __builtin_fabsf(cr[p]) + __builtin_fabsf(ci[p]);
  sabs = xsum16_32(red16(sabs));
  sabs *= 2.0f * dt;
  int ex = (int)((__float_as_uint(sabs) >> 23) & 0xff) - 126;
  ex = ex < -100 ? -100 : (ex > 100 ? 100 : ex);
  const float scale = __uint_as_float((unsigned)(127 + 15 - ex) << 23), iscale = __uint_as_float((unsigned)(127 - 15 + ex) << 23);
  STAMP(1, 1);
  f32x4 kacc[3] = {};
  LAS char* fw = cb + CH_FT + hw * CH_FW;
#pragma unroll 1
  for (int p = 0; p < 2; ++p) {
    const float lap = bf2f((unsigned short)(p ? la_[1] : la_[0])), aip = bf2f((unsigned short)(p ? ai_[1] : ai_[0]));
    const float crp = p ? cr[1] : cr[0], cip = p ? ci[1] : ci[0];
    const float lre = -rbf(expf(lap)) * dt, lim = aip * dt;
    const float al = lre * 1.4426950408889634f, be = lim * 0.15915494309189535f;
    LAS char* wp = fw + lane * 4;
#define PUTH(row, v) *(LAS unsigned*)(wp + (row) * CH_FS) = (v)
    {
      const f32x2 w = cexp_p(al, be, 16.f);
      const float cs = 2.0f * dt * scale;
      const f32x2 c0 = {crp * cs, cip * cs};
      const f32x2 w2 = cmul(w, w), c1 = cmul(c0, w), w4 = cmul(w2, w2), c2 = cmul(c0, w2), c3 = cmul(c1, w2), w8 = cmul(w4, w4), w12 = cmul(w8, w4);
      const unsigned hc[4] = {hcv(c0), hcv(c1), hcv(c2), hcv(c3)}, hw4[3] = {hcv(w4), hcv(w8), hcv(w12)};
#pragma unroll
      for (int j = 0; j < 4; ++j) {
        PUTH(j, hc[j]);
#pragma unroll
        for (int k = 0; k < 3; ++k) PUTH(4 * (k + 1) + j, hmul(hc[j], hw4[k]));
      }
    }
    {
      const f32x2 e1 = cexp_p(al, -be, 1.f);
      const f32x2 e2 = cmul(e1, e1), e3 = cmul(e2, e1), e4 = cmul(e2, e2), e8 = cmul(e4, e4), e12 = cmul(e8, e4);
      unsigned E[16];
      E[0] = 0x00003c00u; E[1] = hcv(e1); E[2] = hcv(e2); E[3] = hcv(e3); E[4] = hcv(e4); E[8] = hcv(e8); E[12] = hcv(e12);
#pragma unroll
      for (int m = 1; m < 4; ++m)
#pragma unroll
        for (int j = 1; j < 4; ++j) E[4 * m + j] = hmul(E[4 * m], E[j]);
#pragma unroll
      for (int k = 0; k < 16; ++k) PUTH(16 + k, E[k]);
      const f32x2 z256 = cexp_p(al, -be, 256.f), z512 = cexp_p(al, -be, 512.f), z1024 = cexp_p(al, -be, 1024.f), z2048 = cexp_p(al, -be, 2048.f);
      const f32x2 z768 = cmul(z512, z256);
      const unsigned h256 = hcv(z256), h512 = hcv(z512), h768 = hcv(z768);
      PUTH(32, h256);
#pragma unroll
      for (int k = 1; k < 8; ++k) PUTH(32 + k, hmul(h256, E[2 * k]));
      PUTH(40, h512); PUTH(44, h768);
#pragma unroll
      for (int k = 1; k < 4; ++k) { PUTH(40 + k, hmul(h512, E[4 * k])); PUTH(44 + k, hmul(h768, E[4 * k])); }
      const unsigned hb[4] = {hcv(z1024), hcv(cmul(z1024, z256)), hcv(cmul(z1024, z512)), hcv(cmul(z1024, z768))};
#pragma unroll
      for (int k = 0; k < 4; ++k) { PUTH(48 + 2 * k, hb[k]); PUTH(49 + 2 * k, hmul(hb[k], E[8])); }
      PUTH(56, hcv(z2048));
    }
#undef PUTH
    asm volatile("s_waitcnt lgkmcnt(0)" ::: "memory");
    LAS const char* rp = fw + (lane & 15) * CH_FS + (lane >> 4) * 16;
    u32x4 fa[4], fb[4][3];
#pragma unroll
    for (int kk = 0; kk < 4; ++kk) {
      fa[kk] = *(LAS const u32x4*)(rp + kk * 64);
#pragma unroll
      for (int t = 0; t < 3; ++t) fb[kk][t] = *(LAS const u32x4*)(rp + (16 + 16 * t) * CH_FS + kk * 64);
    }
#pragma unroll
    for (int kk = 0; kk < 4; ++kk)
#pragma unroll
      for (int t = 0; t < 3; ++t) kacc[t] = mfma_h(fa[kk], fb[kk][t], kacc[t]);
    asm volatile("" ::: "memory");
  }
  STAMP(1, 2);
#pragma unroll
  for (int t = 0; t < 3; ++t) *(LAS f32x4*)(cb + CH_T + hw * 3072 + (16 * t + (lane & 15)) * 64 + (lane >> 4) * 16) = kacc[t];
  lds_sync(cnt, 2, lane);
  {
    const int x0 = 1024 * hw + 16 * lane;
    const unsigned po[8] = {pt0[0], pt0[1], pt0[2], pt0[3], pt1[0], pt1[1], pt1[2], pt1[3]};
    unsigned w[8];
#pragma unroll
    for (int k = 0; k < 8; ++k) {
      LAS const char* a0 = cb + CH_T + (po[k] & 0xffffu);
      LAS const char* a1 = cb + CH_T + (po[k] >> 16);
      w[k] = pk_f16(*(LAS const float*)a0 + *(LAS const float*)(a0 + 3072), *(LAS const float*)a1 + *(LAS const float*)(a1 + 3072));
    }
    *(LAS u32x4*)(cb + CH_X + x0 * 2) = (u32x4){w[0], w[1], w[2], w[3]};
    *(LAS u32x4*)(cb + CH_X + x0 * 2 + 16) = (u32x4){w[4], w[5], w[6], w[7]};
  }
  lds_sync(cnt, 4, lane);
#pragma unroll
  for (int it = 0; it < 3; ++it) {
    const int y = hw * 64 + lane + 128 * it;
    if (it < 2 || y <= 256) {
      const u32x4 r0 = *(LAS const u32x4*)(cb + CH_X + y * 16), r1 = *(LAS const u32x4*)(cb + CH_X + y * 16 + 16);
      unsigned W[8] = {r0[0], r0[1], r0[2], r0[3], r1[0], r1[1], r1[2], r1[3]}, V[7];
#pragma unroll
      for (int i = 0; i < 7; ++i) V[i] = __builtin_amdgcn_alignbit(W[i + 1], W[i], 16);
#pragma unroll
      for (int m = 0; m < 4; ++m) {
        *(LAS u32x4*)(cb + CH_FT + (2 * m) * CH_CS + y * 16) = (u32x4){W[m], W[m + 1], W[m + 2], W[m + 3]};
        *(LAS u32x4*)(cb + CH_FT + (2 * m + 1) * CH_CS + y * 16) = (u32x4){V[m], V[m + 1], V[m + 2], V[m + 3]};
      }
    }
  }
  lds_sync(cnt, 6, lane);
  STAMP(1, 3);
  if (hw) s4d_conv<1>(cb, ws, d, lane, iscale, dsk); else s4d_conv<0>(cb, ws, d, lane, iscale, dsk);
  STAMP(1, 5);
}

__global__ __launch_bounds__(512, 2) void k2_mix(const unsigned short* __restrict__ w_sp, const unsigned short* __restrict__ b_sp, const unsigned short* __restrict__ logA,
                                                 const unsigned short* __restrict__ Aim, const unsigned short* __restrict__ Cre, const unsigned short* __restrict__ Cim,
                                                 const unsigned short* __restrict__ logdt, const unsigned short* __restrict__ Dsk, const unsigned short* __restrict__ w_out,
                                                 char* __restrict__ ws) {
  extern __shared__ __attribute__((aligned(16))) char smem_raw[];
  LAS char* smem = (LAS char*)smem_raw;
  const int tid = threadIdx.x, lane = tid & 63;
  const int wave = __builtin_amdgcn_readfirstlane(tid >> 6);
  const int b = blockIdx.x, xcd = b & 7, rr = b >> 3;
  if (tid < 64) {
    if (lane < 32) *(LAS unsigned*)(smem + K2_CH + CH_CNT + lane * 4) = 0u;
    else *(LAS unsigned*)(smem + K2_CH + K2_CHS + CH_CNT + (lane - 32) * 4) = 0u;
  }
  if (wave < 4) {
    BAR();
    const int T = 8 * (xcd >> 1) + (rr & 7), J = 4 * (xcd & 1) + (rr >> 3);
#define K2_ROT 0
    k2_gemm(smem, w_sp, b_sp, ws, wave, lane, T, J, K2_ROT == 1 ? (2 * (rr & 7) + 4 * (rr >> 3)) & 15 : K2_ROT == 2 ? 2 * (rr & 1) : 0);
  } else {
    const int sw = wave - 4;
    if (b < 224) {
      k2_s4d(smem + K2_CH + (sw >> 1) * K2_CHS, logA, Aim, Cre, Cim, logdt, Dsk, ws, 2 * b + (sw >> 1), sw & 1, lane);
    } else {
      BAR();
      for (int fr = (b - 224) * 4 + sw; fr < 784; fr += 128) {
        const int dt = fr / 28, fb = fr - dt * 28;
        const int dcol = 16 * dt + (lane & 15), q = lane >> 4;
        unsigned short v[8];
#pragma unroll
        for (int jj = 0; jj < 8; ++jj) v[jj] = w_out[(size_t)(32 * fb + 16 * (jj >> 2) + 4 * q + (jj & 3)) * DM + dcol];
        u32x4 o = {(unsigned)v[0] | ((unsigned)v[1] << 16), (unsigned)v[2] | ((unsigned)v[3] << 16), (unsigned)v[4] | ((unsigned)v[5] << 16), (unsigned)v[6] | ((unsigned)v[7] << 16)};
        *(u32x4*)(ws + WS_WOF + ((size_t)fr * 64 + lane) * 16) = o;
      }
    }
  }
}

constexpr int K3_LDS = 49152 + 8448;
__global__ __launch_bounds__(256, 1) void k3_out(const unsigned short* __restrict__ x, char* __restrict__ ws, unsigned short* __restrict__ out) {
  extern __shared__ __attribute__((aligned(16))) char smem_raw[];
  LAS char* smem = (LAS char*)smem_raw;
  const int tid = threadIdx.x, lane = tid & 63;
  const int wave = __builtin_amdgcn_readfirstlane(tid >> 6);
  const int b = blockIdx.x, xcd = b & 7, rr = b >> 3;
  const int T = 4 * xcd + rr / 7, Dt = rr % 7;
  STAMP(2, 0);
  const u32x4* hp = (const u32x4*)(ws + WS_HF + ((size_t)(4 * T * 28 + 7 * wave) * 64 + lane) * 16);
  const u32x4* wp = (const u32x4*)(ws + WS_WOF + ((size_t)(4 * Dt * 28 + 7 * wave) * 64 + lane) * 16);
  u32x4 hf[7][4], wf[7][4];
#pragma unroll
  for (int k = 0; k < 7; ++k)
#pragma unroll
    for (int m = 0; m < 4; ++m) { hf[k][m] = hp[(m * 28 + k) * 64]; wf[k][m] = wp[(m * 28 + k) * 64]; }
  const int erow = tid >> 2, ec = tid & 3;
  const u32x4 st0 = *(const u32x4*)(ws + WS_ST + ((size_t)(64 * Dt + erow) * SEQ + 64 * T + 16 * ec) * 2);
  const u32x4 st1 = *(const u32x4*)(ws + WS_ST + ((size_t)(64 * Dt + erow) * SEQ + 64 * T + 16 * ec + 8) * 2);
  const u32x4 x0 = *(const u32x4*)(x + (size_t)(64 * T + erow) * DM + 64 * Dt + 16 * ec);
  const u32x4 x1 = *(const u32x4*)(x + (size_t)(64 * T + erow) * DM + 64 * Dt + 16 * ec + 8);
  __builtin_amdgcn_sched_barrier(0);
  f32x4 acc[4][4];
#pragma unroll
  for (int n = 0; n < 4; ++n)
#pragma unroll
    for (int m = 0; m < 4; ++m) acc[n][m] = (f32x4){0.f, 0.f, 0.f, 0.f};
#pragma unroll
  for (int k = 0; k < 7; ++k)
#pragma unroll
    for (int n = 0; n < 4; ++n)
#pragma unroll
      for (int m = 0; m < 4; ++m) acc[n][m] = mfma_bf(__builtin_bit_cast(s16x8, wf[k][n]), __builtin_bit_cast(s16x8, hf[k][m]), acc[n][m]);
  STAMP(2, 1);
  LAS char* sl = smem + 49152;
  {
    LAS unsigned* p = (LAS unsigned*)(sl + erow * 132 + ec * 32);
#pragma unroll
    for (int e = 0; e < 4; ++e) { p[e] = st0[e]; p[4 + e] = st1[e]; }
  }
#pragma unroll
  for (int m = 0; m < 4; ++m) {
    if (m != wave) {
      const int slot = m > wave ? m - 1 : m;
#pragma unroll
      for (int n = 0; n < 4; ++n) *(LAS f32x4*)(smem + ((wave * 3 + slot) * 4 + n) * 1024 + lane * 16) = acc[n][m];
    }
  }
  __syncthreads();
  f32x4 o[4];
  if (wave == 0) { for (int n = 0; n < 4; ++n) o[n] = acc[n][0]; }
  else if (wave == 1) { for (int n = 0; n < 4; ++n) o[n] = acc[n][1]; }
  else if (wave == 2) { for (int n = 0; n < 4; ++n) o[n] = acc[n][2]; }
  else { for (int n = 0; n < 4; ++n) o[n] = acc[n][3]; }
#pragma unroll
  for (int sw = 0; sw < 4; ++sw) {
    if (sw != wave) {
      const int slot = wave > sw ? wave - 1 : wave;
#pragma unroll
      for (int n = 0; n < 4; ++n) o[n] += *(LAS const f32x4*)(smem + ((sw * 3 + slot) * 4 + n) * 1024 + lane * 16);
    }
  }
  __syncthreads();
  {
    const int t = 16 * wave + (lane & 15), q = lane >> 4;
#pragma unroll
    for (int n = 0; n < 4; ++n) {
      f32x4 g;
#pragma unroll
      for (int j = 0; j < 4; ++j) {
        const float v = rbf(o[n][j]);
        g[j] = rbf(v * __builtin_amdgcn_rcpf(1.0f + __builtin_amdgcn_exp2f(-1.4426950408889634f * v)));
      }
      *(LAS f32x4*)(smem + t * 272 + (16 * n + 4 * q) * 4) = g;
    }
  }
  __syncthreads();
  {
    unsigned ow[8];
#pragma unroll
    for (int k = 0; k < 4; ++k) {
      const f32x4 g = *(LAS const f32x4*)(smem + erow * 272 + (16 * ec + 4 * k) * 4);
      float r[4];
#pragma unroll
      for (int j = 0; j < 4; ++j) {
        const int dl = 16 * ec + 4 * k + j;
        const float sv = bf2f(*(LAS const unsigned short*)(sl + dl * 132 + erow * 2));
        const unsigned xw = (k < 2 ? x0 : x1)[((4 * k + j) >> 1) & 3];
        const float xv = (j & 1) ? bfhi(xw) : bflo(xw);
        r[j] = xv + rbf(sv * g[j]);
      }
      ow[2 * k] = pk_bf16(r[0], r[1]); ow[2 * k + 1] = pk_bf16(r[2], r[3]);
    }
    u32x4* op = (u32x4*)(out + (size_t)(64 * T + erow) * DM + 64 * Dt + 16 * ec);
    op[0] = (u32x4){ow[0], ow[1], ow[2], ow[3]};
    op[1] = (u32x4){ow[4], ow[5], ow[6], ow[7]};
  }
  STAMP(2, 2);
}

extern "C" void kernel_launch(void* const* d_in, const int* in_sizes, int n_in, void* d_out, int out_size, void* d_ws, size_t ws_size, hipStream_t stream) {
  const unsigned short* x = (const unsigned short*)d_in[0];
  const unsigned short* logA = (const unsigned short*)d_in[1];
  const unsigned short* Aim = (const unsigned short*)d_in[2];
  const unsigned short* Cre = (const unsigned short*)d_in[3];
  const unsigned short* Cim = (const unsigned short*)d_in[4];
  const unsigned short* logdt = (const unsigned short*)d_in[5];
  const unsigned short* Dsk = (const unsigned short*)d_in[6];
  const unsigned short* w_in = (const unsigned short*)d_in[8];
  const unsigned short* w_sp = (const unsigned short*)d_in[9];
  const unsigned short* b_sp = (const unsigned short*)d_in[10];
  const unsigned short* w_out = (const unsigned short*)d_in[11];
  char* ws = (char*)d_ws;
  (void)hipFuncSetAttribute((const void*)k1_gemm1, hipFuncAttributeMaxDynamicSharedMemorySize, K1_LDS);
  (void)hipFuncSetAttribute((const void*)k2_mix, hipFuncAttributeMaxDynamicSharedMemorySize, K2_LDS);
  (void)hipFuncSetAttribute((const void*)k3_out, hipFuncAttributeMaxDynamicSharedMemorySize, K3_LDS);
  k1_gemm1<<<256, 512, K1_LDS, stream>>>(x, w_in, w_sp, ws);
  k2_mix<<<256, 512, K2_LDS, stream>>>(w_sp, b_sp, logA, Aim, Cre, Cim, logdt, Dsk, w_out, ws);
  k3_out<<<224, 256, K3_LDS, stream>>>(x, ws, (unsigned short*)d_out);
}
```

```cpp
#include <hip/hip_runtime.h>
#include <hip/hip_bf16.h>
#include <stdint.h>

#define LAS __attribute__((address_space(3)))
#define GAS __attribute__((address_space(1)))
typedef float f32x2 __attribute__((ext_vector_type(2)));
typedef float f32x4 __attribute__((ext_vector_type(4)));
typedef float f32x16 __attribute__((ext_vector_type(16)));
typedef unsigned u32x2 __attribute__((ext_vector_type(2)));
typedef unsigned u32x4 __attribute__((ext_vector_type(4)));
typedef short s16x4 __attribute__((ext_vector_type(4)));
typedef short s16x8 __attribute__((ext_vector_type(8)));
typedef __bf16 bf16x2 __attribute__((ext_vector_type(2)));
typedef __bf16 bf16x8 __attribute__((ext_vector_type(8)));
typedef _Float16 f16x2 __attribute__((ext_vector_type(2)));
typedef _Float16 f16x8 __attribute__((ext_vector_type(8)));

constexpr int SEQ = 2048, DM = 448, FH = 896, NST = 256;

constexpr unsigned WS_Z1D = 0;
constexpr unsigned WS_Z2F = WS_Z1D + SEQ * FH * 2;
constexpr unsigned WS_HF  = WS_Z2F + SEQ * FH * 2;
constexpr unsigned WS_XT  = WS_HF + SEQ * FH * 2;
constexpr unsigned WS_ST  = WS_XT + SEQ * DM * 2;
constexpr unsigned WS_WOF = WS_ST + SEQ * DM * 2;
constexpr unsigned WS_XCH = WS_WOF + FH * DM * 2;
constexpr unsigned WS_XS1 = WS_XCH + 32 * 64 * 8 * 16;
constexpr unsigned WS_FA  = WS_XS1 + 32 * 1024;
constexpr unsigned WS_FB  = WS_FA + 1024;
constexpr unsigned WS_EP  = WS_FB + 1024;
constexpr unsigned WS_END = WS_EP + 256;

constexpr size_t WS_DBG = 32u << 20;
#define STAMP(kern, slot) do {} while (0)

constexpr unsigned ONES_BF = 0x3f803f80u;
constexpr int AUX_SC1 = 16;
__device__ __forceinline__ float bf2f(unsigned short h) { return __uint_as_float((unsigned)h << 16); }
__device__ __forceinline__ float bflo(unsigned w) { return __uint_as_float(w << 16); }
__device__ __forceinline__ float bfhi(unsigned w) { return __uint_as_float(w & 0xffff0000u); }
__device__ __forceinline__ float rbf(float x) { return (float)(__bf16)x; }
__device__ __forceinline__ unsigned pk_f16(float lo, float hi) {
  f16x2 v = {(_Float16)lo, (_Float16)hi};
  return __builtin_bit_cast(unsigned, v);
}
__device__ __forceinline__ f32x2 up_bf(unsigned w) { return (f32x2){bflo(w), bfhi(w)}; }
__device__ __forceinline__ unsigned rn_pair(f32x2 v) {
  unsigned r;
  asm("v_cvt_pk_bf16_f32 %0, %1, %2" : "=v"(r) : "v"(v.x), "v"(v.y));
  return r;
}
template <int CTRL> __device__ __forceinline__ float dpp_add(float v) {
  int t = __builtin_amdgcn_update_dpp(0, __float_as_int(v), CTRL, 0xf, 0xf, true);
  return v + __int_as_float(t);
}
__device__ __forceinline__ float red8(float v) {
  v = dpp_add<0xB1>(v); v = dpp_add<0x4E>(v); v = dpp_add<0x141>(v);
  return v;
}
__device__ __forceinline__ float red16(float v) { return dpp_add<0x140>(red8(v)); }
__device__ __forceinline__ float xsum32(float v) {
  auto b = __builtin_amdgcn_permlane32_swap(__float_as_uint(v), __float_as_uint(v), false, false);
  return __uint_as_float(b[0]) + __uint_as_float(b[1]);
}
__device__ __forceinline__ float xsum16_32(float v) {
  auto a = __builtin_amdgcn_permlane16_swap(__float_as_uint(v), __float_as_uint(v), false, false);
  return xsum32(__uint_as_float(a[0]) + __uint_as_float(a[1]));
}
__device__ __forceinline__ void glds16(const void* sbase, unsigned voff, unsigned lds) {
  asm volatile("s_mov_b32 m0, %2\n\ts_nop 0\n\tglobal_load_lds_dwordx4 %0, %1" ::"v"(voff), "s"(sbase), "s"(lds) : "memory");
}
#define WAIT_VM(n) asm volatile("s_waitcnt vmcnt(%0)" ::"n"(n) : "memory")
#define BAR() do { asm volatile("s_waitcnt lgkmcnt(0)" ::: "memory"); __builtin_amdgcn_s_barrier(); asm volatile("" ::: "memory"); } while (0)
__device__ __forceinline__ s16x4 tr_read(LAS const char* p) {
  typedef short v4i16 __attribute__((ext_vector_type(4)));
  return __builtin_amdgcn_ds_read_tr16_b64_v4i16((LAS v4i16*)p);
}
__device__ __forceinline__ f32x4 mfma_bf(s16x8 a, s16x8 b, f32x4 c) {
  return __builtin_amdgcn_mfma_f32_16x16x32_bf16(__builtin_bit_cast(bf16x8, a), __builtin_bit_cast(bf16x8, b), c, 0, 0, 0);
}
__device__ __forceinline__ f32x16 mfma32_bf(s16x8 a, s16x8 b, f32x16 c) {
  return __builtin_amdgcn_mfma_f32_32x32x16_bf16(__builtin_bit_cast(bf16x8, a), __builtin_bit_cast(bf16x8, b), c, 0, 0, 0);
}
__device__ __forceinline__ f32x4 mfma_h(u32x4 a, u32x4 b, f32x4 c) {
  return __builtin_amdgcn_mfma_f32_16x16x32_f16(__builtin_bit_cast(f16x8, a), __builtin_bit_cast(f16x8, b), c, 0, 0, 0);
}
__device__ __forceinline__ float dot2bf(unsigned a, unsigned b, float c) {
  return __builtin_amdgcn_fdot2_f32_bf16(__builtin_bit_cast(bf16x2, a), __builtin_bit_cast(bf16x2, b), c, false);
}
__device__ __forceinline__ __amdgpu_buffer_rsrc_t mk_rsrc(const void* p, unsigned bytes) {
  return __builtin_amdgcn_make_buffer_rsrc((void*)p, (short)0, (int)bytes, 0x00020000);
}
typedef __amdgpu_buffer_rsrc_t rsrc_t;
__device__ __forceinline__ void st16(rsrc_t r, unsigned off, u32x4 v) { __builtin_amdgcn_raw_buffer_store_b128(v, r, off, 0, AUX_SC1); }
__device__ __forceinline__ void st8(rsrc_t r, unsigned off, u32x2 v) { __builtin_amdgcn_raw_buffer_store_b64(v, r, off, 0, AUX_SC1); }
__device__ __forceinline__ void st4(rsrc_t r, unsigned off, unsigned v) { __builtin_amdgcn_raw_buffer_store_b32(v, r, off, 0, AUX_SC1); }
__device__ __forceinline__ u32x4 ld16(rsrc_t r, unsigned off) { return __builtin_amdgcn_raw_buffer_load_b128(r, off, 0, AUX_SC1); }
__device__ __forceinline__ void bglds16(rsrc_t rs, unsigned voff, unsigned soff, unsigned lds) {
  asm volatile("s_mov_b32 m0, %3\n\ts_nop 0\n\tbuffer_load_dwordx4 %0, %1, %2 offen lds" ::"v"(voff), "s"(rs), "s"(soff), "s"(lds) : "memory");
}
__device__ __forceinline__ void bload16(u32x4& dst, rsrc_t rs, unsigned voff, unsigned soff) {
  asm volatile("buffer_load_dwordx4 %0, %1, %2, %3 offen sc1" : "=v"(dst) : "v"(voff), "s"(rs), "s"(soff) : "memory");
}
__device__ __forceinline__ void poll_granule(u32x4& gr, rsrc_t rs, unsigned voff, unsigned tag) {
  for (unsigned spins = 0;; ++spins) {
    const bool ok = (gr[1] == tag) && (gr[3] == tag);
    if (__all(ok) || spins > (1u << 20)) break;
    __builtin_amdgcn_s_sleep(1);
    asm volatile("buffer_load_dwordx4 %0, %1, %2, 0 offen sc1\n\ts_waitcnt vmcnt(0)" : "=&v"(gr) : "v"(voff), "s"(rs) : "memory");
  }
}
__device__ __forceinline__ void poll_flags(rsrc_t rs, unsigned voff, unsigned tag) {
  for (unsigned spins = 0;; ++spins) {
    unsigned f;
    asm volatile("buffer_load_dword %0, %1, %2, 0 offen sc1\n\ts_waitcnt vmcnt(0)" : "=&v"(f) : "v"(voff), "s"(rs) : "memory");
    if (__all(f == tag) || spins > (1u << 20)) break;
    __builtin_amdgcn_s_sleep(2);
  }
}
__device__ __forceinline__ void lds_sync(LAS char* cntp, unsigned target, int lane) {
  LAS unsigned* cnt = (LAS unsigned*)cntp;
  asm volatile("s_waitcnt lgkmcnt(0)" ::: "memory");
  if (lane == 0) __hip_atomic_fetch_add(cnt, 1u, __ATOMIC_RELAXED, __HIP_MEMORY_SCOPE_WORKGROUP);
  while ((unsigned)__builtin_amdgcn_readfirstlane(*(volatile LAS unsigned*)cnt) < target) __builtin_amdgcn_s_sleep(1);
  asm volatile("" ::: "memory");
}
template <int V> struct IC { static constexpr int value = V; };
template <int A, int B, class F> __device__ __forceinline__ void static_for(F&& f) {
  if constexpr (A < B) { f(IC<A>{}); static_for<A + 1, B>(f); }
}
__device__ __forceinline__ f32x2 gelu2(f32x2 x) {
  const float c1 = -2.0f * 0.7978845608028654f * 1.4426950408889634f, c2 = c1 * 0.044715f;
  const f32x2 t = x * x;
  const f32x2 a = x * (t * c2 + c1);
  f32x2 d = {__builtin_amdgcn_exp2f(a.x), __builtin_amdgcn_exp2f(a.y)};
  d = d + 1.0f;
  return x * (f32x2){__builtin_amdgcn_rcpf(d.x), __builtin_amdgcn_rcpf(d.y)};
}
__device__ __forceinline__ void ln_scalars(float s, float q, float invn, float& mb, float& rr) {
  const float mean = s * invn;
  float var = q * invn - mean * mean; var = var < 0.f ? 0.f : var;
  mb = rbf(mean);
  rr = __builtin_amdgcn_rcpf(rbf(__builtin_amdgcn_sqrtf(rbf(rbf(var) + 1e-5f))));
}

struct Params {
  const unsigned short *x, *logA, *Aim, *Cre, *Cim, *logdt, *Dsk, *w_in, *w_sp, *b_sp, *w_out;
  char* ws;
  unsigned short* out;
};

constexpr int A_X = 0;
constexpr int A_S = 57344;
constexpr int A_P = A_S;
constexpr int A_M2 = A_S + 2048;
constexpr int A_R2 = A_M2 + 256;
constexpr int A_W1 = A_R2 + 256;
constexpr int A_W = 65536;
constexpr int A_SLOT = 16384;
constexpr int B_RING = 0;

struct W2Src { rsrc_t wr; unsigned voff0, voff1; };
__device__ __forceinline__ W2Src w2_src(const Params& p, int T, int wave, int lane) {
  W2Src s;
  s.wr = mk_rsrc((const char*)p.w_sp + (size_t)T * 64 * 4096 + wave * 1024, 64 * 4096 - wave * 1024);
  s.voff0 = (lane >> 3) * 4096 + (((lane & 7) ^ ((lane >> 4) & 3)) * 16);
  s.voff1 = (8 + (lane >> 3)) * 4096 + (((lane & 7) ^ (4 + ((lane >> 4) & 3))) * 16);
  return s;
}
__device__ __forceinline__ void w2_piece(const W2Src& s, int wave, int pair, int piece) {
  bglds16(s.wr, (piece & 1) ? s.voff1 : s.voff0, (unsigned)((piece >> 1) * 65536 + pair * 128), (unsigned)(B_RING + wave * 16384 + (pair & 1) * 8192 + piece * 1024));
}

__device__ __forceinline__ unsigned make_tag(unsigned ep) {
  unsigned t;
  asm volatile("v_add_u32 %0, 1, %1" : "=v"(t) : "v"(ep));
  return t;
}
__device__ __forceinline__ unsigned phaseA_aux(LAS char* smem, const Params& p, rsrc_t wsr, unsigned ep, int v, int T2, int tid) {
  char* ws = p.ws;
  const int row = tid >> 3, c8 = tid & 7, lane = tid & 63;
  const int wave = __builtin_amdgcn_readfirstlane(tid >> 6);
  u32x4 xv[7];
#pragma unroll
  for (int m = 0; m < 7; ++m) xv[m] = *(const u32x4*)(p.x + (size_t)(64 * v + row) * DM + (c8 + 8 * m) * 8);
  float s = 0.f, q = 0.f;
#pragma unroll
  for (int m = 0; m < 7; ++m)
#pragma unroll
    for (int e = 0; e < 4; ++e) { const unsigned w = xv[m][e]; s = dot2bf(w, ONES_BF, s); q = dot2bf(w, w, q); }
  s = red8(s); q = red8(q);
  float mb, rr;
  ln_scalars(s, q, 1.0f / 448.0f, mb, rr);
  const unsigned tag = make_tag(ep);
  if (c8 == 0) st16(wsr, WS_XS1 + v * 1024 + row * 16, (u32x4){__float_as_uint(mb), tag, __float_as_uint(rr), tag});
  LAS char* tb = smem + A_W;
#pragma unroll
  for (int m = 0; m < 7; ++m)
#pragma unroll
    for (int e = 0; e < 4; ++e) {
      const unsigned w = xv[m][e];
      const f32x2 n3 = up_bf(rn_pair(up_bf(rn_pair(up_bf(w) - mb)) * rr));
      const int d = (c8 + 8 * m) * 8 + 2 * e;
      *(LAS _Float16*)(tb + d * 144 + row * 2) = (_Float16)n3.x;
      *(LAS _Float16*)(tb + (d + 1) * 144 + row * 2) = (_Float16)n3.y;
    }
  BAR();
#pragma unroll
  for (int m = 0; m < 7; ++m) {
    const int d = row + 64 * m;
    st16(wsr, WS_XT + (d * SEQ + 64 * v + 8 * c8) * 2, *(LAS const u32x4*)(tb + d * 144 + c8 * 16));
  }
  for (int fr = v * 8 + wave; fr < 784; fr += 256) {
    const int dt = fr / 28, fb = fr - dt * 28;
    const int dcol = 16 * dt + (lane & 15), qq = lane >> 4;
    unsigned short w[8];
#pragma unroll
    for (int jj = 0; jj < 8; ++jj) w[jj] = p.w_out[(size_t)(32 * fb + 16 * (jj >> 2) + 4 * qq + (jj & 3)) * DM + dcol];
    st16(wsr, WS_WOF + (fr * 64 + lane) * 16,
         (u32x4){(unsigned)w[0] | ((unsigned)w[1] << 16), (unsigned)w[2] | ((unsigned)w[3] << 16), (unsigned)w[4] | ((unsigned)w[5] << 16), (unsigned)w[6] | ((unsigned)w[7] << 16)});
  }
  if (wave < 4) {
    const W2Src w2 = w2_src(p, T2, wave, lane);
#pragma unroll
    for (int k = 0; k < 8; ++k) w2_piece(w2, wave, 0, k);
  }
  return tag;
}

__device__ __forceinline__ unsigned phaseA_gemm(LAS char* smem, const Params& p, rsrc_t wsr, unsigned ep, int b, int ti, int tj, int T2, int tid) {
  char* ws = p.ws;
  const int lane = tid & 63;
  const int wave = __builtin_amdgcn_readfirstlane(tid >> 6);
  const int rh = wave >> 2, cg = wave & 3, h = lane >> 5, l31 = lane & 31;
  const int xrow = 8 * wave + (lane >> 3);
  const unsigned voffx = (unsigned)(xrow * 896 + (((lane & 7) ^ ((xrow >> 1) & 7)) * 16));
  const char* xsrc = (const char*)p.x + (size_t)ti * 64 * 896;
  unsigned voffw[2];
#pragma unroll
  for (int e = 0; e < 2; ++e) voffw[e] = (unsigned)((8 * wave + 4 * e + (lane >> 4)) * 3584 + (((lane & 15) ^ (4 * (lane >> 4))) * 16) + tj * 256);
#define A_XPIECE(g) glds16(xsrc + (g) * 128, voffx, (unsigned)(A_X + (g) * 8192 + wave * 1024))
#define A_WPIECE(G, e) \
  glds16(p.w_in, voffw[e] + (unsigned)(((G) % 7) * 64 * 3584 + ((G) < 7 ? 1792 : 0)), (unsigned)(A_W + ((G) % 6) * A_SLOT + wave * 2048 + (e) * 1024))
#pragma unroll
  for (int g = 0; g < 3; ++g) { A_XPIECE(g); A_WPIECE(g, 0); A_WPIECE(g, 1); }
  LAS const char* ab[2];
  LAS const char* xb[4];
  {
    const int g4 = lane >> 4, qq = (lane & 15) >> 2, p4 = lane & 3;
    const int lb = (8 * (g4 >> 1) + qq) * 256 + (((4 * cg + 2 * (g4 & 1) + (p4 >> 1)) ^ (4 * qq)) * 16) + (p4 & 1) * 8;
    ab[0] = smem + A_W + lb + rh * 4096;
    ab[1] = smem + A_W + lb + (rh ^ 1) * 4096;
    const int row = 32 * rh + l31, sw = (row >> 1) & 7;
#pragma unroll
    for (int i = 0; i < 4; ++i) xb[i] = smem + A_X + row * 128 + (((2 * (i ^ rh) + h) ^ sw) * 16);
  }
  const W2Src w2 = w2_src(p, T2, wave & 3, lane);
  s16x8 xa[28], wb[8];
#define A_FRAGS(st)                                                                                                    \
  do {                                                                                                                 \
    const int G_ = (st) >> 2, i_ = (st) & 3, so_ = (G_ % 6) * A_SLOT + (i_ >> 1) * 8192;                               \
    if ((st) < 28) xa[(st) % 28] = *(LAS const s16x8*)(xb[i_] + G_ * 8192);                                            \
    wb[(st) & 7] = __builtin_shufflevector(tr_read(ab[i_ & 1] + so_), tr_read(ab[i_ & 1] + so_ + 1024), 0, 1, 2, 3, 4, 5, 6, 7); \
  } while (0)
  f32x16 acc2 = {}, acc1 = {};
  float w1a = 0.f, w1b = 0.f;
  u32x4 g1;
  const unsigned g1o = WS_XS1 + ti * 1024 + (32 * rh + l31) * 16;
  unsigned zp[8];
  float m1 = 0.f, r1 = 0.f, ss = 0.f, sq = 0.f;
  const unsigned tag = make_tag(ep);
  WAIT_VM(3);
  BAR();
  STAMP(0, 1);
  A_FRAGS(0); A_FRAGS(1); A_FRAGS(2); A_FRAGS(3);
  auto step = [&](auto stc) __attribute__((always_inline)) {
    constexpr int st = decltype(stc)::value;
    constexpr int G = st >> 2, i = st & 3;
    if (i == 0 && G >= 1) {
      if (G == 1) WAIT_VM(6); else if (G == 2) WAIT_VM(9); else if (G == 3) WAIT_VM(8); else if (G == 4) WAIT_VM(7); else if (G <= 6) WAIT_VM(6);
      else if (G == 7) asm volatile("s_waitcnt vmcnt(3) lgkmcnt(0)" : "+v"(g1)::"memory");
      else if (G == 8) WAIT_VM(6);
      else if (G == 9) WAIT_VM(7); else if (G == 10) WAIT_VM(9);
      else if (G == 11) asm volatile("s_waitcnt vmcnt(10) lgkmcnt(0)" ::: "memory");
      else if (G == 12) WAIT_VM(8);
      __builtin_amdgcn_s_barrier();
      asm volatile("" ::: "memory");
      if (G == 7) STAMP(0, 2);
    }
    __builtin_amdgcn_sched_barrier(0);
    if constexpr (st < 28) acc2 = mfma32_bf(wb[st & 7], xa[st % 28], acc2); else acc1 = mfma32_bf(wb[st & 7], xa[st % 28], acc1);
    if constexpr (st + 4 < 56) A_FRAGS(st + 4);
    if constexpr ((i & 1) == 0) {
      const u32x4 wv = __builtin_bit_cast(u32x4, wb[st & 7]);
#pragma unroll
      for (int e = 0; e < 4; ++e) { const unsigned ww = wv[e]; if (st < 28) w1a = dot2bf(ww, ONES_BF, w1a); else w1b = dot2bf(ww, ONES_BF, w1b); }
    }
    if constexpr (st == 27) {
      const float w1s = xsum32(w1a);
      if (h == 0) *(LAS float*)(smem + A_W1 + (rh * 128 + 32 * cg + l31) * 4) = w1s;
    }
    if constexpr (st == 28) {
      poll_granule(g1, wsr, g1o, tag);
      m1 = __uint_as_float(g1[0]); r1 = __uint_as_float(g1[2]);
    }
    if constexpr (G >= 7 && G <= 10 && i == 1) {
      constexpr int g = G - 7;
      const f32x4 w1 = *(LAS const f32x4*)(smem + A_W1 + (32 * cg + 8 * g + 4 * h) * 4) + *(LAS const f32x4*)(smem + A_W1 + (128 + 32 * cg + 8 * g + 4 * h) * 4);
#pragma unroll
      for (int pp = 0; pp < 2; ++pp) {
        const f32x2 a2 = {acc2[4 * g + 2 * pp], acc2[4 * g + 2 * pp + 1]}, w2v = {w1[2 * pp], w1[2 * pp + 1]};
        const unsigned zz = rn_pair(gelu2(up_bf(rn_pair((a2 - w2v * m1) * r1))));
        zp[2 * g + pp] = zz;
        ss = dot2bf(zz, ONES_BF, ss); sq = dot2bf(zz, zz, sq);
      }
    }
    if constexpr (st == 43) {
      const float s2 = xsum32(ss), q2 = xsum32(sq);
      if (h == 0) *(LAS f32x2*)(smem + A_P + ((32 * rh + l31) * 4 + cg) * 8) = (f32x2){s2, q2};
    }
    if constexpr (st == 44) {
      if (wave == 0) {
        const f32x4 p0 = *(LAS const f32x4*)(smem + A_P + lane * 32), p1 = *(LAS const f32x4*)(smem + A_P + lane * 32 + 16);
        const float s = (p0[0] + p0[2]) + (p1[0] + p1[2]), q = (p0[1] + p0[3]) + (p1[1] + p1[3]);
        u32x4 gr = {__float_as_uint(s), tag, __float_as_uint(q), tag};
        const unsigned vo = WS_XCH + ti * 8192 + lane * 128 + tj * 16;
        asm volatile("buffer_store_dwordx4 %0, %1, %2, 0 offen sc1\n\ts_nop 1" ::"v"(gr), "v"(vo), "s"(wsr) : "memory");
      }
    }
    __builtin_amdgcn_sched_barrier(0);
    if constexpr ((i & 1) == 0) {
      constexpr int ks = st >> 1;
      if constexpr (ks < 4) { A_XPIECE(ks + 3); A_WPIECE(ks + 3, 0); A_WPIECE(ks + 3, 1); }
      else if constexpr (ks < 18) A_WPIECE(5 + (ks >> 1), ks & 1);
    }
    if constexpr (st == 20) asm volatile("buffer_load_dwordx4 %0, %1, %2, 0 offen sc1" : "=v"(g1) : "v"(g1o), "s"(wsr) : "memory");
    if constexpr (st >= 35 && st <= 42) {
      if (wave < 4) w2_piece(w2, wave, 0, st - 35);
      else {
        unsigned po = (unsigned)((b * 4 + (wave - 4)) * 9 + (st - 35)) * 1024u;
        po = po > 8388608u - 1024u ? 8388608u - 1024u : po;
        glds16((const char*)p.w_sp + po, (unsigned)lane * 16u, (unsigned)(A_X + 8192 + (wave - 4) * 1024));
      }
    }
    __builtin_amdgcn_sched_barrier(0);
  };
  static_for<0, 56>(step);
#undef A_FRAGS
#undef A_XPIECE
#undef A_WPIECE
  STAMP(0, 3);
  {
    const float w1s = xsum32(w1b);
    if (h == 0) *(LAS float*)(smem + A_W1 + 1024 + (rh * 128 + 32 * cg + l31) * 4) = w1s;
  }
  BAR();
  u32x4 gr;
  const int row = tid >> 3, jj = tid & 7;
  const unsigned vo = WS_XCH + ti * 8192 + row * 128 + (jj < 7 ? jj : 0) * 16;
  asm volatile("buffer_load_dwordx4 %0, %1, %2, 0 offen sc1" : "=v"(gr) : "v"(vo), "s"(wsr) : "memory");
  {
    const int tt = 4 * ti + 2 * rh + (l31 >> 4);
    unsigned o[4][2];
#pragma unroll
    for (int g = 0; g < 4; ++g) {
      const f32x4 w1 = *(LAS const f32x4*)(smem + A_W1 + 1024 + (32 * cg + 8 * g + 4 * h) * 4) + *(LAS const f32x4*)(smem + A_W1 + 1024 + (128 + 32 * cg + 8 * g + 4 * h) * 4);
#pragma unroll
      for (int pp = 0; pp < 2; ++pp) {
        const f32x2 a2 = {acc1[4 * g + 2 * pp], acc1[4 * g + 2 * pp + 1]}, w2v = {w1[2 * pp], w1[2 * pp + 1]};
        o[g][pp] = rn_pair(gelu2(up_bf(rn_pair((a2 - w2v * m1) * r1))));
      }
    }
    asm volatile("s_waitcnt vmcnt(0)" : "+v"(gr)::"memory");
#pragma unroll
    for (int g = 0; g < 4; ++g) {
      const int ft = 8 * tj + 2 * cg + (g >> 1), lp = (lane & 15) + 16 * (2 * (g & 1) + h);
      st8(wsr, WS_Z1D + ((tt * 56 + ft) * 64 + lp) * 8, (u32x2){o[g][0], o[g][1]});
    }
  }
  STAMP(0, 4);
  poll_granule(gr, wsr, vo, tag);
  {
    const float s = red8(jj < 7 ? __uint_as_float(gr[0]) : 0.f), q = red8(jj < 7 ? __uint_as_float(gr[2]) : 0.f);
    float mb, rr2;
    ln_scalars(s, q, 1.0f / 896.0f, mb, rr2);
    if (jj == 0) { *(LAS float*)(smem + A_M2 + row * 4) = mb; *(LAS float*)(smem + A_R2 + row * 4) = rr2; }
  }
  BAR();
  {
    LAS char* tb = smem + A_W + wave * 2304;
    const int srow = 32 * rh + l31;
    const float mb2 = *(LAS const float*)(smem + A_M2 + srow * 4), rr2 = *(LAS const float*)(smem + A_R2 + srow * 4);
#pragma unroll
    for (int g = 0; g < 4; ++g) {
      unsigned o[2];
#pragma unroll
      for (int pp = 0; pp < 2; ++pp) o[pp] = rn_pair(up_bf(rn_pair(up_bf(zp[2 * g + pp]) - mb2)) * rr2);
      *(LAS u32x2*)(tb + l31 * 72 + (8 * g + 4 * h) * 2) = (u32x2){o[0], o[1]};
    }
    asm volatile("s_waitcnt lgkmcnt(0)" ::: "memory");
    const int q4 = lane >> 4, l15 = lane & 15;
#pragma unroll
    for (int n = 0; n < 2; ++n) {
      LAS const char* rp = tb + (8 * q4 + (l15 >> 2)) * 72 + n * 32 + (lane & 3) * 8;
      const s16x4 lo = tr_read(rp), hi = tr_read(rp + 4 * 72);
      const s16x8 o = __builtin_shufflevector(lo, hi, 0, 1, 2, 3, 4, 5, 6, 7);
      const int ft = 8 * tj + 2 * cg + n, sb = 2 * ti + rh;
      st16(wsr, WS_Z2F + ((ft * 64 + sb) * 64 + lane) * 16, __builtin_bit_cast(u32x4, o));
    }
  }
  return tag;
}

constexpr int B_CH = 65536;
constexpr int B_CHS = 49152;
constexpr int CH_FT = 0;
constexpr int CH_FW = 16416, CH_FS = 288;
constexpr int CH_CS = 4160;
constexpr int CH_U = 33280;
constexpr int CH_T = CH_U + 4608;
constexpr int CH_TS = 49 * 64;
constexpr int CH_CNT = CH_T + 2 * CH_TS;

template <int W>
__device__ __forceinline__ void b_epi(LAS char* smem, rsrc_t wsr, f32x4 (&acc)[7][4], const u32x2 (&z1v)[7], float bs, int lane, int tt, int J) {
  LAS char* cnt = smem + B_CH + CH_CNT + 64;
  LAS char* mine = smem + B_RING + W * 16384 + lane * 16;
  LAS char* other = smem + B_RING + (W ^ 2) * 16384 + lane * 16;
  LAS char* o2 = smem + B_RING + (W ^ 3) * 16384 + lane * 16;
  constexpr int K0 = W & 2, S0 = K0 ^ 2;
#pragma unroll
  for (int n = 0; n < 7; ++n) { *(LAS f32x4*)(mine + n * 1024) = acc[n][S0]; *(LAS f32x4*)(mine + (7 + n) * 1024) = acc[n][S0 + 1]; }
  lds_sync(cnt, 4, lane);
#pragma unroll
  for (int n = 0; n < 7; ++n) { acc[n][K0] += *(LAS const f32x4*)(other + n * 1024); acc[n][K0 + 1] += *(LAS const f32x4*)(other + (7 + n) * 1024); }
  asm volatile("s_waitcnt lgkmcnt(0)" ::: "memory");
#pragma unroll
  for (int n = 0; n < 7; ++n) *(LAS f32x4*)(other + n * 1024) = acc[n][W ^ 1];
  lds_sync(cnt, 8, lane);
  unsigned hp[7][2];
#pragma unroll
  for (int n = 0; n < 7; ++n) {
    const f32x4 v = acc[n][W] + *(LAS const f32x4*)(o2 + n * 1024);
#pragma unroll
    for (int pp = 0; pp < 2; ++pp) {
      const f32x2 g0 = up_bf(rn_pair((f32x2){v[2 * pp], v[2 * pp + 1]})) + bs;
      const unsigned zw = z1v[n][pp];
      hp[n][pp] = rn_pair(up_bf(rn_pair(g0)) * up_bf(zw));
    }
  }
  const unsigned hb = WS_HF + ((tt * 28 + (7 * J >> 1)) * 64 + lane) * 16;
  if (!(J & 1)) {
#pragma unroll
    for (int pp = 0; pp < 3; ++pp) st16(wsr, hb + pp * 1024, (u32x4){hp[2 * pp][0], hp[2 * pp][1], hp[2 * pp + 1][0], hp[2 * pp + 1][1]});
    st8(wsr, hb + 3 * 1024, (u32x2){hp[6][0], hp[6][1]});
  } else {
    st8(wsr, hb + 8, (u32x2){hp[0][0], hp[0][1]});
#pragma unroll
    for (int pp = 0; pp < 3; ++pp) st16(wsr, hb + (pp + 1) * 1024, (u32x4){hp[2 * pp + 1][0], hp[2 * pp + 1][1], hp[2 * pp + 2][0], hp[2 * pp + 2][1]});
  }
}

__device__ __forceinline__ void phaseB_gemm(LAS char* smem, const Params& p, rsrc_t wsr, unsigned tag, int wave, int lane, int T, int J) {
  char* ws = p.ws;
  const rsrc_t zs = mk_rsrc(ws + WS_Z2F + (size_t)(7 * J * 64 + 16 * wave) * 1024, 7 * 65536 - 16 * wave * 1024);
  const W2Src w2 = w2_src(p, T, wave, lane);
  const unsigned voffz = lane * 16;
  const unsigned ring = B_RING + wave * 16384;
  LAS const char* wrd0 = smem + ring + (lane & 15) * 128 + (((lane >> 4) ^ ((lane >> 1) & 7)) * 16);
  LAS const char* wrd1 = smem + ring + (lane & 15) * 128 + (((4 + (lane >> 4)) ^ ((lane >> 1) & 7)) * 16);
  u32x4 z[3][7];
  f32x4 acc[7][4];
#pragma unroll
  for (int n = 0; n < 7; ++n)
#pragma unroll
    for (int m = 0; m < 4; ++m) acc[n][m] = (f32x4){0.f, 0.f, 0.f, 0.f};
#pragma unroll
  for (int k = 0; k < 8; ++k) w2_piece(w2, wave, 1, k);
  {
    const int tj0 = (112 * J) >> 7, tj1 = (112 * J + 111) >> 7;
    poll_flags(wsr, WS_FA + ((lane >> 1) * 8 + ((lane & 1) ? tj1 : tj0)) * 4, tag);
  }
  STAMP(1, 0);
#define B_ISSUE_Z(it) _Pragma("unroll") for (int n = 0; n < 7; ++n) bload16(z[(it) % 3][n], zs, voffz, n * 65536 + (it) * 1024)
  B_ISSUE_Z(0); B_ISSUE_Z(1); B_ISSUE_Z(2);
#undef B_ISSUE_Z
  const int tt = 4 * T + wave;
  u32x2 z1v[7];
  unsigned bsw;
#define B_WAITZ(N) asm volatile("s_waitcnt vmcnt(" #N ")" : "+v"(zc[0]), "+v"(zc[1]), "+v"(zc[2]), "+v"(zc[3]), "+v"(zc[4]), "+v"(zc[5]), "+v"(zc[6])::"memory")
#pragma unroll
  for (int it = 0; it < 16; ++it) {
    u32x4(&zc)[7] = z[it % 3];
    if (it <= 1) B_WAITZ(14); else if (it <= 13) B_WAITZ(22); else if (it == 14) B_WAITZ(15); else B_WAITZ(8);
    s16x8 wf[4];
#pragma unroll
    for (int m = 0; m < 4; ++m) wf[m] = *(LAS const s16x8*)(((it & 1) ? wrd1 : wrd0) + ((it >> 1) & 1) * 8192 + m * 2048);
#pragma unroll
    for (int n = 0; n < 7; ++n) {
#pragma unroll
      for (int m = 0; m < 4; ++m) acc[n][m] = mfma_bf(__builtin_bit_cast(s16x8, zc[n]), wf[m], acc[n][m]);
      __builtin_amdgcn_sched_barrier(0);
      if (it + 3 < 16) {
        bload16(zc[n], zs, voffz, n * 65536 + (it + 3) * 1024);
        if ((it & 1) && n >= 1 && n <= 4) { w2_piece(w2, wave, (it + 3) >> 1, 2 * (n - 1)); w2_piece(w2, wave, (it + 3) >> 1, 2 * (n - 1) + 1); }
      }
      if (it == 13) {
        const unsigned vo = WS_Z1D + (tt * 56 + 7 * J + n) * 512 + lane * 8;
        asm volatile("buffer_load_dwordx2 %0, %1, %2, 0 offen sc1" : "=v"(z1v[n]) : "v"(vo), "s"(wsr) : "memory");
        if (n == 6) {
          const unsigned vb = (unsigned)(16 * tt + (lane & 15)) * 2u;
          asm volatile("global_load_ushort %0, %1, %2" : "=v"(bsw) : "v"(vb), "s"(p.b_sp) : "memory");
        }
      }
      __builtin_amdgcn_sched_barrier(0);
    }
  }
#undef B_WAITZ
  STAMP(1, 1);
  asm volatile("s_waitcnt vmcnt(0)" : "+v"(z1v[0]), "+v"(z1v[1]), "+v"(z1v[2]), "+v"(z1v[3]), "+v"(z1v[4]), "+v"(z1v[5]), "+v"(z1v[6]), "+v"(bsw)::"memory");
  const float bs = bf2f((unsigned short)bsw);
  if (wave == 0) b_epi<0>(smem, wsr, acc, z1v, bs, lane, tt, J);
  else if (wave == 1) b_epi<1>(smem, wsr, acc, z1v, bs, lane, tt, J);
  else if (wave == 2) b_epi<2>(smem, wsr, acc, z1v, bs, lane, tt, J);
  else b_epi<3>(smem, wsr, acc, z1v, bs, lane, tt, J);
  STAMP(1, 2);
}

__device__ __forceinline__ f32x2 cmul(f32x2 a, f32x2 b) {
  f32x2 t, r;
  asm("v_pk_mul_f32 %0, %1, %2 op_sel:[0,0] op_sel_hi:[0,1]" : "=v"(t) : "v"(a), "v"(b));
  asm("v_pk_fma_f32 %0, %1, %2, %3 op_sel:[1,1,0] op_sel_hi:[1,0,1] neg_lo:[1,0,0]" : "=v"(r) : "v"(a), "v"(b), "v"(t));
  return r;
}
__device__ __forceinline__ unsigned hmul(unsigned a, unsigned b) {
  unsigned t, r;
  asm("v_pk_mul_f16 %0, %1, %2 op_sel:[0,0] op_sel_hi:[0,1]" : "=v"(t) : "v"(a), "v"(b));
  asm("v_pk_fma_f16 %0, %1, %2, %3 op_sel:[1,1,0] op_sel_hi:[1,0,1] neg_lo:[1,0,0]" : "=v"(r) : "v"(a), "v"(b), "v"(t));
  return r;
}
__device__ __forceinline__ unsigned hcv(f32x2 v) { return pk_f16(v.x, v.y); }
__device__ __forceinline__ f32x2 cexp_p(float al, float be, float pp) {
  const float e = __builtin_amdgcn_exp2f(al * pp), th = __builtin_amdgcn_fractf(be * pp);
  return (f32x2){__builtin_amdgcn_cosf(th), __builtin_amdgcn_sinf(th)} * e;
}
struct PosTab { unsigned short v[2064]; };
constexpr int pos_bf16(int j) {
  if (j < 256) return j;
  int sh = 1; while ((j >> sh) >= 256) ++sh;
  const int q = j >> sh, r = j & ((1 << sh) - 1), h = 1 << (sh - 1);
  return (r > h || (r == h && (q & 1)) ? q + 1 : q) << sh;
}
constexpr PosTab make_postab() {
  PosTab t = {};
  for (int x = 0; x < 2048; ++x) {
    const int p = pos_bf16(2047 - x);
    int col = 0, a = 0;
    if (p < 256) { a = p >> 4; col = p & 15; }
    else if (p < 512) { const int r = p - 256; a = r >> 4; col = 16 + ((r & 15) >> 1); }
    else if (p < 1024) { const int r = p - 512; a = (r & 255) >> 4; col = 24 + 4 * (r >> 8) + ((r & 15) >> 2); }
    else if (p < 2048) { const int r = p - 1024; a = (r & 255) >> 4; col = 32 + 2 * (r >> 8) + ((r & 15) >> 3); }
    else { a = 0; col = 40; }
    t.v[x] = (unsigned short)((col * 16 + a) * 4);
  }
  for (int x = 2048; x < 2064; ++x) t.v[x] = 48 * 64;
  return t;
}
__device__ const PosTab POSTAB = make_postab();

template <int HW>
__device__ __forceinline__ void s4d_conv(LAS char* cb, rsrc_t wsr, int d, int lane, float iscale, float dsk) {
  constexpr int NF = HW ? 7 : 8;
  f32x4 acc[8] = {};
  const int r = lane & 15, h = lane >> 4;
  LAS const char* ab = cb + CH_FT + ((7 - r) & 7) * CH_CS + (255 - (r >> 3) - 2 * (h >> 1) + (h & 1)) * 16 - (64 * 7 + 512 * 7);
  LAS const char* bb = cb + CH_U + 512 + 32 * r - 32 * (h >> 1) + 16 * (h & 1) - 64 * 7;
  u32x4 A[2][8], B[2][8];
#define CONV_LOAD(ar, buf)                                                                                   \
  _Pragma("unroll") for (int k = 0; k < NF; ++k) {                                                           \
    B[buf][k] = *(LAS const u32x4*)(bb + 512 * k + 64 * (7 - (ar)));                                         \
    A[buf][k] = *(LAS const u32x4*)(ab + 512 * (7 - k) + 64 * (7 - (ar)));                                   \
  }
  CONV_LOAD(0, 0);
#pragma unroll
  for (int ar = 0; ar < 8; ++ar) {
    __builtin_amdgcn_sched_barrier(0);
    if (ar < 7) { CONV_LOAD(ar + 1, (ar + 1) & 1); }
#pragma unroll
    for (int al = 0; al < NF; ++al)
#pragma unroll
      for (int be = 0; be < NF; ++be) {
        const int tau = al + be;
        if (HW ? (tau >= 4 && tau <= 6) : (tau <= 3 || tau == 7)) acc[tau] = mfma_h(A[ar & 1][al], B[ar & 1][be], acc[tau]);
      }
#pragma unroll
    for (int i = 0; i < 16; ++i) { __builtin_amdgcn_sched_group_barrier(0x008, 1, 0); __builtin_amdgcn_sched_group_barrier(0x100, 1, 0); }
    __builtin_amdgcn_sched_barrier(0);
  }
#undef CONV_LOAD
  const int q = lane >> 4;
#pragma unroll
  for (int t = 0; t < 8; ++t) {
    if (HW ? (t >= 4 && t <= 6) : (t <= 3 || t == 7)) {
      const f32x4 v = acc[t];
      const int l0 = 256 * t + 16 * r + 4 * q;
      const u32x2 uw = *(LAS const u32x2*)(cb + CH_U + 512 + l0 * 2);
      unsigned o[2];
#pragma unroll
      for (int pp = 0; pp < 2; ++pp) {
        const unsigned uwp = uw[pp];
        const f16x2 ua = __builtin_bit_cast(f16x2, uwp);
        const f32x2 y2 = up_bf(rn_pair((f32x2){v[2 * pp], v[2 * pp + 1]} * iscale)), k2 = up_bf(rn_pair((f32x2){(float)ua[0], (float)ua[1]} * dsk));
        o[pp] = rn_pair(y2 + k2);
      }
      st8(wsr, WS_ST + (d * SEQ + l0) * 2, (u32x2){o[0], o[1]});
    }
  }
}

__device__ __forceinline__ void phaseB_s4d(LAS char* cb, const Params& p, rsrc_t wsr, unsigned tag, int d, int hw, int lane) {
  char* ws = p.ws;
  LAS char* cnt = cb + CH_CNT;
  unsigned la_[2], ai_[2], cr_[4], ci_[4], ldt_, dsk_;
  u32x4 pt0, pt1, pt2;
  {
    const unsigned vo = (unsigned)(d * NST + 128 * hw + lane) * 2u, vo2 = (unsigned)(d * NST + ((128 * hw + 128 + lane) & 255)) * 2u, vd = (unsigned)d * 2u;
    asm volatile("global_load_ushort %0, %1, %2" : "=v"(la_[0]) : "v"(vo), "s"(p.logA) : "memory");
    asm volatile("global_load_ushort %0, %1, %2 offset:128" : "=v"(la_[1]) : "v"(vo), "s"(p.logA) : "memory");
    asm volatile("global_load_ushort %0, %1, %2" : "=v"(ai_[0]) : "v"(vo), "s"(p.Aim) : "memory");
    asm volatile("global_load_ushort %0, %1, %2 offset:128" : "=v"(ai_[1]) : "v"(vo), "s"(p.Aim) : "memory");
    asm volatile("global_load_ushort %0, %1, %2" : "=v"(cr_[0]) : "v"(vo), "s"(p.Cre) : "memory");
    asm volatile("global_load_ushort %0, %1, %2 offset:128" : "=v"(cr_[1]) : "v"(vo), "s"(p.Cre) : "memory");
    asm volatile("global_load_ushort %0, %1, %2" : "=v"(cr_[2]) : "v"(vo2), "s"(p.Cre) : "memory");
    asm volatile("global_load_ushort %0, %1, %2 offset:128" : "=v"(cr_[3]) : "v"(vo2), "s"(p.Cre) : "memory");
    asm volatile("global_load_ushort %0, %1, %2" : "=v"(ci_[0]) : "v"(vo), "s"(p.Cim) : "memory");
    asm volatile("global_load_ushort %0, %1, %2 offset:128" : "=v"(ci_[1]) : "v"(vo), "s"(p.Cim) : "memory");
    asm volatile("global_load_ushort %0, %1, %2" : "=v"(ci_[2]) : "v"(vo2), "s"(p.Cim) : "memory");
    asm volatile("global_load_ushort %0, %1, %2 offset:128" : "=v"(ci_[3]) : "v"(vo2), "s"(p.Cim) : "memory");
    asm volatile("global_load_ushort %0, %1, %2" : "=v"(ldt_) : "v"(vd), "s"(p.logdt) : "memory");
    asm volatile("global_load_ushort %0, %1, %2" : "=v"(dsk_) : "v"(vd), "s"(p.Dsk) : "memory");
    const char* pb = (const char*)POSTAB.v + hw * 2048;
    const unsigned vp = lane * 32;
    asm volatile("global_load_dwordx4 %0, %1, %2" : "=v"(pt0) : "v"(vp), "s"(pb) : "memory");
    asm volatile("global_load_dwordx4 %0, %1, %2 offset:16" : "=v"(pt1) : "v"(vp), "s"(pb) : "memory");
    asm volatile("global_load_dwordx4 %0, %1, %2 offset:32" : "=v"(pt2) : "v"(vp), "s"(pb) : "memory");
    if (hw == 0 && lane < 32) *(LAS u32x4*)(cb + CH_U + lane * 16) = (u32x4){0u, 0u, 0u, 0u};
    if (lane < 4) *(LAS u32x4*)(cb + CH_T + hw * CH_TS + 48 * 64 + lane * 16) = (u32x4){0u, 0u, 0u, 0u};
  }
  BAR();
  __builtin_amdgcn_s_setprio(3);
  asm volatile("s_waitcnt vmcnt(0)" : "+v"(la_[0]), "+v"(la_[1]), "+v"(ai_[0]), "+v"(ai_[1]), "+v"(cr_[0]), "+v"(cr_[1]), "+v"(cr_[2]), "+v"(cr_[3]), "+v"(ci_[0]), "+v"(ci_[1]),
               "+v"(ci_[2]), "+v"(ci_[3]), "+v"(ldt_), "+v"(dsk_), "+v"(pt0), "+v"(pt1), "+v"(pt2)::"memory");
  STAMP(1, 0);
  float cr[4], ci[4];
#pragma unroll
  for (int k = 0; k < 4; ++k) { cr[k] = bf2f((unsigned short)cr_[k]); ci[k] = bf2f((unsigned short)ci_[k]); }
  const float dt = rbf(__builtin_amdgcn_exp2f(bf2f((unsigned short)ldt_) * 1.4426950408889634f)), dsk = bf2f((unsigned short)dsk_);
  float sabs = 0.f;
#pragma unroll
  for (int k = 0; k < 4; ++k) sabs += __builtin_fabsf(cr[k]) + __builtin_fabsf(ci[k]);
  sabs = xsum16_32(red16(sabs));
  sabs *= 2.0f * dt;
  int ex = (int)((__float_as_uint(sabs) >> 23) & 0xff) - 126;
  ex = ex < -100 ? -100 : (ex > 100 ? 100 : ex);
  const float scale = __uint_as_float((unsigned)(127 + 15 - ex) << 23), iscale = __uint_as_float((unsigned)(127 - 15 + ex) << 23);
  f32x4 kacc[3] = {};
  LAS char* fw = cb + CH_FT + hw * CH_FW;
#pragma unroll
  for (int ps = 0; ps < 2; ++ps) {
    const float lap = bf2f((unsigned short)la_[ps]), aip = bf2f((unsigned short)ai_[ps]);
    const float crp = cr[ps], cip = ci[ps];
    const float lre = -rbf(__builtin_amdgcn_exp2f(lap * 1.4426950408889634f)) * dt, lim = aip * dt;
    const float al = lre * 1.4426950408889634f, be = lim * 0.15915494309189535f;
    LAS char* wp = fw + lane * 4;
#define PUTH(row, v) *(LAS unsigned*)(wp + (row) * CH_FS) = (v)
    {
      const f32x2 w = cexp_p(al, be, 16.f);
      const float cs = 2.0f * dt * scale;
      const f32x2 c0 = {crp * cs, cip * cs};
      const f32x2 w2 = cmul(w, w), c1 = cmul(c0, w), w4 = cmul(w2, w2), c2 = cmul(c0, w2), c3 = cmul(c1, w2), w8 = cmul(w4, w4), w12 = cmul(w8, w4);
      const unsigned hc[4] = {hcv(c0), hcv(c1), hcv(c2), hcv(c3)}, hw4[3] = {hcv(w4), hcv(w8), hcv(w12)};
#pragma unroll
      for (int j = 0; j < 4; ++j) {
        PUTH(j, hc[j]);
#pragma unroll
        for (int k = 0; k < 3; ++k) PUTH(4 * (k + 1) + j, hmul(hc[j], hw4[k]));
      }
    }
    {
      const f32x2 e1 = cexp_p(al, -be, 1.f);
      const f32x2 e2 = cmul(e1, e1), e3 = cmul(e2, e1), e4 = cmul(e2, e2), e8 = cmul(e4, e4), e12 = cmul(e8, e4);
      unsigned E[16];
      E[0] = 0x00003c00u; E[1] = hcv(e1); E[2] = hcv(e2); E[3] = hcv(e3); E[4] = hcv(e4); E[8] = hcv(e8); E[12] = hcv(e12);
#pragma unroll
      for (int m = 1; m < 4; ++m)
#pragma unroll
        for (int j = 1; j < 4; ++j) E[4 * m + j] = hmul(E[4 * m], E[j]);
#pragma unroll
      for (int k = 0; k < 16; ++k) PUTH(16 + k, E[k]);
      const f32x2 z256 = cexp_p(al, -be, 256.f), z512 = cexp_p(al, -be, 512.f), z1024 = cexp_p(al, -be, 1024.f), z2048 = cexp_p(al, -be, 2048.f);
      const f32x2 z768 = cmul(z512, z256);
      const unsigned h256 = hcv(z256), h512 = hcv(z512), h768 = hcv(z768);
      PUTH(32, h256);
#pragma unroll
      for (int k = 1; k < 8; ++k) PUTH(32 + k, hmul(h256, E[2 * k]));
      PUTH(40, h512); PUTH(44, h768);
#pragma unroll
      for (int k = 1; k < 4; ++k) { PUTH(40 + k, hmul(h512, E[4 * k])); PUTH(44 + k, hmul(h768, E[4 * k])); }
      const unsigned hb[4] = {hcv(z1024), hcv(cmul(z1024, z256)), hcv(cmul(z1024, z512)), hcv(cmul(z1024, z768))};
#pragma unroll
      for (int k = 0; k < 4; ++k) { PUTH(48 + 2 * k, hb[k]); PUTH(49 + 2 * k, hmul(hb[k], E[8])); }
      PUTH(56, hcv(z2048));
    }
#undef PUTH
    asm volatile("s_waitcnt lgkmcnt(0)" ::: "memory");
    LAS const char* rp = fw + (lane & 15) * CH_FS + (lane >> 4) * 16;
    u32x4 fa[4], fb[4][3];
#pragma unroll
    for (int kk = 0; kk < 4; ++kk) {
      fa[kk] = *(LAS const u32x4*)(rp + kk * 64);
#pragma unroll
      for (int t = 0; t < 3; ++t) fb[kk][t] = *(LAS const u32x4*)(rp + (16 + 16 * t) * CH_FS + kk * 64);
    }
#pragma unroll
    for (int kk = 0; kk < 4; ++kk)
#pragma unroll
      for (int t = 0; t < 3; ++t) kacc[t] = mfma_h(fa[kk], fb[kk][t], kacc[t]);
    asm volatile("" ::: "memory");
  }
  STAMP(1, 1);
  u32x4 u0, u1;
  poll_flags(wsr, WS_FA + ((lane & 31) * 8 + 7) * 4, tag);
  {
    const unsigned vu = WS_XT + d * SEQ * 2 + hw * 2048 + lane * 16;
    asm volatile("buffer_load_dwordx4 %0, %1, %2, 0 offen sc1" : "=v"(u0) : "v"(vu), "s"(wsr) : "memory");
    asm volatile("buffer_load_dwordx4 %0, %1, %2, 0 offen offset:1024 sc1" : "=v"(u1) : "v"(vu), "s"(wsr) : "memory");
  }
#pragma unroll
  for (int t = 0; t < 3; ++t) *(LAS f32x4*)(cb + CH_T + hw * CH_TS + (16 * t + (lane & 15)) * 64 + (lane >> 4) * 16) = kacc[t];
  lds_sync(cnt, 2, lane);
  {
    const int y = 2 * (64 * hw + lane);
    const unsigned po[12] = {pt0[0], pt0[1], pt0[2], pt0[3], pt1[0], pt1[1], pt1[2], pt1[3], pt2[0], pt2[1], pt2[2], pt2[3]};
    unsigned W[12], V[11];
#pragma unroll
    for (int k = 0; k < 12; ++k) {
      LAS const char* a0 = cb + CH_T + (po[k] & 0xffffu);
      LAS const char* a1 = cb + CH_T + (po[k] >> 16);
      W[k] = pk_f16(*(LAS const float*)a0 + *(LAS const float*)(a0 + CH_TS), *(LAS const float*)a1 + *(LAS const float*)(a1 + CH_TS));
    }
#pragma unroll
    for (int i = 0; i < 11; ++i) V[i] = __builtin_amdgcn_alignbit(W[i + 1], W[i], 16);
#pragma unroll
    for (int m = 0; m < 4; ++m) {
      *(LAS u32x4*)(cb + CH_FT + (2 * m) * CH_CS + y * 16) = (u32x4){W[m], W[m + 1], W[m + 2], W[m + 3]};
      *(LAS u32x4*)(cb + CH_FT + (2 * m) * CH_CS + y * 16 + 16) = (u32x4){W[m + 4], W[m + 5], W[m + 6], W[m + 7]};
      *(LAS u32x4*)(cb + CH_FT + (2 * m + 1) * CH_CS + y * 16) = (u32x4){V[m], V[m + 1], V[m + 2], V[m + 3]};
      *(LAS u32x4*)(cb + CH_FT + (2 * m + 1) * CH_CS + y * 16 + 16) = (u32x4){V[m + 4], V[m + 5], V[m + 6], V[m + 7]};
    }
    if (hw == 1 && lane < 8) *(LAS u32x4*)(cb + CH_FT + lane * CH_CS + 256 * 16) = (u32x4){0u, 0u, 0u, 0u};
  }
  asm volatile("s_waitcnt vmcnt(0)" : "+v"(u0), "+v"(u1)::"memory");
  *(LAS u32x4*)(cb + CH_U + 512 + hw * 2048 + lane * 16) = u0;
  *(LAS u32x4*)(cb + CH_U + 512 + hw * 2048 + 1024 + lane * 16) = u1;
  lds_sync(cnt, 4, lane);
  STAMP(1, 2);
  if (hw) s4d_conv<1>(cb, wsr, d, lane, iscale, dsk); else s4d_conv<0>(cb, wsr, d, lane, iscale, dsk);
  __builtin_amdgcn_s_setprio(0);
  STAMP(1, 3);
}

constexpr int C_PART = 0;
constexpr int C_XS = 65536;
constexpr int C_SS = C_XS + 64 * 136;
constexpr int C_OS = C_SS + 64 * 136;

__device__ __forceinline__ void phaseC(LAS char* smem, const Params& p, rsrc_t wsr, unsigned tag, int T, int Dt, int tid) {
  char* ws = p.ws;
  const int lane = tid & 63;
  const int wave = __builtin_amdgcn_readfirstlane(tid >> 6);
  unsigned fo;
  {
    const int J = lane & 7, bh = 2 * (T >> 3) + (J >> 2) + 8 * ((T & 7) + 8 * (J & 3));
    const int bs = 32 * Dt + ((lane - 8) & 31);
    fo = WS_FB + (lane < 8 ? bh : bs) * 4;
  }
  if (wave < 4) {
    const unsigned ho = WS_HF + ((4 * T * 28 + 7 * wave) * 64 + lane) * 16, wo = WS_WOF + ((4 * Dt * 28 + 7 * wave) * 64 + lane) * 16;
    u32x4 hf[7][4], wf[7][4];
    f32x4 acc[4][4];
#pragma unroll
    for (int k = 0; k < 7; ++k)
#pragma unroll
      for (int m = 0; m < 4; ++m) wf[k][m] = ld16(wsr, wo + (m * 28 + k) * 1024);
    __builtin_amdgcn_sched_barrier(0);
    poll_flags(wsr, fo, tag);
    STAMP(2, 0);
#pragma unroll
    for (int n = 0; n < 4; ++n)
#pragma unroll
      for (int m = 0; m < 4; ++m) acc[n][m] = (f32x4){0.f, 0.f, 0.f, 0.f};
#pragma unroll
    for (int k = 0; k < 4; ++k)
#pragma unroll
      for (int m = 0; m < 4; ++m) hf[k][m] = ld16(wsr, ho + (m * 28 + k) * 1024);
    __builtin_amdgcn_sched_barrier(0);
#pragma unroll
    for (int k = 0; k < 7; ++k) {
#pragma unroll
      for (int n = 0; n < 4; ++n)
#pragma unroll
        for (int m = 0; m < 4; ++m) acc[n][m] = mfma_bf(__builtin_bit_cast(s16x8, wf[k][n]), __builtin_bit_cast(s16x8, hf[k][m]), acc[n][m]);
      __builtin_amdgcn_sched_barrier(0);
      if (k + 4 < 7) {
#pragma unroll
        for (int m = 0; m < 4; ++m) hf[k + 4][m] = ld16(wsr, ho + (m * 28 + k + 4) * 1024);
      }
      __builtin_amdgcn_sched_barrier(0);
    }
    STAMP(2, 1);
#pragma unroll
    for (int n = 0; n < 4; ++n)
#pragma unroll
      for (int m = 0; m < 4; ++m) *(LAS f32x4*)(smem + C_PART + ((wave * 4 + m) * 4 + n) * 1024 + lane * 16) = acc[n][m];
  } else {
    const int u = tid - 256, row = u >> 2, c = u & 3;
    const u32x4 x0 = *(const u32x4*)(p.x + (size_t)(64 * T + row) * DM + 64 * Dt + 16 * c);
    const u32x4 x1 = *(const u32x4*)(p.x + (size_t)(64 * T + row) * DM + 64 * Dt + 16 * c + 8);
#pragma unroll
    for (int e = 0; e < 2; ++e) {
      *(LAS u32x2*)(smem + C_XS + row * 136 + c * 32 + e * 8) = (u32x2){x0[2 * e], x0[2 * e + 1]};
      *(LAS u32x2*)(smem + C_XS + row * 136 + c * 32 + 16 + e * 8) = (u32x2){x1[2 * e], x1[2 * e + 1]};
    }
    poll_flags(wsr, fo, tag);
    const unsigned so = WS_ST + ((64 * Dt + row) * SEQ + 64 * T + 16 * c) * 2;
    const u32x4 s0 = ld16(wsr, so), s1 = ld16(wsr, so + 16);
#pragma unroll
    for (int e = 0; e < 2; ++e) {
      *(LAS u32x2*)(smem + C_SS + row * 136 + c * 32 + e * 8) = (u32x2){s0[2 * e], s0[2 * e + 1]};
      *(LAS u32x2*)(smem + C_SS + row * 136 + c * 32 + 16 + e * 8) = (u32x2){s1[2 * e], s1[2 * e + 1]};
    }
  }
  BAR();
  {
    const int m = wave & 3, l15 = lane & 15, q = lane >> 4, t = 16 * m + l15;
#pragma unroll
    for (int nn = 0; nn < 2; ++nn) {
      const int n = 2 * (wave >> 2) + nn;
      f32x4 o = *(LAS const f32x4*)(smem + C_PART + ((0 * 4 + m) * 4 + n) * 1024 + lane * 16);
#pragma unroll
      for (int sw = 1; sw < 4; ++sw) o += *(LAS const f32x4*)(smem + C_PART + ((sw * 4 + m) * 4 + n) * 1024 + lane * 16);
      const s16x4 sv = tr_read(smem + C_SS + (16 * n + 4 * q + (l15 >> 2)) * 136 + (16 * m + 4 * (lane & 3)) * 2);
      const u32x2 sw2 = __builtin_bit_cast(u32x2, sv);
      const u32x2 xw = *(LAS const u32x2*)(smem + C_XS + t * 136 + (16 * n + 4 * q) * 2);
      unsigned ow[2];
#pragma unroll
      for (int pp = 0; pp < 2; ++pp) {
        const f32x2 v = up_bf(rn_pair((f32x2){o[2 * pp], o[2 * pp + 1]}));
        const f32x2 e = v * -1.4426950408889634f;
        const f32x2 dd = (f32x2){__builtin_amdgcn_exp2f(e.x), __builtin_amdgcn_exp2f(e.y)} + 1.0f;
        const f32x2 g = up_bf(rn_pair(v * (f32x2){__builtin_amdgcn_rcpf(dd.x), __builtin_amdgcn_rcpf(dd.y)}));
        const unsigned s_ = sw2[pp], x_ = xw[pp];
        ow[pp] = rn_pair(up_bf(x_) + up_bf(rn_pair(up_bf(s_) * g)));
      }
      *(LAS u32x2*)(smem + C_OS + t * 144 + (16 * n + 4 * q) * 2) = (u32x2){ow[0], ow[1]};
    }
  }
  BAR();
  {
    const int row = tid >> 3, c = tid & 7;
    *(u32x4*)(p.out + (size_t)(64 * T + row) * DM + 64 * Dt + 8 * c) = *(LAS const u32x4*)(smem + C_OS + row * 144 + c * 16);
  }
}

__global__ __launch_bounds__(512, 2) void mixer_block(Params p) {
  extern __shared__ __attribute__((aligned(16))) char smem_raw[];
  LAS char* smem = (LAS char*)smem_raw;
  char* ws = p.ws;
  const int tid = threadIdx.x, lane = tid & 63;
  const int wave = __builtin_amdgcn_readfirstlane(tid >> 6);
  const int b = blockIdx.x, xcd = b & 7, rr = b >> 3;
  const rsrc_t wsr = mk_rsrc(ws, WS_END);
  STAMP(0, 0);
  const unsigned ep = __hip_atomic_load((GAS unsigned*)(ws + WS_EP), __ATOMIC_RELAXED, __HIP_MEMORY_SCOPE_AGENT);
  unsigned tag;
  const int T2 = 8 * (xcd >> 1) + (rr & 7), J2 = 4 * (xcd & 1) + (rr >> 3);
  int fa;
  if (b < 224) {
    const int ti = 4 * xcd + rr / 7, tj = rr % 7;
    tag = phaseA_gemm(smem, p, wsr, ep, b, ti, tj, T2, tid);
    fa = ti * 8 + tj;
  } else {
    tag = phaseA_aux(smem, p, wsr, ep, b - 224, T2, tid);
    fa = (b - 224) * 8 + 7;
  }
  WAIT_VM(0);
  BAR();
  if (tid == 0) st4(wsr, WS_FA + fa * 4, tag);
  STAMP(0, 5);
  if (tid < 64) {
    if (lane < 32) *(LAS unsigned*)(smem + B_CH + CH_CNT + lane * 4) = 0u;
    else *(LAS unsigned*)(smem + B_CH + B_CHS + CH_CNT + (lane - 32) * 4) = 0u;
  }
  if (wave < 4) {
    BAR();
    phaseB_gemm(smem, p, wsr, tag, wave, lane, T2, J2);
  } else if (b < 224) {
    const int sw = wave - 4;
    phaseB_s4d(smem + B_CH + (sw >> 1) * B_CHS, p, wsr, tag, 2 * b + (sw >> 1), sw & 1, lane);
  } else {
    BAR();
    if (b == 255 && wave == 4) {
#pragma unroll
      for (int k = 0; k < 4; ++k) poll_flags(wsr, WS_FA + (64 * k + lane) * 4, tag);
      if (lane == 0) st4(wsr, WS_EP, tag);
    }
  }
  WAIT_VM(0);
  BAR();
  if (tid == 0) st4(wsr, WS_FB + b * 4, tag);
  STAMP(1, 4);
  if (b < 224) phaseC(smem, p, wsr, tag, 4 * xcd + rr / 7, rr % 7, tid);
  STAMP(2, 2);
}

extern "C" void kernel_launch(void* const* d_in, const int* in_sizes, int n_in, void* d_out, int out_size, void* d_ws, size_t ws_size, hipStream_t stream) {
  Params p;
  p.x = (const unsigned short*)d_in[0];
  p.logA = (const unsigned short*)d_in[1];
  p.Aim = (const unsigned short*)d_in[2];
  p.Cre = (const unsigned short*)d_in[3];
  p.Cim = (const unsigned short*)d_in[4];
  p.logdt = (const unsigned short*)d_in[5];
  p.Dsk = (const unsigned short*)d_in[6];
  p.w_in = (const unsigned short*)d_in[8];
  p.w_sp = (const unsigned short*)d_in[9];
  p.b_sp = (const unsigned short*)d_in[10];
  p.w_out = (const unsigned short*)d_in[11];
  p.ws = (char*)d_ws;
  p.out = (unsigned short*)d_out;
  (void)hipFuncSetAttribute((const void*)mixer_block, hipFuncAttributeMaxDynamicSharedMemorySize, 163840);
  mixer_block<<<256, 512, 163840, stream>>>(p);
}
```

```cpp
#include <hip/hip_runtime.h>
#include <hip/hip_bf16.h>
#include <stdint.h>

#define LAS __attribute__((address_space(3)))
#define GAS __attribute__((address_space(1)))
typedef float f32x2 __attribute__((ext_vector_type(2)));
typedef float f32x4 __attribute__((ext_vector_type(4)));
typedef float f32x16 __attribute__((ext_vector_type(16)));
typedef unsigned u32x2 __attribute__((ext_vector_type(2)));
typedef unsigned u32x4 __attribute__((ext_vector_type(4)));
typedef short s16x4 __attribute__((ext_vector_type(4)));
typedef short s16x8 __attribute__((ext_vector_type(8)));
typedef __bf16 bf16x2 __attribute__((ext_vector_type(2)));
typedef __bf16 bf16x8 __attribute__((ext_vector_type(8)));
typedef _Float16 f16x2 __attribute__((ext_vector_type(2)));
typedef _Float16 f16x8 __attribute__((ext_vector_type(8)));

constexpr int SEQ = 2048, DM = 448, FH = 896, NST = 256;

constexpr unsigned WS_Z1D = 0;
constexpr unsigned WS_Z2F = WS_Z1D + SEQ * FH * 2;
constexpr unsigned WS_HF  = WS_Z2F + SEQ * FH * 2;
constexpr unsigned WS_XT  = WS_HF + SEQ * FH * 2;
constexpr unsigned WS_ST  = WS_XT + SEQ * DM * 2;
constexpr unsigned WS_WOF = WS_ST + SEQ * DM * 2;
constexpr unsigned WS_XCH = WS_WOF + FH * DM * 2;
constexpr unsigned WS_XS1 = WS_XCH + 32 * 64 * 8 * 16;
constexpr unsigned WS_FA  = WS_XS1 + 32 * 1024;
constexpr unsigned WS_FB  = WS_FA + 1024;
constexpr unsigned WS_EP  = WS_FB + 1024;
constexpr unsigned WS_END = WS_EP + 256;

constexpr size_t WS_DBG = 32u << 20;
#define STAMP(kern, slot) do {} while (0)

constexpr unsigned ONES_BF = 0x3f803f80u;
constexpr int AUX_SC1 = 16;
__device__ __forceinline__ float bf2f(unsigned short h) { return __uint_as_float((unsigned)h << 16); }
__device__ __forceinline__ float bflo(unsigned w) { return __uint_as_float(w << 16); }
__device__ __forceinline__ float bfhi(unsigned w) { return __uint_as_float(w & 0xffff0000u); }
__device__ __forceinline__ float rbf(float x) { return (float)(__bf16)x; }
__device__ __forceinline__ unsigned pk_f16(float lo, float hi) {
  f16x2 v = {(_Float16)lo, (_Float16)hi};
  return __builtin_bit_cast(unsigned, v);
}
__device__ __forceinline__ f32x2 up_bf(unsigned w) { return (f32x2){bflo(w), bfhi(w)}; }
__device__ __forceinline__ unsigned rn_pair(f32x2 v) {
  unsigned r;
  asm("v_cvt_pk_bf16_f32 %0, %1, %2" : "=v"(r) : "v"(v.x), "v"(v.y));
  return r;
}
template <int CTRL> __device__ __forceinline__ float dpp_add(float v) {
  int t = __builtin_amdgcn_update_dpp(0, __float_as_int(v), CTRL, 0xf, 0xf, true);
  return v + __int_as_float(t);
}
__device__ __forceinline__ float red8(float v) {
  v = dpp_add<0xB1>(v); v = dpp_add<0x4E>(v); v = dpp_add<0x141>(v);
  return v;
}
__device__ __forceinline__ float red16(float v) { return dpp_add<0x140>(red8(v)); }
__device__ __forceinline__ float xsum32(float v) {
  auto b = __builtin_amdgcn_permlane32_swap(__float_as_uint(v), __float_as_uint(v), false, false);
  return __uint_as_float(b[0]) + __uint_as_float(b[1]);
}
__device__ __forceinline__ float xsum16_32(float v) {
  auto a = __builtin_amdgcn_permlane16_swap(__float_as_uint(v), __float_as_uint(v), false, false);
  return xsum32(__uint_as_float(a[0]) + __uint_as_float(a[1]));
}
__device__ __forceinline__ void glds16(const void* sbase, unsigned voff, unsigned lds) {
  asm volatile("s_mov_b32 m0, %2\n\ts_nop 0\n\tglobal_load_lds_dwordx4 %0, %1" ::"v"(voff), "s"(sbase), "s"(lds) : "memory");
}
#define WAIT_VM(n) asm volatile("s_waitcnt vmcnt(%0)" ::"n"(n) : "memory")
#define BAR() do { asm volatile("s_waitcnt lgkmcnt(0)" ::: "memory"); __builtin_amdgcn_s_barrier(); asm volatile("" ::: "memory"); } while (0)
__device__ __forceinline__ s16x4 tr_read(LAS const char* p) {
  typedef short v4i16 __attribute__((ext_vector_type(4)));
  return __builtin_amdgcn_ds_read_tr16_b64_v4i16((LAS v4i16*)p);
}
__device__ __forceinline__ f32x4 mfma_bf(s16x8 a, s16x8 b, f32x4 c) {
  return __builtin_amdgcn_mfma_f32_16x16x32_bf16(__builtin_bit_cast(bf16x8, a), __builtin_bit_cast(bf16x8, b), c, 0, 0, 0);
}
__device__ __forceinline__ f32x16 mfma32_bf(s16x8 a, s16x8 b, f32x16 c) {
  return __builtin_amdgcn_mfma_f32_32x32x16_bf16(__builtin_bit_cast(bf16x8, a), __builtin_bit_cast(bf16x8, b), c, 0, 0, 0);
}
__device__ __forceinline__ f32x4 mfma_h(u32x4 a, u32x4 b, f32x4 c) {
  return __builtin_amdgcn_mfma_f32_16x16x32_f16(__builtin_bit_cast(f16x8, a), __builtin_bit_cast(f16x8, b), c, 0, 0, 0);
}
__device__ __forceinline__ float dot2bf(unsigned a, unsigned b, float c) {
  return __builtin_amdgcn_fdot2_f32_bf16(__builtin_bit_cast(bf16x2, a), __builtin_bit_cast(bf16x2, b), c, false);
}
__device__ __forceinline__ __amdgpu_buffer_rsrc_t mk_rsrc(const void* p, unsigned bytes) {
  return __builtin_amdgcn_make_buffer_rsrc((void*)p, (short)0, (int)bytes, 0x00020000);
}
typedef __amdgpu_buffer_rsrc_t rsrc_t;
__device__ __forceinline__ void st16(rsrc_t r, unsigned off, u32x4 v) { __builtin_amdgcn_raw_buffer_store_b128(v, r, off, 0, AUX_SC1); }
__device__ __forceinline__ void st8(rsrc_t r, unsigned off, u32x2 v) { __builtin_amdgcn_raw_buffer_store_b64(v, r, off, 0, AUX_SC1); }
__device__ __forceinline__ void st4(rsrc_t r, unsigned off, unsigned v) { __builtin_amdgcn_raw_buffer_store_b32(v, r, off, 0, AUX_SC1); }
__device__ __forceinline__ u32x4 ld16(rsrc_t r, unsigned off) { return __builtin_amdgcn_raw_buffer_load_b128(r, off, 0, AUX_SC1); }
__device__ __forceinline__ void bglds16(rsrc_t rs, unsigned voff, unsigned soff, unsigned lds) {
  asm volatile("s_mov_b32 m0, %3\n\ts_nop 0\n\tbuffer_load_dwordx4 %0, %1, %2 offen lds" ::"v"(voff), "s"(rs), "s"(soff), "s"(lds) : "memory");
}
__device__ __forceinline__ void bload16(u32x4& dst, rsrc_t rs, unsigned voff, unsigned soff) {
  asm volatile("buffer_load_dwordx4 %0, %1, %2, %3 offen sc1" : "=v"(dst) : "v"(voff), "s"(rs), "s"(soff) : "memory");
}
__device__ __forceinline__ void poll_granule(u32x4& gr, rsrc_t rs, unsigned voff, unsigned tag) {
  for (unsigned spins = 0;; ++spins) {
    const bool ok = (gr[1] == tag) && (gr[3] == tag);
    if (__all(ok) || spins > (1u << 20)) break;
    __builtin_amdgcn_s_sleep(1);
    asm volatile("buffer_load_dwordx4 %0, %1, %2, 0 offen sc1\n\ts_waitcnt vmcnt(0)" : "=&v"(gr) : "v"(voff), "s"(rs) : "memory");
  }
}
__device__ __forceinline__ void poll_flags(rsrc_t rs, unsigned voff, unsigned tag) {
  for (unsigned spins = 0;; ++spins) {
    unsigned f;
    asm volatile("buffer_load_dword %0, %1, %2, 0 offen sc1\n\ts_waitcnt vmcnt(0)" : "=&v"(f) : "v"(voff), "s"(rs) : "memory");
    if (__all(f == tag) || spins > (1u << 20)) break;
    __builtin_amdgcn_s_sleep(2);
  }
}
__device__ __forceinline__ void lds_sync(LAS char* cntp, unsigned target, int lane) {
  LAS unsigned* cnt = (LAS unsigned*)cntp;
  asm volatile("s_waitcnt lgkmcnt(0)" ::: "memory");
  if (lane == 0) __hip_atomic_fetch_add(cnt, 1u, __ATOMIC_RELAXED, __HIP_MEMORY_SCOPE_WORKGROUP);
  while ((unsigned)__builtin_amdgcn_readfirstlane(*(volatile LAS unsigned*)cnt) < target) __builtin_amdgcn_s_sleep(1);
  asm volatile("" ::: "memory");
}
template <int V> struct IC { static constexpr int value = V; };
template <int A, int B, class F> __device__ __forceinline__ void static_for(F&& f) {
  if constexpr (A < B) { f(IC<A>{}); static_for<A + 1, B>(f); }
}
__device__ __forceinline__ f32x2 gelu2(f32x2 x) {
  const float c1 = -2.0f * 0.7978845608028654f * 1.4426950408889634f, c2 = c1 * 0.044715f;
  const f32x2 t = x * x;
  const f32x2 a = x * (t * c2 + c1);
  f32x2 d = {__builtin_amdgcn_exp2f(a.x), __builtin_amdgcn_exp2f(a.y)};
  d = d + 1.0f;
  return x * (f32x2){__builtin_amdgcn_rcpf(d.x), __builtin_amdgcn_rcpf(d.y)};
}
__device__ __forceinline__ void ln_scalars(float s, float q, float invn, float& mb, float& rr) {
  const float mean = s * invn;
  float var = q * invn - mean * mean; var = var < 0.f ? 0.f : var;
  mb = rbf(mean);
  rr = __builtin_amdgcn_rcpf(rbf(__builtin_amdgcn_sqrtf(rbf(rbf(var) + 1e-5f))));
}

struct Params {
  const unsigned short *x, *logA, *Aim, *Cre, *Cim, *logdt, *Dsk, *w_in, *w_sp, *b_sp, *w_out;
  char* ws;
  unsigned short* out;
};
#define TLBWARM 1

constexpr int A_X = 0;
constexpr int A_S = 57344;
constexpr int A_P = A_S;
constexpr int A_M2 = A_S + 2048;
constexpr int A_R2 = A_M2 + 256;
constexpr int A_W1 = A_R2 + 256;
constexpr int A_W = 65536;
constexpr int A_SLOT = 16384;
constexpr int B_RING = 0;

struct W2Src { rsrc_t wr; unsigned voff0, voff1; };
__device__ __forceinline__ W2Src w2_src(const Params& p, int T, int wave, int lane) {
  W2Src s;
  s.wr = mk_rsrc((const char*)p.w_sp + (size_t)T * 64 * 4096 + wave * 1024, 64 * 4096 - wave * 1024);
  s.voff0 = (lane >> 3) * 4096 + (((lane & 7) ^ ((lane >> 4) & 3)) * 16);
  s.voff1 = (8 + (lane >> 3)) * 4096 + (((lane & 7) ^ (4 + ((lane >> 4) & 3))) * 16);
  return s;
}
__device__ __forceinline__ void w2_piece(const W2Src& s, int wave, int pair, int piece) {
  bglds16(s.wr, (piece & 1) ? s.voff1 : s.voff0, (unsigned)((piece >> 1) * 65536 + pair * 128), (unsigned)(B_RING + wave * 16384 + (pair & 1) * 8192 + piece * 1024));
}

__device__ __forceinline__ unsigned make_tag(unsigned ep) {
  unsigned t;
  asm volatile("v_add_u32 %0, 1, %1" : "=v"(t) : "v"(ep));
  return t;
}
__device__ __forceinline__ unsigned phaseA_aux(LAS char* smem, const Params& p, rsrc_t wsr, unsigned ep, int v, int T2, int tid) {
  char* ws = p.ws;
  const int row = tid >> 3, c8 = tid & 7, lane = tid & 63;
  const int wave = __builtin_amdgcn_readfirstlane(tid >> 6);
  u32x4 xv[7];
#pragma unroll
  for (int m = 0; m < 7; ++m) xv[m] = *(const u32x4*)(p.x + (size_t)(64 * v + row) * DM + (c8 + 8 * m) * 8);
  unsigned touch;
  {
    const char* a;
    if (tid < 256) a = ws + (size_t)(tid < (int)(WS_END >> 16) ? tid : 0) * 65536;
    else if (tid < 384) a = (const char*)p.w_sp + (size_t)(tid - 256) * 65536;
    else if (tid < 416) a = (const char*)p.w_in + (size_t)(tid - 384 < 24 ? tid - 384 : 0) * 65536;
    else if (tid < 432) a = (const char*)p.w_out + (size_t)(tid - 416 < 12 ? tid - 416 : 0) * 65536;
    else if (tid < 448) a = ((tid >> 2) & 3) == 0 ? (const char*)p.logA + (tid & 3) * 57344 : ((tid >> 2) & 3) == 1 ? (const char*)p.Aim + (tid & 3) * 57344
                          : ((tid >> 2) & 3) == 2 ? (const char*)p.Cre + (tid & 3) * 57344 : (const char*)p.Cim + (tid & 3) * 57344;
    else if (tid < 480) a = (const char*)p.out + (size_t)(tid - 448 < 28 ? tid - 448 : 0) * 65536;
    else a = (const char*)p.x + (size_t)(tid - 480 < 28 ? tid - 480 : 0) * 65536;
    asm volatile("global_load_dword %0, %1, off" : "=v"(touch) : "v"(a) : "memory");
  }
  float s = 0.f, q = 0.f;
#pragma unroll
  for (int m = 0; m < 7; ++m)
#pragma unroll
    for (int e = 0; e < 4; ++e) { const unsigned w = xv[m][e]; s = dot2bf(w, ONES_BF, s); q = dot2bf(w, w, q); }
  s = red8(s); q = red8(q);
  float mb, rr;
  ln_scalars(s, q, 1.0f / 448.0f, mb, rr);
  const unsigned tag = make_tag(ep);
  asm volatile("s_waitcnt vmcnt(0)" : "+v"(touch)::"memory");
  if (c8 == 0) st16(wsr, WS_XS1 + v * 1024 + row * 16, (u32x4){__float_as_uint(mb), tag, __float_as_uint(rr), tag});
  LAS char* tb = smem + A_W;
#pragma unroll
  for (int m = 0; m < 7; ++m)
#pragma unroll
    for (int e = 0; e < 4; ++e) {
      const unsigned w = xv[m][e];
      const f32x2 n3 = up_bf(rn_pair(up_bf(rn_pair(up_bf(w) - mb)) * rr));
      const int d = (c8 + 8 * m) * 8 + 2 * e;
      *(LAS _Float16*)(tb + d * 144 + row * 2) = (_Float16)n3.x;
      *(LAS _Float16*)(tb + (d + 1) * 144 + row * 2) = (_Float16)n3.y;
    }
  BAR();
#pragma unroll
  for (int m = 0; m < 7; ++m) {
    const int d = row + 64 * m;
    st16(wsr, WS_XT + (d * SEQ + 64 * v + 8 * c8) * 2, *(LAS const u32x4*)(tb + d * 144 + c8 * 16));
  }
  for (int fr = v * 8 + wave; fr < 784; fr += 256) {
    const int dt = fr / 28, fb = fr - dt * 28;
    const int dcol = 16 * dt + (lane & 15), qq = lane >> 4;
    unsigned short w[8];
#pragma unroll
    for (int jj = 0; jj < 8; ++jj) w[jj] = p.w_out[(size_t)(32 * fb + 16 * (jj >> 2) + 4 * qq + (jj & 3)) * DM + dcol];
    st16(wsr, WS_WOF + (fr * 64 + lane) * 16,
         (u32x4){(unsigned)w[0] | ((unsigned)w[1] << 16), (unsigned)w[2] | ((unsigned)w[3] << 16), (unsigned)w[4] | ((unsigned)w[5] << 16), (unsigned)w[6] | ((unsigned)w[7] << 16)});
  }
  if (wave < 4) {
    const W2Src w2 = w2_src(p, T2, wave, lane);
#pragma unroll
    for (int k = 0; k < 8; ++k) w2_piece(w2, wave, 0, k);
  }
  return tag;
}

__device__ __forceinline__ unsigned phaseA_gemm(LAS char* smem, const Params& p, rsrc_t wsr, unsigned ep, int b, int ti, int tj, int T2, int tid) {
  char* ws = p.ws;
  const int lane = tid & 63;
  const int wave = __builtin_amdgcn_readfirstlane(tid >> 6);
  const int rh = wave >> 2, cg = wave & 3, h = lane >> 5, l31 = lane & 31;
  const int xrow = 8 * wave + (lane >> 3);
  const unsigned voffx = (unsigned)(xrow * 896 + (((lane & 7) ^ ((xrow >> 1) & 7)) * 16));
  const char* xsrc = (const char*)p.x + (size_t)ti * 64 * 896;
  unsigned voffw[2];
#pragma unroll
  for (int e = 0; e < 2; ++e) voffw[e] = (unsigned)((8 * wave + 4 * e + (lane >> 4)) * 3584 + (((lane & 15) ^ (4 * (lane >> 4))) * 16) + tj * 256);
#define A_XPIECE(g) glds16(xsrc + (g) * 128, voffx, (unsigned)(A_X + (g) * 8192 + wave * 1024))
#define A_WPIECE(G, e) \
  glds16(p.w_in, voffw[e] + (unsigned)(((G) % 7) * 64 * 3584 + ((G) < 7 ? 1792 : 0)), (unsigned)(A_W + ((G) % 6) * A_SLOT + wave * 2048 + (e) * 1024))
#pragma unroll
  for (int g = 0; g < 3; ++g) { A_XPIECE(g); A_WPIECE(g, 0); A_WPIECE(g, 1); }
  LAS const char* ab[2];
  LAS const char* xb[4];
  {
    const int g4 = lane >> 4, qq = (lane & 15) >> 2, p4 = lane & 3;
    const int lb = (8 * (g4 >> 1) + qq) * 256 + (((4 * cg + 2 * (g4 & 1) + (p4 >> 1)) ^ (4 * qq)) * 16) + (p4 & 1) * 8;
    ab[0] = smem + A_W + lb + rh * 4096;
    ab[1] = smem + A_W + lb + (rh ^ 1) * 4096;
    const int row = 32 * rh + l31, sw = (row >> 1) & 7;
#pragma unroll
    for (int i = 0; i < 4; ++i) xb[i] = smem + A_X + row * 128 + (((2 * (i ^ rh) + h) ^ sw) * 16);
  }
  const W2Src w2 = w2_src(p, T2, wave & 3, lane);
  s16x8 xa[28], wb[8];
#define A_FRAGS(st)                                                                                                    \
  do {                                                                                                                 \
    const int G_ = (st) >> 2, i_ = (st) & 3, so_ = (G_ % 6) * A_SLOT + (i_ >> 1) * 8192;                               \
    if ((st) < 28) xa[(st) % 28] = *(LAS const s16x8*)(xb[i_] + G_ * 8192);                                            \
    wb[(st) & 7] = __builtin_shufflevector(tr_read(ab[i_ & 1] + so_), tr_read(ab[i_ & 1] + so_ + 1024), 0, 1, 2, 3, 4, 5, 6, 7); \
  } while (0)
  f32x16 acc2 = {}, acc1 = {};
  float w1a = 0.f, w1b = 0.f;
  u32x4 g1;
  const unsigned g1o = WS_XS1 + ti * 1024 + (32 * rh + l31) * 16;
  unsigned zp[8];
  float m1 = 0.f, r1 = 0.f, ss = 0.f, sq = 0.f;
  const unsigned tag = make_tag(ep);
  WAIT_VM(3);
  BAR();
  STAMP(0, 1);
  A_FRAGS(0); A_FRAGS(1); A_FRAGS(2); A_FRAGS(3);
  auto step = [&](auto stc) __attribute__((always_inline)) {
    constexpr int st = decltype(stc)::value;
    constexpr int G = st >> 2, i = st & 3;
    if (i == 0 && G >= 1) {
      if (G == 1) WAIT_VM(6); else if (G == 2) WAIT_VM(9); else if (G == 3) WAIT_VM(8); else if (G == 4) WAIT_VM(7); else if (G <= 6) WAIT_VM(6);
      else if (G == 7) asm volatile("s_waitcnt vmcnt(3) lgkmcnt(0)" : "+v"(g1)::"memory");
      else if (G == 8) WAIT_VM(6);
      else if (G == 9) WAIT_VM(7); else if (G == 10) WAIT_VM(9);
      else if (G == 11) asm volatile("s_waitcnt vmcnt(10) lgkmcnt(0)" ::: "memory");
      else if (G == 12) WAIT_VM(8);
      __builtin_amdgcn_s_barrier();
      asm volatile("" ::: "memory");
      if (G == 7) STAMP(0, 2);
    }
    __builtin_amdgcn_sched_barrier(0);
    if constexpr (st < 28) acc2 = mfma32_bf(wb[st & 7], xa[st % 28], acc2); else acc1 = mfma32_bf(wb[st & 7], xa[st % 28], acc1);
    if constexpr (st + 4 < 56) A_FRAGS(st + 4);
    if constexpr ((i & 1) == 0) {
      const u32x4 wv = __builtin_bit_cast(u32x4, wb[st & 7]);
#pragma unroll
      for (int e = 0; e < 4; ++e) { const unsigned ww = wv[e]; if (st < 28) w1a = dot2bf(ww, ONES_BF, w1a); else w1b = dot2bf(ww, ONES_BF, w1b); }
    }
    if constexpr (st == 27) {
      const float w1s = xsum32(w1a);
      if (h == 0) *(LAS float*)(smem + A_W1 + (rh * 128 + 32 * cg + l31) * 4) = w1s;
    }
    if constexpr (st == 28) {
      poll_granule(g1, wsr, g1o, tag);
      m1 = __uint_as_float(g1[0]); r1 = __uint_as_float(g1[2]);
    }
    if constexpr (G >= 7 && G <= 10 && i == 1) {
      constexpr int g = G - 7;
      const f32x4 w1 = *(LAS const f32x4*)(smem + A_W1 + (32 * cg + 8 * g + 4 * h) * 4) + *(LAS const f32x4*)(smem + A_W1 + (128 + 32 * cg + 8 * g + 4 * h) * 4);
#pragma unroll
      for (int pp = 0; pp < 2; ++pp) {
        const f32x2 a2 = {acc2[4 * g + 2 * pp], acc2[4 * g + 2 * pp + 1]}, w2v = {w1[2 * pp], w1[2 * pp + 1]};
        const unsigned zz = rn_pair(gelu2(up_bf(rn_pair((a2 - w2v * m1) * r1))));
        zp[2 * g + pp] = zz;
        ss = dot2bf(zz, ONES_BF, ss); sq = dot2bf(zz, zz, sq);
      }
    }
    if constexpr (st == 43) {
      const float s2 = xsum32(ss), q2 = xsum32(sq);
      if (h == 0) *(LAS f32x2*)(smem + A_P + ((32 * rh + l31) * 4 + cg) * 8) = (f32x2){s2, q2};
    }
    if constexpr (st == 44) {
      if (wave == 0) {
        const f32x4 p0 = *(LAS const f32x4*)(smem + A_P + lane * 32), p1 = *(LAS const f32x4*)(smem + A_P + lane * 32 + 16);
        const float s = (p0[0] + p0[2]) + (p1[0] + p1[2]), q = (p0[1] + p0[3]) + (p1[1] + p1[3]);
        u32x4 gr = {__float_as_uint(s), tag, __float_as_uint(q), tag};
        const unsigned vo = WS_XCH + ti * 8192 + lane * 128 + tj * 16;
        asm volatile("buffer_store_dwordx4 %0, %1, %2, 0 offen sc1\n\ts_nop 1" ::"v"(gr), "v"(vo), "s"(wsr) : "memory");
      }
    }
    __builtin_amdgcn_sched_barrier(0);
    if constexpr ((i & 1) == 0) {
      constexpr int ks = st >> 1;
      if constexpr (ks < 4) { A_XPIECE(ks + 3); A_WPIECE(ks + 3, 0); A_WPIECE(ks + 3, 1); }
      else if constexpr (ks < 18) A_WPIECE(5 + (ks >> 1), ks & 1);
    }
    if constexpr (st == 20) asm volatile("buffer_load_dwordx4 %0, %1, %2, 0 offen sc1" : "=v"(g1) : "v"(g1o), "s"(wsr) : "memory");
    if constexpr (st >= 35 && st <= 42) {
      if (wave < 4) w2_piece(w2, wave, 0, st - 35);
      else {
        unsigned po = (unsigned)((b * 4 + (wave - 4)) * 9 + (st - 35)) * 1024u;
        po = po > 8388608u - 1024u ? 8388608u - 1024u : po;
        glds16((const char*)p.w_sp + po, (unsigned)lane * 16u, (unsigned)(A_X + 8192 + (wave - 4) * 1024));
      }
    }
    __builtin_amdgcn_sched_barrier(0);
  };
  static_for<0, 56>(step);
#undef A_FRAGS
#undef A_XPIECE
#undef A_WPIECE
  STAMP(0, 3);
  {
    const float w1s = xsum32(w1b);
    if (h == 0) *(LAS float*)(smem + A_W1 + 1024 + (rh * 128 + 32 * cg + l31) * 4) = w1s;
  }
  BAR();
  u32x4 gr;
  const int row = tid >> 3, jj = tid & 7;
  const unsigned vo = WS_XCH + ti * 8192 + row * 128 + (jj < 7 ? jj : 0) * 16;
  asm volatile("buffer_load_dwordx4 %0, %1, %2, 0 offen sc1" : "=v"(gr) : "v"(vo), "s"(wsr) : "memory");
  {
    const int tt = 4 * ti + 2 * rh + (l31 >> 4);
    unsigned o[4][2];
#pragma unroll
    for (int g = 0; g < 4; ++g) {
      const f32x4 w1 = *(LAS const f32x4*)(smem + A_W1 + 1024 + (32 * cg + 8 * g + 4 * h) * 4) + *(LAS const f32x4*)(smem + A_W1 + 1024 + (128 + 32 * cg + 8 * g + 4 * h) * 4);
#pragma unroll
      for (int pp = 0; pp < 2; ++pp) {
        const f32x2 a2 = {acc1[4 * g + 2 * pp], acc1[4 * g + 2 * pp + 1]}, w2v = {w1[2 * pp], w1[2 * pp + 1]};
        o[g][pp] = rn_pair(gelu2(up_bf(rn_pair((a2 - w2v * m1) * r1))));
      }
    }
    asm volatile("s_waitcnt vmcnt(0)" : "+v"(gr)::"memory");
#pragma unroll
    for (int g = 0; g < 4; ++g) {
      const int ft = 8 * tj + 2 * cg + (g >> 1), lp = (lane & 15) + 16 * (2 * (g & 1) + h);
      st8(wsr, WS_Z1D + ((tt * 56 + ft) * 64 + lp) * 8, (u32x2){o[g][0], o[g][1]});
    }
  }
  STAMP(0, 4);
  poll_granule(gr, wsr, vo, tag);
  {
    const float s = red8(jj < 7 ? __uint_as_float(gr[0]) : 0.f), q = red8(jj < 7 ? __uint_as_float(gr[2]) : 0.f);
    float mb, rr2;
    ln_scalars(s, q, 1.0f / 896.0f, mb, rr2);
    if (jj == 0) { *(LAS float*)(smem + A_M2 + row * 4) = mb; *(LAS float*)(smem + A_R2 + row * 4) = rr2; }
  }
  BAR();
  {
    LAS char* tb = smem + A_W + wave * 2304;
    const int srow = 32 * rh + l31;
    const float mb2 = *(LAS const float*)(smem + A_M2 + srow * 4), rr2 = *(LAS const float*)(smem + A_R2 + srow * 4);
#pragma unroll
    for (int g = 0; g < 4; ++g) {
      unsigned o[2];
#pragma unroll
      for (int pp = 0; pp < 2; ++pp) o[pp] = rn_pair(up_bf(rn_pair(up_bf(zp[2 * g + pp]) - mb2)) * rr2);
      *(LAS u32x2*)(tb + l31 * 72 + (8 * g + 4 * h) * 2) = (u32x2){o[0], o[1]};
    }
    asm volatile("s_waitcnt lgkmcnt(0)" ::: "memory");
    const int q4 = lane >> 4, l15 = lane & 15;
#pragma unroll
    for (int n = 0; n < 2; ++n) {
      LAS const char* rp = tb + (8 * q4 + (l15 >> 2)) * 72 + n * 32 + (lane & 3) * 8;
      const s16x4 lo = tr_read(rp), hi = tr_read(rp + 4 * 72);
      const s16x8 o = __builtin_shufflevector(lo, hi, 0, 1, 2, 3, 4, 5, 6, 7);
      const int ft = 8 * tj + 2 * cg + n, sb = 2 * ti + rh;
      st16(wsr, WS_Z2F + ((ft * 64 + sb) * 64 + lane) * 16, __builtin_bit_cast(u32x4, o));
    }
  }
  return tag;
}

constexpr int B_CH = 65536;
constexpr int B_CHS = 49152;
constexpr int CH_FT = 0;
constexpr int CH_FW = 16416, CH_FS = 288;
constexpr int CH_CS = 4160;
constexpr int CH_U = 33280;
constexpr int CH_T = CH_U + 4608;
constexpr int CH_TS = 49 * 64;
constexpr int CH_CNT = CH_T + 2 * CH_TS;

template <int W>
__device__ __forceinline__ void b_epi(LAS char* smem, rsrc_t wsr, f32x4 (&acc)[7][4], const u32x2 (&z1v)[7], float bs, int lane, int tt, int J) {
  LAS char* cnt = smem + B_CH + CH_CNT + 64;
  LAS char* mine = smem + B_RING + W * 16384 + lane * 16;
  LAS char* other = smem + B_RING + (W ^ 2) * 16384 + lane * 16;
  LAS char* o2 = smem + B_RING + (W ^ 3) * 16384 + lane * 16;
  constexpr int K0 = W & 2, S0 = K0 ^ 2;
#pragma unroll
  for (int n = 0; n < 7; ++n) { *(LAS f32x4*)(mine + n * 1024) = acc[n][S0]; *(LAS f32x4*)(mine + (7 + n) * 1024) = acc[n][S0 + 1]; }
  lds_sync(cnt, 4, lane);
#pragma unroll
  for (int n = 0; n < 7; ++n) { acc[n][K0] += *(LAS const f32x4*)(other + n * 1024); acc[n][K0 + 1] += *(LAS const f32x4*)(other + (7 + n) * 1024); }
  asm volatile("s_waitcnt lgkmcnt(0)" ::: "memory");
#pragma unroll
  for (int n = 0; n < 7; ++n) *(LAS f32x4*)(other + n * 1024) = acc[n][W ^ 1];
  lds_sync(cnt, 8, lane);
  unsigned hp[7][2];
#pragma unroll
  for (int n = 0; n < 7; ++n) {
    const f32x4 v = acc[n][W] + *(LAS const f32x4*)(o2 + n * 1024);
#pragma unroll
    for (int pp = 0; pp < 2; ++pp) {
      const f32x2 g0 = up_bf(rn_pair((f32x2){v[2 * pp], v[2 * pp + 1]})) + bs;
      const unsigned zw = z1v[n][pp];
      hp[n][pp] = rn_pair(up_bf(rn_pair(g0)) * up_bf(zw));
    }
  }
  const unsigned hb = WS_HF + ((tt * 28 + (7 * J >> 1)) * 64 + lane) * 16;
  if (!(J & 1)) {
#pragma unroll
    for (int pp = 0; pp < 3; ++pp) st16(wsr, hb + pp * 1024, (u32x4){hp[2 * pp][0], hp[2 * pp][1], hp[2 * pp + 1][0], hp[2 * pp + 1][1]});
    st8(wsr, hb + 3 * 1024, (u32x2){hp[6][0], hp[6][1]});
  } else {
    st8(wsr, hb + 8, (u32x2){hp[0][0], hp[0][1]});
#pragma unroll
    for (int pp = 0; pp < 3; ++pp) st16(wsr, hb + (pp + 1) * 1024, (u32x4){hp[2 * pp + 1][0], hp[2 * pp + 1][1], hp[2 * pp + 2][0], hp[2 * pp + 2][1]});
  }
}

__device__ __forceinline__ void phaseB_gemm(LAS char* smem, const Params& p, rsrc_t wsr, unsigned tag, int wave, int lane, int T, int J) {
  char* ws = p.ws;
  const rsrc_t zs = mk_rsrc(ws + WS_Z2F + (size_t)(7 * J * 64 + 16 * wave) * 1024, 7 * 65536 - 16 * wave * 1024);
  const W2Src w2 = w2_src(p, T, wave, lane);
  const unsigned voffz = lane * 16;
  const unsigned ring = B_RING + wave * 16384;
  LAS const char* wrd0 = smem + ring + (lane & 15) * 128 + (((lane >> 4) ^ ((lane >> 1) & 7)) * 16);
  LAS const char* wrd1 = smem + ring + (lane & 15) * 128 + (((4 + (lane >> 4)) ^ ((lane >> 1) & 7)) * 16);
  u32x4 z[3][7];
  f32x4 acc[7][4];
#pragma unroll
  for (int n = 0; n < 7; ++n)
#pragma unroll
    for (int m = 0; m < 4; ++m) acc[n][m] = (f32x4){0.f, 0.f, 0.f, 0.f};
#pragma unroll
  for (int k = 0; k < 8; ++k) w2_piece(w2, wave, 1, k);
  {
    const int tj0 = (112 * J) >> 7, tj1 = (112 * J + 111) >> 7;
    poll_flags(wsr, WS_FA + ((lane >> 1) * 8 + ((lane & 1) ? tj1 : tj0)) * 4, tag);
  }
  STAMP(1, 0);
#define B_ISSUE_Z(it) _Pragma("unroll") for (int n = 0; n < 7; ++n) bload16(z[(it) % 3][n], zs, voffz, n * 65536 + (it) * 1024)
  B_ISSUE_Z(0); B_ISSUE_Z(1); B_ISSUE_Z(2);
#undef B_ISSUE_Z
  const int tt = 4 * T + wave;
  u32x2 z1v[7];
  unsigned bsw;
#define B_WAITZ(N) asm volatile("s_waitcnt vmcnt(" #N ")" : "+v"(zc[0]), "+v"(zc[1]), "+v"(zc[2]), "+v"(zc[3]), "+v"(zc[4]), "+v"(zc[5]), "+v"(zc[6])::"memory")
#pragma unroll
  for (int it = 0; it < 16; ++it) {
    u32x4(&zc)[7] = z[it % 3];
    if (it <= 1) B_WAITZ(14); else if (it <= 13) B_WAITZ(22); else if (it == 14) B_WAITZ(15); else B_WAITZ(8);
    s16x8 wf[4];
#pragma unroll
    for (int m = 0; m < 4; ++m) wf[m] = *(LAS const s16x8*)(((it & 1) ? wrd1 : wrd0) + ((it >> 1) & 1) * 8192 + m * 2048);
#pragma unroll
    for (int n = 0; n < 7; ++n) {
#pragma unroll
      for (int m = 0; m < 4; ++m) acc[n][m] = mfma_bf(__builtin_bit_cast(s16x8, zc[n]), wf[m], acc[n][m]);
      __builtin_amdgcn_sched_barrier(0);
      if (it + 3 < 16) {
        bload16(zc[n], zs, voffz, n * 65536 + (it + 3) * 1024);
        if ((it & 1) && n >= 1 && n <= 4) { w2_piece(w2, wave, (it + 3) >> 1, 2 * (n - 1)); w2_piece(w2, wave, (it + 3) >> 1, 2 * (n - 1) + 1); }
      }
      if (it == 13) {
        const unsigned vo = WS_Z1D + (tt * 56 + 7 * J + n) * 512 + lane * 8;
        asm volatile("buffer_load_dwordx2 %0, %1, %2, 0 offen sc1" : "=v"(z1v[n]) : "v"(vo), "s"(wsr) : "memory");
        if (n == 6) {
          const unsigned vb = (unsigned)(16 * tt + (lane & 15)) * 2u;
          asm volatile("global_load_ushort %0, %1, %2" : "=v"(bsw) : "v"(vb), "s"(p.b_sp) : "memory");
        }
      }
      __builtin_amdgcn_sched_barrier(0);
    }
  }
#undef B_WAITZ
  STAMP(1, 1);
  asm volatile("s_waitcnt vmcnt(0)" : "+v"(z1v[0]), "+v"(z1v[1]), "+v"(z1v[2]), "+v"(z1v[3]), "+v"(z1v[4]), "+v"(z1v[5]), "+v"(z1v[6]), "+v"(bsw)::"memory");
  const float bs = bf2f((unsigned short)bsw);
  if (wave == 0) b_epi<0>(smem, wsr, acc, z1v, bs, lane, tt, J);
  else if (wave == 1) b_epi<1>(smem, wsr, acc, z1v, bs, lane, tt, J);
  else if (wave == 2) b_epi<2>(smem, wsr, acc, z1v, bs, lane, tt, J);
  else b_epi<3>(smem, wsr, acc, z1v, bs, lane, tt, J);
  STAMP(1, 2);
}

__device__ __forceinline__ f32x2 cmul(f32x2 a, f32x2 b) {
  f32x2 t, r;
  asm("v_pk_mul_f32 %0, %1, %2 op_sel:[0,0] op_sel_hi:[0,1]" : "=v"(t) : "v"(a), "v"(b));
  asm("v_pk_fma_f32 %0, %1, %2, %3 op_sel:[1,1,0] op_sel_hi:[1,0,1] neg_lo:[1,0,0]" : "=v"(r) : "v"(a), "v"(b), "v"(t));
  return r;
}
__device__ __forceinline__ unsigned hmul(unsigned a, unsigned b) {
  unsigned t, r;
  asm("v_pk_mul_f16 %0, %1, %2 op_sel:[0,0] op_sel_hi:[0,1]" : "=v"(t) : "v"(a), "v"(b));
  asm("v_pk_fma_f16 %0, %1, %2, %3 op_sel:[1,1,0] op_sel_hi:[1,0,1] neg_lo:[1,0,0]" : "=v"(r) : "v"(a), "v"(b), "v"(t));
  return r;
}
__device__ __forceinline__ unsigned hcv(f32x2 v) { return pk_f16(v.x, v.y); }
__device__ __forceinline__ f32x2 cexp_p(float al, float be, float pp) {
  const float e = __builtin_amdgcn_exp2f(al * pp), th = __builtin_amdgcn_fractf(be * pp);
  return (f32x2){__builtin_amdgcn_cosf(th), __builtin_amdgcn_sinf(th)} * e;
}
struct PosTab { unsigned short v[2064]; };
constexpr int pos_bf16(int j) {
  if (j < 256) return j;
  int sh = 1; while ((j >> sh) >= 256) ++sh;
  const int q = j >> sh, r = j & ((1 << sh) - 1), h = 1 << (sh - 1);
  return (r > h || (r == h && (q & 1)) ? q + 1 : q) << sh;
}
constexpr PosTab make_postab() {
  PosTab t = {};
  for (int x = 0; x < 2048; ++x) {
    const int p = pos_bf16(2047 - x);
    int col = 0, a = 0;
    if (p < 256) { a = p >> 4; col = p & 15; }
    else if (p < 512) { const int r = p - 256; a = r >> 4; col = 16 + ((r & 15) >> 1); }
    else if (p < 1024) { const int r = p - 512; a = (r & 255) >> 4; col = 24 + 4 * (r >> 8) + ((r & 15) >> 2); }
    else if (p < 2048) { const int r = p - 1024; a = (r & 255) >> 4; col = 32 + 2 * (r >> 8) + ((r & 15) >> 3); }
    else { a = 0; col = 40; }
    t.v[x] = (unsigned short)((col * 16 + a) * 4);
  }
  for (int x = 2048; x < 2064; ++x) t.v[x] = 48 * 64;
  return t;
}
__device__ const PosTab POSTAB = make_postab();

template <int HW>
__device__ __forceinline__ void s4d_conv(LAS char* cb, rsrc_t wsr, int d, int lane, float iscale, float dsk) {
  constexpr int NF = HW ? 7 : 8;
  f32x4 acc[8] = {};
  const int r = lane & 15, h = lane >> 4;
  LAS const char* ab = cb + CH_FT + ((7 - r) & 7) * CH_CS + (255 - (r >> 3) - 2 * (h >> 1) + (h & 1)) * 16 - (64 * 7 + 512 * 7);
  LAS const char* bb = cb + CH_U + 512 + 32 * r - 32 * (h >> 1) + 16 * (h & 1) - 64 * 7;
  u32x4 A[2][8], B[2][8];
#define CONV_LOAD(ar, buf)                                                                                   \
  _Pragma("unroll") for (int k = 0; k < NF; ++k) {                                                           \
    B[buf][k] = *(LAS const u32x4*)(bb + 512 * k + 64 * (7 - (ar)));                                         \
    A[buf][k] = *(LAS const u32x4*)(ab + 512 * (7 - k) + 64 * (7 - (ar)));                                   \
  }
  CONV_LOAD(0, 0);
#pragma unroll
  for (int ar = 0; ar < 8; ++ar) {
    __builtin_amdgcn_sched_barrier(0);
    if (ar < 7) { CONV_LOAD(ar + 1, (ar + 1) & 1); }
#pragma unroll
    for (int al = 0; al < NF; ++al)
#pragma unroll
      for (int be = 0; be < NF; ++be) {
        const int tau = al + be;
        if (HW ? (tau >= 4 && tau <= 6) : (tau <= 3 || tau == 7)) acc[tau] = mfma_h(A[ar & 1][al], B[ar & 1][be], acc[tau]);
      }
#pragma unroll
    for (int i = 0; i < 16; ++i) { __builtin_amdgcn_sched_group_barrier(0x008, 1, 0); __builtin_amdgcn_sched_group_barrier(0x100, 1, 0); }
    __builtin_amdgcn_sched_barrier(0);
  }
#undef CONV_LOAD
  const int q = lane >> 4;
#pragma unroll
  for (int t = 0; t < 8; ++t) {
    if (HW ? (t >= 4 && t <= 6) : (t <= 3 || t == 7)) {
      const f32x4 v = acc[t];
      const int l0 = 256 * t + 16 * r + 4 * q;
      const u32x2 uw = *(LAS const u32x2*)(cb + CH_U + 512 + l0 * 2);
      unsigned o[2];
#pragma unroll
      for (int pp = 0; pp < 2; ++pp) {
        const unsigned uwp = uw[pp];
        const f16x2 ua = __builtin_bit_cast(f16x2, uwp);
        const f32x2 y2 = up_bf(rn_pair((f32x2){v[2 * pp], v[2 * pp + 1]} * iscale)), k2 = up_bf(rn_pair((f32x2){(float)ua[0], (float)ua[1]} * dsk));
        o[pp] = rn_pair(y2 + k2);
      }
      st8(wsr, WS_ST + (d * SEQ + l0) * 2, (u32x2){o[0], o[1]});
    }
  }
}

__device__ __forceinline__ void phaseB_s4d(LAS char* cb, const Params& p, rsrc_t wsr, unsigned tag, int d, int hw, int lane) {
  char* ws = p.ws;
  LAS char* cnt = cb + CH_CNT;
  unsigned la_[2], ai_[2], cr_[4], ci_[4], ldt_, dsk_;
  u32x4 pt0, pt1, pt2;
  {
    const unsigned vo = (unsigned)(d * NST + 128 * hw + lane) * 2u, vo2 = (unsigned)(d * NST + ((128 * hw + 128 + lane) & 255)) * 2u, vd = (unsigned)d * 2u;
    asm volatile("global_load_ushort %0, %1, %2" : "=v"(la_[0]) : "v"(vo), "s"(p.logA) : "memory");
    asm volatile("global_load_ushort %0, %1, %2 offset:128" : "=v"(la_[1]) : "v"(vo), "s"(p.logA) : "memory");
    asm volatile("global_load_ushort %0, %1, %2" : "=v"(ai_[0]) : "v"(vo), "s"(p.Aim) : "memory");
    asm volatile("global_load_ushort %0, %1, %2 offset:128" : "=v"(ai_[1]) : "v"(vo), "s"(p.Aim) : "memory");
    asm volatile("global_load_ushort %0, %1, %2" : "=v"(cr_[0]) : "v"(vo), "s"(p.Cre) : "memory");
    asm volatile("global_load_ushort %0, %1, %2 offset:128" : "=v"(cr_[1]) : "v"(vo), "s"(p.Cre) : "memory");
    asm volatile("global_load_ushort %0, %1, %2" : "=v"(cr_[2]) : "v"(vo2), "s"(p.Cre) : "memory");
    asm volatile("global_load_ushort %0, %1, %2 offset:128" : "=v"(cr_[3]) : "v"(vo2), "s"(p.Cre) : "memory");
    asm volatile("global_load_ushort %0, %1, %2" : "=v"(ci_[0]) : "v"(vo), "s"(p.Cim) : "memory");
    asm volatile("global_load_ushort %0, %1, %2 offset:128" : "=v"(ci_[1]) : "v"(vo), "s"(p.Cim) : "memory");
    asm volatile("global_load_ushort %0, %1, %2" : "=v"(ci_[2]) : "v"(vo2), "s"(p.Cim) : "memory");
    asm volatile("global_load_ushort %0, %1, %2 offset:128" : "=v"(ci_[3]) : "v"(vo2), "s"(p.Cim) : "memory");
    asm volatile("global_load_ushort %0, %1, %2" : "=v"(ldt_) : "v"(vd), "s"(p.logdt) : "memory");
    asm volatile("global_load_ushort %0, %1, %2" : "=v"(dsk_) : "v"(vd), "s"(p.Dsk) : "memory");
    const char* pb = (const char*)POSTAB.v + hw * 2048;
    const unsigned vp = lane * 32;
    asm volatile("global_load_dwordx4 %0, %1, %2" : "=v"(pt0) : "v"(vp), "s"(pb) : "memory");
    asm volatile("global_load_dwordx4 %0, %1, %2 offset:16" : "=v"(pt1) : "v"(vp), "s"(pb) : "memory");
    asm volatile("global_load_dwordx4 %0, %1, %2 offset:32" : "=v"(pt2) : "v"(vp), "s"(pb) : "memory");
    if (hw == 0 && lane < 32) *(LAS u32x4*)(cb + CH_U + lane * 16) = (u32x4){0u, 0u, 0u, 0u};
    if (lane < 4) *(LAS u32x4*)(cb + CH_T + hw * CH_TS + 48 * 64 + lane * 16) = (u32x4){0u, 0u, 0u, 0u};
  }
  BAR();
  __builtin_amdgcn_s_setprio(3);
  asm volatile("s_waitcnt vmcnt(0)" : "+v"(la_[0]), "+v"(la_[1]), "+v"(ai_[0]), "+v"(ai_[1]), "+v"(cr_[0]), "+v"(cr_[1]), "+v"(cr_[2]), "+v"(cr_[3]), "+v"(ci_[0]), "+v"(ci_[1]),
               "+v"(ci_[2]), "+v"(ci_[3]), "+v"(ldt_), "+v"(dsk_), "+v"(pt0), "+v"(pt1), "+v"(pt2)::"memory");
  STAMP(1, 0);
  float cr[4], ci[4];
#pragma unroll
  for (int k = 0; k < 4; ++k) { cr[k] = bf2f((unsigned short)cr_[k]); ci[k] = bf2f((unsigned short)ci_[k]); }
  const float dt = rbf(__builtin_amdgcn_exp2f(bf2f((unsigned short)ldt_) * 1.4426950408889634f)), dsk = bf2f((unsigned short)dsk_);
  float sabs = 0.f;
#pragma unroll
  for (int k = 0; k < 4; ++k) sabs += __builtin_fabsf(cr[k]) + __builtin_fabsf(ci[k]);
  sabs = xsum16_32(red16(sabs));
  sabs *= 2.0f * dt;
  int ex = (int)((__float_as_uint(sabs) >> 23) & 0xff) - 126;
  ex = ex < -100 ? -100 : (ex > 100 ? 100 : ex);
  const float scale = __uint_as_float((unsigned)(127 + 15 - ex) << 23), iscale = __uint_as_float((unsigned)(127 - 15 + ex) << 23);
  f32x4 kacc[3] = {};
  LAS char* fw = cb + CH_FT + hw * CH_FW;
#pragma unroll
  for (int ps = 0; ps < 2; ++ps) {
    const float lap = bf2f((unsigned short)la_[ps]), aip = bf2f((unsigned short)ai_[ps]);
    const float crp = cr[ps], cip = ci[ps];
    const float lre = -rbf(__builtin_amdgcn_exp2f(lap * 1.4426950408889634f)) * dt, lim = aip * dt;
    const float al = lre * 1.4426950408889634f, be = lim * 0.15915494309189535f;
    LAS char* wp = fw + lane * 4;
#define PUTH(row, v) *(LAS unsigned*)(wp + (row) * CH_FS) = (v)
    {
      const f32x2 w = cexp_p(al, be, 16.f);
      const float cs = 2.0f * dt * scale;
      const f32x2 c0 = {crp * cs, cip * cs};
      const f32x2 w2 = cmul(w, w), c1 = cmul(c0, w), w4 = cmul(w2, w2), c2 = cmul(c0, w2), c3 = cmul(c1, w2), w8 = cmul(w4, w4), w12 = cmul(w8, w4);
      const unsigned hc[4] = {hcv(c0), hcv(c1), hcv(c2), hcv(c3)}, hw4[3] = {hcv(w4), hcv(w8), hcv(w12)};
#pragma unroll
      for (int j = 0; j < 4; ++j) {
        PUTH(j, hc[j]);
#pragma unroll
        for (int k = 0; k < 3; ++k) PUTH(4 * (k + 1) + j, hmul(hc[j], hw4[k]));
      }
    }
    {
      const f32x2 e1 = cexp_p(al, -be, 1.f);
      const f32x2 e2 = cmul(e1, e1), e3 = cmul(e2, e1), e4 = cmul(e2, e2), e8 = cmul(e4, e4), e12 = cmul(e8, e4);
      unsigned E[16];
      E[0] = 0x00003c00u; E[1] = hcv(e1); E[2] = hcv(e2); E[3] = hcv(e3); E[4] = hcv(e4); E[8] = hcv(e8); E[12] = hcv(e12);
#pragma unroll
      for (int m = 1; m < 4; ++m)
#pragma unroll
        for (int j = 1; j < 4; ++j) E[4 * m + j] = hmul(E[4 * m], E[j]);
#pragma unroll
      for (int k = 0; k < 16; ++k) PUTH(16 + k, E[k]);
      const f32x2 z256 = cexp_p(al, -be, 256.f), z512 = cexp_p(al, -be, 512.f), z1024 = cexp_p(al, -be, 1024.f), z2048 = cexp_p(al, -be, 2048.f);
      const f32x2 z768 = cmul(z512, z256);
      const unsigned h256 = hcv(z256), h512 = hcv(z512), h768 = hcv(z768);
      PUTH(32, h256);
#pragma unroll
      for (int k = 1; k < 8; ++k) PUTH(32 + k, hmul(h256, E[2 * k]));
      PUTH(40, h512); PUTH(44, h768);
#pragma unroll
      for (int k = 1; k < 4; ++k) { PUTH(40 + k, hmul(h512, E[4 * k])); PUTH(44 + k, hmul(h768, E[4 * k])); }
      const unsigned hb[4] = {hcv(z1024), hcv(cmul(z1024, z256)), hcv(cmul(z1024, z512)), hcv(cmul(z1024, z768))};
#pragma unroll
      for (int k = 0; k < 4; ++k) { PUTH(48 + 2 * k, hb[k]); PUTH(49 + 2 * k, hmul(hb[k], E[8])); }
      PUTH(56, hcv(z2048));
    }
#undef PUTH
    asm volatile("s_waitcnt lgkmcnt(0)" ::: "memory");
    LAS const char* rp = fw + (lane & 15) * CH_FS + (lane >> 4) * 16;
    u32x4 fa[4], fb[4][3];
#pragma unroll
    for (int kk = 0; kk < 4; ++kk) {
      fa[kk] = *(LAS const u32x4*)(rp + kk * 64);
#pragma unroll
      for (int t = 0; t < 3; ++t) fb[kk][t] = *(LAS const u32x4*)(rp + (16 + 16 * t) * CH_FS + kk * 64);
    }
#pragma unroll
    for (int kk = 0; kk < 4; ++kk)
#pragma unroll
      for (int t = 0; t < 3; ++t) kacc[t] = mfma_h(fa[kk], fb[kk][t], kacc[t]);
    asm volatile("" ::: "memory");
  }
  STAMP(1, 1);
  u32x4 u0, u1;
  poll_flags(wsr, WS_FA + ((lane & 31) * 8 + 7) * 4, tag);
  {
    const unsigned vu = WS_XT + d * SEQ * 2 + hw * 2048 + lane * 16;
    asm volatile("buffer_load_dwordx4 %0, %1, %2, 0 offen sc1" : "=v"(u0) : "v"(vu), "s"(wsr) : "memory");
    asm volatile("buffer_load_dwordx4 %0, %1, %2, 0 offen offset:1024 sc1" : "=v"(u1) : "v"(vu), "s"(wsr) : "memory");
  }
#pragma unroll
  for (int t = 0; t < 3; ++t) *(LAS f32x4*)(cb + CH_T + hw * CH_TS + (16 * t + (lane & 15)) * 64 + (lane >> 4) * 16) = kacc[t];
  lds_sync(cnt, 2, lane);
  {
    const int y = 2 * (64 * hw + lane);
    const unsigned po[12] = {pt0[0], pt0[1], pt0[2], pt0[3], pt1[0], pt1[1], pt1[2], pt1[3], pt2[0], pt2[1], pt2[2], pt2[3]};
    unsigned W[12], V[11];
#pragma unroll
    for (int k = 0; k < 12; ++k) {
      LAS const char* a0 = cb + CH_T + (po[k] & 0xffffu);
      LAS const char* a1 = cb + CH_T + (po[k] >> 16);
      W[k] = pk_f16(*(LAS const float*)a0 + *(LAS const float*)(a0 + CH_TS), *(LAS const float*)a1 + *(LAS const float*)(a1 + CH_TS));
    }
#pragma unroll
    for (int i = 0; i < 11; ++i) V[i] = __builtin_amdgcn_alignbit(W[i + 1], W[i], 16);
#pragma unroll
    for (int m = 0; m < 4; ++m) {
      *(LAS u32x4*)(cb + CH_FT + (2 * m) * CH_CS + y * 16) = (u32x4){W[m], W[m + 1], W[m + 2], W[m + 3]};
      *(LAS u32x4*)(cb + CH_FT + (2 * m) * CH_CS + y * 16 + 16) = (u32x4){W[m + 4], W[m + 5], W[m + 6], W[m + 7]};
      *(LAS u32x4*)(cb + CH_FT + (2 * m + 1) * CH_CS + y * 16) = (u32x4){V[m], V[m + 1], V[m + 2], V[m + 3]};
      *(LAS u32x4*)(cb + CH_FT + (2 * m + 1) * CH_CS + y * 16 + 16) = (u32x4){V[m + 4], V[m + 5], V[m + 6], V[m + 7]};
    }
    if (hw == 1 && lane < 8) *(LAS u32x4*)(cb + CH_FT + lane * CH_CS + 256 * 16) = (u32x4){0u, 0u, 0u, 0u};
  }
  asm volatile("s_waitcnt vmcnt(0)" : "+v"(u0), "+v"(u1)::"memory");
  *(LAS u32x4*)(cb + CH_U + 512 + hw * 2048 + lane * 16) = u0;
  *(LAS u32x4*)(cb + CH_U + 512 + hw * 2048 + 1024 + lane * 16) = u1;
  lds_sync(cnt, 4, lane);
  STAMP(1, 2);
  if (hw) s4d_conv<1>(cb, wsr, d, lane, iscale, dsk); else s4d_conv<0>(cb, wsr, d, lane, iscale, dsk);
  __builtin_amdgcn_s_setprio(0);
  STAMP(1, 3);
}

constexpr int C_PART = 0;
constexpr int C_XS = 65536;
constexpr int C_SS = C_XS + 64 * 136;
constexpr int C_OS = C_SS + 64 * 136;

__device__ __forceinline__ void phaseC(LAS char* smem, const Params& p, rsrc_t wsr, unsigned tag, int T, int Dt, int tid) {
  char* ws = p.ws;
  const int lane = tid & 63;
  const int wave = __builtin_amdgcn_readfirstlane(tid >> 6);
  unsigned fo;
  {
    const int J = lane & 7, bh = 2 * (T >> 3) + (J >> 2) + 8 * ((T & 7) + 8 * (J & 3));
    const int bs = 32 * Dt + ((lane - 8) & 31);
    fo = WS_FB + (lane < 8 ? bh : bs) * 4;
  }
  if (wave < 4) {
    const unsigned ho = WS_HF + ((4 * T * 28 + 7 * wave) * 64 + lane) * 16, wo = WS_WOF + ((4 * Dt * 28 + 7 * wave) * 64 + lane) * 16;
    u32x4 hf[7][4], wf[7][4];
    f32x4 acc[4][4];
#pragma unroll
    for (int k = 0; k < 7; ++k)
#pragma unroll
      for (int m = 0; m < 4; ++m) wf[k][m] = ld16(wsr, wo + (m * 28 + k) * 1024);
    __builtin_amdgcn_sched_barrier(0);
    poll_flags(wsr, fo, tag);
    STAMP(2, 0);
#pragma unroll
    for (int n = 0; n < 4; ++n)
#pragma unroll
      for (int m = 0; m < 4; ++m) acc[n][m] = (f32x4){0.f, 0.f, 0.f, 0.f};
#pragma unroll
    for (int k = 0; k < 4; ++k)
#pragma unroll
      for (int m = 0; m < 4; ++m) hf[k][m] = ld16(wsr, ho + (m * 28 + k) * 1024);
    __builtin_amdgcn_sched_barrier(0);
#pragma unroll
    for (int k = 0; k < 7; ++k) {
#pragma unroll
      for (int n = 0; n < 4; ++n)
#pragma unroll
        for (int m = 0; m < 4; ++m) acc[n][m] = mfma_bf(__builtin_bit_cast(s16x8, wf[k][n]), __builtin_bit_cast(s16x8, hf[k][m]), acc[n][m]);
      __builtin_amdgcn_sched_barrier(0);
      if (k + 4 < 7) {
#pragma unroll
        for (int m = 0; m < 4; ++m) hf[k + 4][m] = ld16(wsr, ho + (m * 28 + k + 4) * 1024);
      }
      __builtin_amdgcn_sched_barrier(0);
    }
    STAMP(2, 1);
#pragma unroll
    for (int n = 0; n < 4; ++n)
#pragma unroll
      for (int m = 0; m < 4; ++m) *(LAS f32x4*)(smem + C_PART + ((wave * 4 + m) * 4 + n) * 1024 + lane * 16) = acc[n][m];
  } else {
    const int u = tid - 256, row = u >> 2, c = u & 3;
    const u32x4 x0 = *(const u32x4*)(p.x + (size_t)(64 * T + row) * DM + 64 * Dt + 16 * c);
    const u32x4 x1 = *(const u32x4*)(p.x + (size_t)(64 * T + row) * DM + 64 * Dt + 16 * c + 8);
#pragma unroll
    for (int e = 0; e < 2; ++e) {
      *(LAS u32x2*)(smem + C_XS + row * 136 + c * 32 + e * 8) = (u32x2){x0[2 * e], x0[2 * e + 1]};
      *(LAS u32x2*)(smem + C_XS + row * 136 + c * 32 + 16 + e * 8) = (u32x2){x1[2 * e], x1[2 * e + 1]};
    }
    poll_flags(wsr, fo, tag);
    const unsigned so = WS_ST + ((64 * Dt + row) * SEQ + 64 * T + 16 * c) * 2;
    const u32x4 s0 = ld16(wsr, so), s1 = ld16(wsr, so + 16);
#pragma unroll
    for (int e = 0; e < 2; ++e) {
      *(LAS u32x2*)(smem + C_SS + row * 136 + c * 32 + e * 8) = (u32x2){s0[2 * e], s0[2 * e + 1]};
      *(LAS u32x2*)(smem + C_SS + row * 136 + c * 32 + 16 + e * 8) = (u32x2){s1[2 * e], s1[2 * e + 1]};
    }
  }
  BAR();
  {
    const int m = wave & 3, l15 = lane & 15, q = lane >> 4, t = 16 * m + l15;
#pragma unroll
    for (int nn = 0; nn < 2; ++nn) {
      const int n = 2 * (wave >> 2) + nn;
      f32x4 o = *(LAS const f32x4*)(smem + C_PART + ((0 * 4 + m) * 4 + n) * 1024 + lane * 16);
#pragma unroll
      for (int sw = 1; sw < 4; ++sw) o += *(LAS const f32x4*)(smem + C_PART + ((sw * 4 + m) * 4 + n) * 1024 + lane * 16);
      const s16x4 sv = tr_read(smem + C_SS + (16 * n + 4 * q + (l15 >> 2)) * 136 + (16 * m + 4 * (lane & 3)) * 2);
      const u32x2 sw2 = __builtin_bit_cast(u32x2, sv);
      const u32x2 xw = *(LAS const u32x2*)(smem + C_XS + t * 136 + (16 * n + 4 * q) * 2);
      unsigned ow[2];
#pragma unroll
      for (int pp = 0; pp < 2; ++pp) {
        const f32x2 v = up_bf(rn_pair((f32x2){o[2 * pp], o[2 * pp + 1]}));
        const f32x2 e = v * -1.4426950408889634f;
        const f32x2 dd = (f32x2){__builtin_amdgcn_exp2f(e.x), __builtin_amdgcn_exp2f(e.y)} + 1.0f;
        const f32x2 g = up_bf(rn_pair(v * (f32x2){__builtin_amdgcn_rcpf(dd.x), __builtin_amdgcn_rcpf(dd.y)}));
        const unsigned s_ = sw2[pp], x_ = xw[pp];
        ow[pp] = rn_pair(up_bf(x_) + up_bf(rn_pair(up_bf(s_) * g)));
      }
      *(LAS u32x2*)(smem + C_OS + t * 144 + (16 * n + 4 * q) * 2) = (u32x2){ow[0], ow[1]};
    }
  }
  BAR();
  {
    const int row = tid >> 3, c = tid & 7;
    *(u32x4*)(p.out + (size_t)(64 * T + row) * DM + 64 * Dt + 8 * c) = *(LAS const u32x4*)(smem + C_OS + row * 144 + c * 16);
  }
}

__global__ __launch_bounds__(512, 2) void mixer_block(Params p) {
  extern __shared__ __attribute__((aligned(16))) char smem_raw[];
  LAS char* smem = (LAS char*)smem_raw;
  char* ws = p.ws;
  const int tid = threadIdx.x, lane = tid & 63;
  const int wave = __builtin_amdgcn_readfirstlane(tid >> 6);
  const int b = blockIdx.x, xcd = b & 7, rr = b >> 3;
  const rsrc_t wsr = mk_rsrc(ws, WS_END);
  STAMP(0, 0);
  const unsigned ep = __hip_atomic_load((GAS unsigned*)(ws + WS_EP), __ATOMIC_RELAXED, __HIP_MEMORY_SCOPE_AGENT);
  unsigned tag;
  const int T2 = 8 * (xcd >> 1) + (rr & 7), J2 = 4 * (xcd & 1) + (rr >> 3);
  int fa;
  if (b < 224) {
    const int ti = 4 * xcd + rr / 7, tj = rr % 7;
    tag = phaseA_gemm(smem, p, wsr, ep, b, ti, tj, T2, tid);
    fa = ti * 8 + tj;
  } else {
    tag = phaseA_aux(smem, p, wsr, ep, b - 224, T2, tid);
    fa = (b - 224) * 8 + 7;
  }
  WAIT_VM(0);
  BAR();
  if (tid == 0) st4(wsr, WS_FA + fa * 4, tag);
  STAMP(0, 5);
  if (tid < 64) {
    if (lane < 32) *(LAS unsigned*)(smem + B_CH + CH_CNT + lane * 4) = 0u;
    else *(LAS unsigned*)(smem + B_CH + B_CHS + CH_CNT + (lane - 32) * 4) = 0u;
  }
  if (wave < 4) {
    BAR();
    phaseB_gemm(smem, p, wsr, tag, wave, lane, T2, J2);
  } else if (b < 224) {
    const int sw = wave - 4;
    phaseB_s4d(smem + B_CH + (sw >> 1) * B_CHS, p, wsr, tag, 2 * b + (sw >> 1), sw & 1, lane);
  } else {
    BAR();
    if (b == 255 && wave == 4) {
#pragma unroll
      for (int k = 0; k < 4; ++k) poll_flags(wsr, WS_FA + (64 * k + lane) * 4, tag);
      if (lane == 0) st4(wsr, WS_EP, tag);
    }
  }
  WAIT_VM(0);
  BAR();
  if (tid == 0) st4(wsr, WS_FB + b * 4, tag);
  STAMP(1, 4);
  if (b < 224) phaseC(smem, p, wsr, tag, 4 * xcd + rr / 7, rr % 7, tid);
  STAMP(2, 2);
}

extern "C" void kernel_launch(void* const* d_in, const int* in_sizes, int n_in, void* d_out, int out_size, void* d_ws, size_t ws_size, hipStream_t stream) {
  Params p;
  p.x = (const unsigned short*)d_in[0];
  p.logA = (const unsigned short*)d_in[1];
  p.Aim = (const unsigned short*)d_in[2];
  p.Cre = (const unsigned short*)d_in[3];
  p.Cim = (const unsigned short*)d_in[4];
  p.logdt = (const unsigned short*)d_in[5];
  p.Dsk = (const unsigned short*)d_in[6];
  p.w_in = (const unsigned short*)d_in[8];
  p.w_sp = (const unsigned short*)d_in[9];
  p.b_sp = (const unsigned short*)d_in[10];
  p.w_out = (const unsigned short*)d_in[11];
  p.ws = (char*)d_ws;
  p.out = (unsigned short*)d_out;
  (void)hipFuncSetAttribute((const void*)mixer_block, hipFuncAttributeMaxDynamicSharedMemorySize, 163840);
  mixer_block<<<256, 512, 163840, stream>>>(p);
}
```

```cpp
#include <hip/hip_runtime.h>
#include <hip/hip_bf16.h>
#include <stdint.h>

#define LAS __attribute__((address_space(3)))
#define GAS __attribute__((address_space(1)))
typedef float f32x2 __attribute__((ext_vector_type(2)));
typedef float f32x4 __attribute__((ext_vector_type(4)));
typedef float f32x16 __attribute__((ext_vector_type(16)));
typedef unsigned u32x2 __attribute__((ext_vector_type(2)));
typedef unsigned u32x4 __attribute__((ext_vector_type(4)));
typedef short s16x4 __attribute__((ext_vector_type(4)));
typedef short s16x8 __attribute__((ext_vector_type(8)));
typedef __bf16 bf16x2 __attribute__((ext_vector_type(2)));
typedef __bf16 bf16x8 __attribute__((ext_vector_type(8)));
typedef _Float16 f16x2 __attribute__((ext_vector_type(2)));
typedef _Float16 f16x8 __attribute__((ext_vector_type(8)));

constexpr int SEQ = 2048, DM = 448, FH = 896, NST = 256;

constexpr unsigned WS_Z1D = 0;
constexpr unsigned WS_Z2F = WS_Z1D + SEQ * FH * 2;
constexpr unsigned WS_HF  = WS_Z2F + SEQ * FH * 2;
constexpr unsigned WS_XT  = WS_HF + SEQ * FH * 2;
constexpr unsigned WS_ST  = WS_XT + SEQ * DM * 2;
constexpr unsigned WS_WOF = WS_ST + SEQ * DM * 2;
constexpr unsigned WS_XCH = WS_WOF + FH * DM * 2;
constexpr unsigned WS_XS1 = WS_XCH + 32 * 64 * 8 * 16;
constexpr unsigned WS_FA  = WS_XS1 + 32 * 1024;
constexpr unsigned WS_FZ  = WS_FA + 8192;
constexpr unsigned WS_FB  = WS_FZ + 1024;
constexpr unsigned WS_EP  = WS_FB + 8192;
constexpr unsigned WS_END = WS_EP + 256;

constexpr size_t WS_DBG = 32u << 20;
#define STAMP(kern, slot) do {} while (0)

constexpr unsigned ONES_BF = 0x3f803f80u;
constexpr int AUX_SC1 = 16;
__device__ __forceinline__ float bf2f(unsigned short h) { return __uint_as_float((unsigned)h << 16); }
__device__ __forceinline__ float bflo(unsigned w) { return __uint_as_float(w << 16); }
__device__ __forceinline__ float bfhi(unsigned w) { return __uint_as_float(w & 0xffff0000u); }
__device__ __forceinline__ float rbf(float x) { return (float)(__bf16)x; }
__device__ __forceinline__ unsigned pk_f16(float lo, float hi) {
  f16x2 v = {(_Float16)lo, (_Float16)hi};
  return __builtin_bit_cast(unsigned, v);
}
__device__ __forceinline__ f32x2 up_bf(unsigned w) { return (f32x2){bflo(w), bfhi(w)}; }
__device__ __forceinline__ unsigned rn_pair(f32x2 v) {
  unsigned r;
  asm("v_cvt_pk_bf16_f32 %0, %1, %2" : "=v"(r) : "v"(v.x), "v"(v.y));
  return r;
}
template <int CTRL> __device__ __forceinline__ float dpp_add(float v) {
  int t = __builtin_amdgcn_update_dpp(0, __float_as_int(v), CTRL, 0xf, 0xf, true);
  return v + __int_as_float(t);
}
__device__ __forceinline__ float red8(float v) {
  v = dpp_add<0xB1>(v); v = dpp_add<0x4E>(v); v = dpp_add<0x141>(v);
  return v;
}
__device__ __forceinline__ float red16(float v) { return dpp_add<0x140>(red8(v)); }
__device__ __forceinline__ float xsum32(float v) {
  auto b = __builtin_amdgcn_permlane32_swap(__float_as_uint(v), __float_as_uint(v), false, false);
  return __uint_as_float(b[0]) + __uint_as_float(b[1]);
}
__device__ __forceinline__ float xsum16_32(float v) {
  auto a = __builtin_amdgcn_permlane16_swap(__float_as_uint(v), __float_as_uint(v), false, false);
  return xsum32(__uint_as_float(a[0]) + __uint_as_float(a[1]));
}
__device__ __forceinline__ void glds16(const void* sbase, unsigned voff, unsigned lds) {
  asm volatile("s_mov_b32 m0, %2\n\ts_nop 0\n\tglobal_load_lds_dwordx4 %0, %1" ::"v"(voff), "s"(sbase), "s"(lds) : "memory");
}
#define WAIT_VM(n) asm volatile("s_waitcnt vmcnt(%0)" ::"n"(n) : "memory")
#define BAR() do { asm volatile("s_waitcnt lgkmcnt(0)" ::: "memory"); __builtin_amdgcn_s_barrier(); asm volatile("" ::: "memory"); } while (0)
__device__ __forceinline__ s16x4 tr_read(LAS const char* p) {
  typedef short v4i16 __attribute__((ext_vector_type(4)));
  return __builtin_amdgcn_ds_read_tr16_b64_v4i16((LAS v4i16*)p);
}
__device__ __forceinline__ f32x4 mfma_bf(s16x8 a, s16x8 b, f32x4 c) {
  return __builtin_amdgcn_mfma_f32_16x16x32_bf16(__builtin_bit_cast(bf16x8, a), __builtin_bit_cast(bf16x8, b), c, 0, 0, 0);
}
__device__ __forceinline__ void mfma_acc(f32x4& c, u32x4 a, s16x8 b) {
  asm volatile("v_mfma_f32_16x16x32_bf16 %0, %1, %2, %0" : "+v"(c) : "v"(a), "v"(b));
}
#define MFMA_DONE() asm volatile("s_nop 7\n\ts_nop 7" ::: "memory")
__device__ __forceinline__ f32x16 mfma32_bf(s16x8 a, s16x8 b, f32x16 c) {
  return __builtin_amdgcn_mfma_f32_32x32x16_bf16(__builtin_bit_cast(bf16x8, a), __builtin_bit_cast(bf16x8, b), c, 0, 0, 0);
}
__device__ __forceinline__ f32x4 mfma_h(u32x4 a, u32x4 b, f32x4 c) {
  return __builtin_amdgcn_mfma_f32_16x16x32_f16(__builtin_bit_cast(f16x8, a), __builtin_bit_cast(f16x8, b), c, 0, 0, 0);
}
__device__ __forceinline__ float dot2bf(unsigned a, unsigned b, float c) {
  return __builtin_amdgcn_fdot2_f32_bf16(__builtin_bit_cast(bf16x2, a), __builtin_bit_cast(bf16x2, b), c, false);
}
__device__ __forceinline__ __amdgpu_buffer_rsrc_t mk_rsrc(const void* p, unsigned bytes) {
  return __builtin_amdgcn_make_buffer_rsrc((void*)p, (short)0, (int)bytes, 0x00020000);
}
typedef __amdgpu_buffer_rsrc_t rsrc_t;
__device__ __forceinline__ void st16(rsrc_t r, unsigned off, u32x4 v) { __builtin_amdgcn_raw_buffer_store_b128(v, r, off, 0, AUX_SC1); }
__device__ __forceinline__ void st8(rsrc_t r, unsigned off, u32x2 v) { __builtin_amdgcn_raw_buffer_store_b64(v, r, off, 0, AUX_SC1); }
__device__ __forceinline__ void st4(rsrc_t r, unsigned off, unsigned v) { __builtin_amdgcn_raw_buffer_store_b32(v, r, off, 0, AUX_SC1); }
__device__ __forceinline__ u32x4 ld16(rsrc_t r, unsigned off) { return __builtin_amdgcn_raw_buffer_load_b128(r, off, 0, AUX_SC1); }
__device__ __forceinline__ void bglds16(rsrc_t rs, unsigned voff, unsigned soff, unsigned lds) {
  asm volatile("s_mov_b32 m0, %3\n\ts_nop 0\n\tbuffer_load_dwordx4 %0, %1, %2 offen lds" ::"v"(voff), "s"(rs), "s"(soff), "s"(lds) : "memory");
}
__device__ __forceinline__ void bload16(u32x4& dst, rsrc_t rs, unsigned voff, unsigned soff) {
  asm volatile("buffer_load_dwordx4 %0, %1, %2, %3 offen sc1" : "=v"(dst) : "v"(voff), "s"(rs), "s"(soff) : "memory");
}
__device__ __forceinline__ void poll_granule(u32x4& gr, rsrc_t rs, unsigned voff, unsigned tag) {
  for (unsigned spins = 0;; ++spins) {
    const bool ok = (gr[1] == tag) && (gr[3] == tag);
    if (__all(ok) || spins > (1u << 20)) break;
    __builtin_amdgcn_s_sleep(1);
    asm volatile("buffer_load_dwordx4 %0, %1, %2, 0 offen sc1\n\ts_waitcnt vmcnt(0)" : "=&v"(gr) : "v"(voff), "s"(rs) : "memory");
  }
}
__device__ __forceinline__ void poll_flags(rsrc_t rs, unsigned voff, unsigned tag) {
  for (unsigned spins = 0;; ++spins) {
    unsigned f;
    asm volatile("buffer_load_dword %0, %1, %2, 0 offen sc1\n\ts_waitcnt vmcnt(0)" : "=&v"(f) : "v"(voff), "s"(rs) : "memory");
    if (__all(f == tag) || spins > (1u << 20)) break;
    __builtin_amdgcn_s_sleep(4);
  }
}
__device__ __forceinline__ void group_wait(bool poller, rsrc_t rs, unsigned voff, unsigned tag, LAS char* word) {
  if (poller) {
    poll_flags(rs, voff, tag);
    *(volatile LAS unsigned*)word = 1u;
  } else {
    while ((unsigned)__builtin_amdgcn_readfirstlane(*(volatile LAS unsigned*)word) == 0u) __builtin_amdgcn_s_sleep(2);
  }
  asm volatile("" ::: "memory");
}
__device__ __forceinline__ void lds_sync(LAS char* cntp, unsigned target, int lane) {
  LAS unsigned* cnt = (LAS unsigned*)cntp;
  asm volatile("s_waitcnt lgkmcnt(0)" ::: "memory");
  if (lane == 0) __hip_atomic_fetch_add(cnt, 1u, __ATOMIC_RELAXED, __HIP_MEMORY_SCOPE_WORKGROUP);
  while ((unsigned)__builtin_amdgcn_readfirstlane(*(volatile LAS unsigned*)cnt) < target) __builtin_amdgcn_s_sleep(1);
  asm volatile("" ::: "memory");
}
template <int V> struct IC { static constexpr int value = V; };
template <int A, int B, class F> __device__ __forceinline__ void static_for(F&& f) {
  if constexpr (A < B) { f(IC<A>{}); static_for<A + 1, B>(f); }
}
__device__ __forceinline__ f32x2 gelu2(f32x2 x) {
  const float c1 = -2.0f * 0.7978845608028654f * 1.4426950408889634f, c2 = c1 * 0.044715f;
  const f32x2 t = x * x;
  const f32x2 a = x * (t * c2 + c1);
  f32x2 d = {__builtin_amdgcn_exp2f(a.x), __builtin_amdgcn_exp2f(a.y)};
  d = d + 1.0f;
  return x * (f32x2){__builtin_amdgcn_rcpf(d.x), __builtin_amdgcn_rcpf(d.y)};
}
__device__ __forceinline__ void ln_scalars(float s, float q, float invn, float& mb, float& rr) {
  const float mean = s * invn;
  float var = q * invn - mean * mean; var = var < 0.f ? 0.f : var;
  mb = rbf(mean);
  rr = __builtin_amdgcn_rcpf(rbf(__builtin_amdgcn_sqrtf(rbf(rbf(var) + 1e-5f))));
}

struct Params {
  const unsigned short *x, *logA, *Aim, *Cre, *Cim, *logdt, *Dsk, *w_in, *w_sp, *b_sp, *w_out;
  char* ws;
  unsigned short* out;
};
#define A_ABL 0
#define TLBWARM 1

constexpr int A_X = 0;
constexpr int A_S = 57344;
constexpr int A_P = A_S;
constexpr int A_W1 = A_S + 2048;
constexpr int A_W = 65536;
constexpr int A_TB = A_W;
constexpr int A_M2 = A_W + 32768;
constexpr int A_R2 = A_M2 + 256;
constexpr int A_W1B = A_R2 + 256;
constexpr int A_SLOT = 16384;
constexpr int B_RING = 0;

struct W2Src { rsrc_t wr; unsigned voff0, voff1; };
__device__ __forceinline__ W2Src w2_src(const Params& p, int T, int wave, int lane) {
  W2Src s;
  s.wr = mk_rsrc((const char*)p.w_sp + (size_t)T * 64 * 4096 + wave * 1024, 64 * 4096 - wave * 1024);
  s.voff0 = (lane >> 3) * 4096 + (((lane & 7) ^ ((lane >> 4) & 3)) * 16);
  s.voff1 = (8 + (lane >> 3)) * 4096 + (((lane & 7) ^ (4 + ((lane >> 4) & 3))) * 16);
  return s;
}
__device__ __forceinline__ void w2_piece(const W2Src& s, int wave, int pair, int piece) {
  bglds16(s.wr, (piece & 1) ? s.voff1 : s.voff0, (unsigned)((piece >> 1) * 65536 + pair * 128), (unsigned)(B_RING + wave * 16384 + (pair & 1) * 8192 + piece * 1024));
}

constexpr int AUX_T = 0;
constexpr int AUX_CNT = B_RING + 65536 + 44160 + 80;
constexpr int AUX_PF = 131072;
__device__ __forceinline__ void phaseA_aux(LAS char* smem, const Params& p, rsrc_t wsr, unsigned tag, int v, int tid) {
  char* ws = p.ws;
  const int lane = tid & 63;
  const int wave = __builtin_amdgcn_readfirstlane(tid >> 6);
  const int row = tid >> 2, c4 = tid & 3;
  u32x4 xv[14];
#pragma unroll
  for (int m = 0; m < 14; ++m) xv[m] = *(const u32x4*)(p.x + (size_t)(64 * v + row) * DM + (c4 + 4 * m) * 8);
  unsigned touch[2];
#pragma unroll
  for (int k = 0; k < 2; ++k) {
    const int i = tid + 256 * k;
    const char* a;
    if (i < 256) a = ws + (size_t)(i < (int)(WS_END >> 16) ? i : 0) * 65536;
    else if (i < 384) a = (const char*)p.w_sp + (size_t)(i - 256) * 65536;
    else if (i < 416) a = (const char*)p.w_in + (size_t)(i - 384 < 24 ? i - 384 : 0) * 65536;
    else if (i < 432) a = (const char*)p.w_out + (size_t)(i - 416 < 12 ? i - 416 : 0) * 65536;
    else if (i < 448) a = ((i >> 2) & 3) == 0 ? (const char*)p.logA + (i & 3) * 57344 : ((i >> 2) & 3) == 1 ? (const char*)p.Aim + (i & 3) * 57344
                        : ((i >> 2) & 3) == 2 ? (const char*)p.Cre + (i & 3) * 57344 : (const char*)p.Cim + (i & 3) * 57344;
    else if (i < 480) a = (const char*)p.out + (size_t)(i - 448 < 28 ? i - 448 : 0) * 65536;
    else a = (const char*)p.x + (size_t)(i - 480 < 28 ? i - 480 : 0) * 65536;
    asm volatile("global_load_dword %0, %1, off" : "=v"(touch[k]) : "v"(a) : "memory");
  }
  float s = 0.f, q = 0.f;
#pragma unroll
  for (int m = 0; m < 14; ++m)
#pragma unroll
    for (int e = 0; e < 4; ++e) { const unsigned w = xv[m][e]; s = dot2bf(w, ONES_BF, s); q = dot2bf(w, w, q); }
  s = dpp_add<0x4E>(dpp_add<0xB1>(s)); q = dpp_add<0x4E>(dpp_add<0xB1>(q));
  float mb, rr;
  ln_scalars(s, q, 1.0f / 448.0f, mb, rr);
  asm volatile("s_waitcnt vmcnt(0)" : "+v"(touch[0]), "+v"(touch[1])::"memory");
  if (c4 == 0) st16(wsr, WS_XS1 + v * 1024 + row * 16, (u32x4){__float_as_uint(mb), tag, __float_as_uint(rr), tag});
  unsigned short wv[7][8];
#pragma unroll
  for (int k = 0; k < 7; ++k) {
    int fr = v * 4 + wave + 128 * k; fr = fr < 784 ? fr : 783;
    const int dt = fr / 28, fb = fr - dt * 28;
    const int dcol = 16 * dt + (lane & 15), qq = lane >> 4;
#pragma unroll
    for (int jj = 0; jj < 8; ++jj) wv[k][jj] = p.w_out[(size_t)(32 * fb + 16 * (jj >> 2) + 4 * qq + (jj & 3)) * DM + dcol];
  }
  LAS char* tb = smem + AUX_T;
#pragma unroll
  for (int m = 0; m < 14; ++m)
#pragma unroll
    for (int e = 0; e < 4; ++e) {
      const unsigned w = xv[m][e];
      const f32x2 n3 = up_bf(rn_pair(up_bf(rn_pair(up_bf(w) - mb)) * rr));
      const int d = (c4 + 4 * m) * 8 + 2 * e;
      *(LAS _Float16*)(tb + d * 136 + row * 2) = (_Float16)n3.x;
      *(LAS _Float16*)(tb + (d + 1) * 136 + row * 2) = (_Float16)n3.y;
    }
  lds_sync(smem + AUX_CNT, 4, lane);
#pragma unroll
  for (int m = 0; m < 14; ++m) {
    const int d = (tid >> 3) + 32 * m, c8 = tid & 7;
    const u32x2 lo = *(LAS const u32x2*)(tb + d * 136 + c8 * 16), hi = *(LAS const u32x2*)(tb + d * 136 + c8 * 16 + 8);
    st16(wsr, WS_XT + (d * SEQ + 64 * v + 8 * c8) * 2, (u32x4){lo[0], lo[1], hi[0], hi[1]});
  }
#pragma unroll
  for (int k = 0; k < 7; ++k) {
    const int fr = v * 4 + wave + 128 * k;
    if (fr < 784)
      st16(wsr, WS_WOF + (fr * 64 + lane) * 16,
           (u32x4){(unsigned)wv[k][0] | ((unsigned)wv[k][1] << 16), (unsigned)wv[k][2] | ((unsigned)wv[k][3] << 16), (unsigned)wv[k][4] | ((unsigned)wv[k][5] << 16),
                   (unsigned)wv[k][6] | ((unsigned)wv[k][7] << 16)});
  }
  WAIT_VM(0);
  lds_sync(smem + AUX_CNT, 8, lane);
  if (tid < 8) st4(wsr, WS_FA + tid * 1024 + (7 * 32 + v) * 4, tag);
}

__device__ __forceinline__ void phaseA_gemm(LAS char* smem, const Params& p, rsrc_t wsr, unsigned tag, int ti, int tj, int T2, int tid) {
  char* ws = p.ws;
  const int lane = tid & 63;
  const int wave = __builtin_amdgcn_readfirstlane(tid >> 6);
  const int rh = wave >> 2, cg = wave & 3, h = lane >> 5, l31 = lane & 31;
  const int xrow = 8 * wave + (lane >> 3);
  const unsigned voffx = (unsigned)(xrow * 896 + (((lane & 7) ^ ((xrow >> 1) & 7)) * 16));
  const char* xsrc = (const char*)p.x + (size_t)ti * 64 * 896;
  unsigned voffw[2];
#pragma unroll
  for (int e = 0; e < 2; ++e) voffw[e] = (unsigned)((8 * wave + 4 * e + (lane >> 4)) * 3584 + (((lane & 15) ^ (4 * (lane >> 4))) * 16) + tj * 256);
#define A_XPIECE(g) glds16(xsrc + (g) * 128, voffx, (unsigned)(A_X + (g) * 8192 + wave * 1024))
#define A_WPIECE(G, e) \
  glds16(p.w_in, voffw[e] + (unsigned)(((G) % 7) * 64 * 3584 + ((G) < 7 ? 1792 : 0)), (unsigned)(A_W + ((G) % 6) * A_SLOT + wave * 2048 + (e) * 1024))
#pragma unroll
  for (int g = 0; g < 6; ++g) { A_XPIECE(g); A_WPIECE(g, 0); A_WPIECE(g, 1); }
  A_XPIECE(6);
  LAS const char* ab[2];
  LAS const char* xb[4];
  {
    const int g4 = lane >> 4, qq = (lane & 15) >> 2, p4 = lane & 3;
    const int lb = (8 * (g4 >> 1) + qq) * 256 + (((4 * cg + 2 * (g4 & 1) + (p4 >> 1)) ^ (4 * qq)) * 16) + (p4 & 1) * 8;
    ab[0] = smem + A_W + lb + rh * 4096;
    ab[1] = smem + A_W + lb + (rh ^ 1) * 4096;
    const int row = 32 * rh + l31, sw = (row >> 1) & 7;
#pragma unroll
    for (int i = 0; i < 4; ++i) xb[i] = smem + A_X + row * 128 + (((2 * (i ^ rh) + h) ^ sw) * 16);
  }
  s16x8 xa[28], wb[8];
#define A_FRAGS(st)                                                                                                    \
  do {                                                                                                                 \
    const int G_ = (st) >> 2, i_ = (st) & 3, so_ = (G_ % 6) * A_SLOT + (i_ >> 1) * 8192;                               \
    if ((st) < 28 && (!(A_ABL & 16) || (st) < 4)) xa[(st) % 28] = *(LAS const s16x8*)(xb[i_] + G_ * 8192);                                            \
    wb[(st) & 7] = __builtin_shufflevector(tr_read(ab[i_ & 1] + so_), tr_read(ab[i_ & 1] + so_ + 1024), 0, 1, 2, 3, 4, 5, 6, 7); \
  } while (0)
  f32x16 acc2 = {}, acc1 = {};
  float w1a = 0.f, w1b = 0.f;
  u32x4 g1;
  const unsigned g1o = WS_XS1 + ti * 1024 + (32 * rh + l31) * 16;
  unsigned zp[8] = {};
  float m1 = 0.f, r1 = 0.f, ss = 0.f, sq = 0.f;
  u32x4 gr;
  const int row = tid >> 3, jj = tid & 7;
  const unsigned vo = WS_XCH + ti * 8192 + row * 128 + (jj < 7 ? jj : 0) * 16;
  WAIT_VM(13);
  BAR();
  STAMP(0, 1);
  A_FRAGS(0); A_FRAGS(1); A_FRAGS(2); A_FRAGS(3);
  auto step = [&](auto stc) __attribute__((always_inline)) {
    constexpr int st = decltype(stc)::value;
    constexpr int G = st >> 2, i = st & 3;
    if (i == 0 && G >= 1) {
      if (G == 1) WAIT_VM(10); else if (G == 2) WAIT_VM(9); else if (G == 3) WAIT_VM(8); else if (G == 4) WAIT_VM(7); else if (G <= 6) WAIT_VM(6);
      else if (G == 7) asm volatile("s_waitcnt vmcnt(3) lgkmcnt(0)" : "+v"(g1)::"memory");
      else if (G == 8) WAIT_VM(6);
      else if (G == 9) WAIT_VM(6); else if (G == 10) WAIT_VM(4);
      else if (G == 11) asm volatile("s_waitcnt vmcnt(2) lgkmcnt(0)" ::: "memory");
      else if (G == 12) WAIT_VM(0);
      if (!(A_ABL & 2)) __builtin_amdgcn_s_barrier();
      asm volatile("" ::: "memory");
      if (G == 7) STAMP(0, 2);
    }
    __builtin_amdgcn_sched_barrier(0);
    if constexpr (st < 28) acc2 = mfma32_bf(wb[st & 7], xa[(A_ABL & 16) ? (st & 3) : st % 28], acc2); else acc1 = mfma32_bf(wb[st & 7], xa[(A_ABL & 16) ? (st & 3) : st % 28], acc1);
    if constexpr (st + 4 < 56) A_FRAGS(st + 4);
    if constexpr ((i & 1) == 0 && !(A_ABL & 32)) {
      const u32x4 wv = __builtin_bit_cast(u32x4, wb[st & 7]);
#pragma unroll
      for (int e = 0; e < 4; ++e) { const unsigned ww = wv[e]; if (st < 28) w1a = dot2bf(ww, ONES_BF, w1a); else w1b = dot2bf(ww, ONES_BF, w1b); }
    }
    if constexpr (st == 27) {
      const float w1s = xsum32(w1a);
      if (h == 0) *(LAS float*)(smem + A_W1 + (rh * 128 + 32 * cg + l31) * 4) = w1s;
    }
    if constexpr (st == 28) {
      poll_granule(g1, wsr, g1o, tag);
      m1 = __uint_as_float(g1[0]); r1 = __uint_as_float(g1[2]);
    }
    if constexpr (G >= 7 && G <= 10 && i == 1 && !(A_ABL & 1)) {
      constexpr int g = G - 7;
      const f32x4 w1 = *(LAS const f32x4*)(smem + A_W1 + (32 * cg + 8 * g + 4 * h) * 4) + *(LAS const f32x4*)(smem + A_W1 + (128 + 32 * cg + 8 * g + 4 * h) * 4);
#pragma unroll
      for (int pp = 0; pp < 2; ++pp) {
        const f32x2 a2 = {acc2[4 * g + 2 * pp], acc2[4 * g + 2 * pp + 1]}, w2v = {w1[2 * pp], w1[2 * pp + 1]};
        const unsigned zz = rn_pair(gelu2(up_bf(rn_pair((a2 - w2v * m1) * r1))));
        zp[2 * g + pp] = zz;
        ss = dot2bf(zz, ONES_BF, ss); sq = dot2bf(zz, zz, sq);
      }
    }
    if constexpr (st == 43) {
      const float s2 = xsum32(ss), q2 = xsum32(sq);
      if (h == 0) *(LAS f32x2*)(smem + A_P + ((32 * rh + l31) * 4 + cg) * 8) = (f32x2){s2, q2};
    }
    if constexpr (st == 44) {
      if (wave == 0) {
        const f32x4 p0 = *(LAS const f32x4*)(smem + A_P + lane * 32), p1 = *(LAS const f32x4*)(smem + A_P + lane * 32 + 16);
        const float s = (p0[0] + p0[2]) + (p1[0] + p1[2]), q = (p0[1] + p0[3]) + (p1[1] + p1[3]);
        u32x4 gr = {__float_as_uint(s), tag, __float_as_uint(q), tag};
        const unsigned vo = WS_XCH + ti * 8192 + lane * 128 + tj * 16;
        asm volatile("buffer_store_dwordx4 %0, %1, %2, 0 offen sc1\n\ts_nop 1" ::"v"(gr), "v"(vo), "s"(wsr) : "memory");
      }
    }
    __builtin_amdgcn_sched_barrier(0);
    if constexpr ((i & 1) == 0 && !(A_ABL & 4)) {
      constexpr int ks = st >> 1;
      if constexpr (ks >= 2 && ks < 18) A_WPIECE(5 + (ks >> 1), ks & 1);
    }
    if constexpr (st == 20) asm volatile("buffer_load_dwordx4 %0, %1, %2, 0 offen sc1" : "=v"(g1) : "v"(g1o), "s"(wsr) : "memory");
    if constexpr (st == 52) asm volatile("buffer_load_dwordx4 %0, %1, %2, 0 offen sc1" : "=v"(gr) : "v"(vo), "s"(wsr) : "memory");
    __builtin_amdgcn_sched_barrier(0);
  };
  static_for<0, 56>(step);
#undef A_FRAGS
#undef A_XPIECE
#undef A_WPIECE
  STAMP(0, 3);
  {
    const float w1s = xsum32(w1b);
    if (h == 0) *(LAS float*)(smem + A_W1B + (rh * 128 + 32 * cg + l31) * 4) = w1s;
  }
  asm volatile("s_waitcnt vmcnt(0)" : "+v"(gr)::"memory");
  poll_granule(gr, wsr, vo, tag);
  {
    const float s = red8(jj < 7 ? __uint_as_float(gr[0]) : 0.f), q = red8(jj < 7 ? __uint_as_float(gr[2]) : 0.f);
    float mb, rr2;
    ln_scalars(s, q, 1.0f / 896.0f, mb, rr2);
    if (jj == 0) { *(LAS float*)(smem + A_M2 + row * 4) = mb; *(LAS float*)(smem + A_R2 + row * 4) = rr2; }
  }
  BAR();
  {
    LAS char* tb = smem + A_TB + wave * 2304;
    const int srow = 32 * rh + l31;
    const float mb2 = *(LAS const float*)(smem + A_M2 + srow * 4), rr2 = *(LAS const float*)(smem + A_R2 + srow * 4);
#pragma unroll
    for (int g = 0; g < 4; ++g) {
      unsigned o[2];
#pragma unroll
      for (int pp = 0; pp < 2; ++pp) o[pp] = rn_pair(up_bf(rn_pair(up_bf(zp[2 * g + pp]) - mb2)) * rr2);
      *(LAS u32x2*)(tb + l31 * 72 + (8 * g + 4 * h) * 2) = (u32x2){o[0], o[1]};
    }
    asm volatile("s_waitcnt lgkmcnt(0)" ::: "memory");
    const int q4 = lane >> 4, l15 = lane & 15;
#pragma unroll
    for (int n = 0; n < 2; ++n) {
      LAS const char* rp = tb + (8 * q4 + (l15 >> 2)) * 72 + n * 32 + (lane & 3) * 8;
      const s16x4 lo = tr_read(rp), hi = tr_read(rp + 4 * 72);
      const s16x8 o = __builtin_shufflevector(lo, hi, 0, 1, 2, 3, 4, 5, 6, 7);
      const int ft = 8 * tj + 2 * cg + n, sb = 2 * ti + rh;
      st16(wsr, WS_Z2F + ((ft * 64 + sb) * 64 + lane) * 16, __builtin_bit_cast(u32x4, o));
    }
  }
  STAMP(0, 4);
  {
    const int tt = 4 * ti + 2 * rh + (l31 >> 4);
    unsigned o[4][2];
    auto part = [&](auto gc) __attribute__((always_inline)) {
      constexpr int g = decltype(gc)::value;
      const f32x4 w1 = *(LAS const f32x4*)(smem + A_W1B + (32 * cg + 8 * g + 4 * h) * 4) + *(LAS const f32x4*)(smem + A_W1B + (128 + 32 * cg + 8 * g + 4 * h) * 4);
#pragma unroll
      for (int pp = 0; pp < 2; ++pp) {
        const f32x2 a2 = {acc1[4 * g + 2 * pp], acc1[4 * g + 2 * pp + 1]}, w2v = {w1[2 * pp], w1[2 * pp + 1]};
        o[g][pp] = rn_pair(gelu2(up_bf(rn_pair((a2 - w2v * m1) * r1))));
      }
    };
    static_for<0, 2>(part);
    __builtin_amdgcn_sched_barrier(0);
    WAIT_VM(0);
    BAR();
    if (tid < 8) st4(wsr, WS_FA + tid * 1024 + (tj * 32 + ti) * 4, tag);
    STAMP(0, 5);
    if (wave < 4) {
      const W2Src w2 = w2_src(p, T2, wave, lane);
#pragma unroll
      for (int k = 0; k < 16; ++k) w2_piece(w2, wave, k >> 3, k & 7);
    }
    static_for<2, 4>(part);
#pragma unroll
    for (int g = 0; g < 4; ++g) {
      const int ft = 8 * tj + 2 * cg + (g >> 1), lp = (lane & 15) + 16 * (2 * (g & 1) + h);
      st8(wsr, WS_Z1D + ((tt * 56 + ft) * 64 + lp) * 8, (u32x2){o[g][0], o[g][1]});
    }
  }
  WAIT_VM(0);
  BAR();
  if (tid == 0) st4(wsr, WS_FZ + (tj * 32 + ti) * 4, tag);
}

constexpr int B_CH = 65536;
constexpr int B_CHS = 49152;
constexpr int CH_FT = 0;
constexpr int CH_FW = 16416, CH_FS = 288;
constexpr int CH_CS = 4160;
constexpr int CH_U = 33280;
constexpr int CH_T = CH_U + 4608;
constexpr int CH_TS = 49 * 64;
constexpr int CH_CNT = CH_T + 2 * CH_TS;

template <int W>
__device__ __forceinline__ void b_epi(LAS char* smem, rsrc_t wsr, f32x4 (&acc)[7][4], const u32x2 (&z1v)[7], float bs, int lane, int tt, int J) {
  LAS char* cnt = smem + B_CH + CH_CNT + 64;
  LAS char* mine = smem + B_RING + W * 16384 + lane * 16;
  LAS char* other = smem + B_RING + (W ^ 2) * 16384 + lane * 16;
  LAS char* o2 = smem + B_RING + (W ^ 3) * 16384 + lane * 16;
  constexpr int K0 = W & 2, S0 = K0 ^ 2;
#pragma unroll
  for (int n = 0; n < 7; ++n) { *(LAS f32x4*)(mine + n * 1024) = acc[n][S0]; *(LAS f32x4*)(mine + (7 + n) * 1024) = acc[n][S0 + 1]; }
  lds_sync(cnt, 4, lane);
#pragma unroll
  for (int n = 0; n < 7; ++n) { acc[n][K0] += *(LAS const f32x4*)(other + n * 1024); acc[n][K0 + 1] += *(LAS const f32x4*)(other + (7 + n) * 1024); }
  asm volatile("s_waitcnt lgkmcnt(0)" ::: "memory");
#pragma unroll
  for (int n = 0; n < 7; ++n) *(LAS f32x4*)(other + n * 1024) = acc[n][W ^ 1];
  lds_sync(cnt, 8, lane);
  unsigned hp[7][2];
#pragma unroll
  for (int n = 0; n < 7; ++n) {
    const f32x4 v = acc[n][W] + *(LAS const f32x4*)(o2 + n * 1024);
#pragma unroll
    for (int pp = 0; pp < 2; ++pp) {
      const f32x2 g0 = up_bf(rn_pair((f32x2){v[2 * pp], v[2 * pp + 1]})) + bs;
      const unsigned zw = z1v[n][pp];
      hp[n][pp] = rn_pair(up_bf(rn_pair(g0)) * up_bf(zw));
    }
  }
  const unsigned hb = WS_HF + ((tt * 28 + (7 * J >> 1)) * 64 + lane) * 16;
  if (!(J & 1)) {
#pragma unroll
    for (int pp = 0; pp < 3; ++pp) st16(wsr, hb + pp * 1024, (u32x4){hp[2 * pp][0], hp[2 * pp][1], hp[2 * pp + 1][0], hp[2 * pp + 1][1]});
    st8(wsr, hb + 3 * 1024, (u32x2){hp[6][0], hp[6][1]});
  } else {
    st8(wsr, hb + 8, (u32x2){hp[0][0], hp[0][1]});
#pragma unroll
    for (int pp = 0; pp < 3; ++pp) st16(wsr, hb + (pp + 1) * 1024, (u32x4){hp[2 * pp + 1][0], hp[2 * pp + 1][1], hp[2 * pp + 2][0], hp[2 * pp + 2][1]});
  }
}

__device__ __forceinline__ void phaseB_gemm(LAS char* smem, const Params& p, rsrc_t wsr, unsigned tag, int rep, int wave, int lane, int T, int J) {
  char* ws = p.ws;
  const rsrc_t zs = mk_rsrc(ws + WS_Z2F + (size_t)(7 * J * 64 + 16 * wave) * 1024, 7 * 65536 - 16 * wave * 1024);
  const W2Src w2 = w2_src(p, T, wave, lane);
  const unsigned voffz = lane * 16;
  const unsigned ring = B_RING + wave * 16384;
  LAS const char* wrd0 = smem + ring + (lane & 15) * 128 + (((lane >> 4) ^ ((lane >> 1) & 7)) * 16);
  LAS const char* wrd1 = smem + ring + (lane & 15) * 128 + (((4 + (lane >> 4)) ^ ((lane >> 1) & 7)) * 16);
  u32x4 z[3][7];
  f32x4 acc[7][4];
  {
    const int tj0 = (112 * J) >> 7, tj1 = (112 * J + 111) >> 7;
    group_wait(wave == 0, wsr, WS_FA + rep * 1024 + (lane < 32 ? tj0 : tj1) * 128 + (lane & 31) * 4, tag, smem + B_CH + CH_CNT + 72);
  }
  STAMP(1, 0);
#define B_ISSUE_Z(it) _Pragma("unroll") for (int n = 0; n < 7; ++n) bload16(z[(it) % 3][n], zs, voffz, n * 65536 + (it) * 1024)
  B_ISSUE_Z(0); B_ISSUE_Z(1); B_ISSUE_Z(2);
#undef B_ISSUE_Z
  const int tt = 4 * T + wave;
  u32x2 z1v[7];
  unsigned bsw, fz;
#define B_WAITZ(N) asm volatile("s_waitcnt vmcnt(" #N ")" : "+v"(zc[0]), "+v"(zc[1]), "+v"(zc[2]), "+v"(zc[3]), "+v"(zc[4]), "+v"(zc[5]), "+v"(zc[6])::"memory")
#pragma unroll
  for (int it = 0; it < 16; ++it) {
    u32x4(&zc)[7] = z[it % 3];
    if (it <= 1) B_WAITZ(14); else if (it <= 13) B_WAITZ(22); else if (it == 14) B_WAITZ(15); else B_WAITZ(8);
    s16x8 wf[4];
#pragma unroll
    for (int m = 0; m < 4; ++m) wf[m] = *(LAS const s16x8*)(((it & 1) ? wrd1 : wrd0) + ((it >> 1) & 1) * 8192 + m * 2048);
#pragma unroll
    for (int n = 0; n < 7; ++n) {
#pragma unroll
      for (int m = 0; m < 4; ++m) {
        if (it == 0) acc[n][m] = mfma_bf(__builtin_bit_cast(s16x8, zc[n]), wf[m], (f32x4){0.f, 0.f, 0.f, 0.f});
        else mfma_acc(acc[n][m], zc[n], wf[m]);
      }
      __builtin_amdgcn_sched_barrier(0);
      if (it + 3 < 16) {
        bload16(zc[n], zs, voffz, n * 65536 + (it + 3) * 1024);
        if ((it & 1) && n >= 1 && n <= 4) { w2_piece(w2, wave, (it + 3) >> 1, 2 * (n - 1)); w2_piece(w2, wave, (it + 3) >> 1, 2 * (n - 1) + 1); }
      }
      if (it == 9 && n == 6) {
        int ln = lane; asm volatile("" : "+v"(ln));
        const unsigned fzo = WS_FZ + (((ln & 1) ? (112 * J + 111) >> 7 : (112 * J) >> 7) * 32 + T) * 4;
        asm volatile("buffer_load_dword %0, %1, %2, 0 offen sc1" : "=v"(fz) : "v"(fzo), "s"(wsr) : "memory");
      }
      if (it == 13) {
        if (n == 0) {
          asm volatile("" : "+v"(fz));
          if (!__all(fz == tag)) {
            int ln = lane; asm volatile("" : "+v"(ln));
            poll_flags(wsr, WS_FZ + (((ln & 1) ? (112 * J + 111) >> 7 : (112 * J) >> 7) * 32 + T) * 4, tag);
          }
        }
        int l8 = lane; asm volatile("" : "+v"(l8));
        const unsigned vo = WS_Z1D + (tt * 56 + 7 * J + n) * 512 + l8 * 8;
        asm volatile("buffer_load_dwordx2 %0, %1, %2, 0 offen sc1" : "=v"(z1v[n]) : "v"(vo), "s"(wsr) : "memory");
        if (n == 6) {
          const unsigned vb = (unsigned)(16 * tt + (l8 & 15)) * 2u;
          asm volatile("global_load_ushort %0, %1, %2" : "=v"(bsw) : "v"(vb), "s"(p.b_sp) : "memory");
        }
      }
      __builtin_amdgcn_sched_barrier(0);
    }
  }
#undef B_WAITZ
  MFMA_DONE();
  STAMP(1, 1);
  asm volatile("s_waitcnt vmcnt(0)" : "+v"(z1v[0]), "+v"(z1v[1]), "+v"(z1v[2]), "+v"(z1v[3]), "+v"(z1v[4]), "+v"(z1v[5]), "+v"(z1v[6]), "+v"(bsw)::"memory");
  const float bs = bf2f((unsigned short)bsw);
  if (wave == 0) b_epi<0>(smem, wsr, acc, z1v, bs, lane, tt, J);
  else if (wave == 1) b_epi<1>(smem, wsr, acc, z1v, bs, lane, tt, J);
  else if (wave == 2) b_epi<2>(smem, wsr, acc, z1v, bs, lane, tt, J);
  else b_epi<3>(smem, wsr, acc, z1v, bs, lane, tt, J);
  STAMP(1, 2);
}

__device__ __forceinline__ f32x2 cmul(f32x2 a, f32x2 b) {
  f32x2 t, r;
  asm("v_pk_mul_f32 %0, %1, %2 op_sel:[0,0] op_sel_hi:[0,1]" : "=v"(t) : "v"(a), "v"(b));
  asm("v_pk_fma_f32 %0, %1, %2, %3 op_sel:[1,1,0] op_sel_hi:[1,0,1] neg_lo:[1,0,0]" : "=v"(r) : "v"(a), "v"(b), "v"(t));
  return r;
}
__device__ __forceinline__ unsigned hmul(unsigned a, unsigned b) {
  unsigned t, r;
  asm("v_pk_mul_f16 %0, %1, %2 op_sel:[0,0] op_sel_hi:[0,1]" : "=v"(t) : "v"(a), "v"(b));
  asm("v_pk_fma_f16 %0, %1, %2, %3 op_sel:[1,1,0] op_sel_hi:[1,0,1] neg_lo:[1,0,0]" : "=v"(r) : "v"(a), "v"(b), "v"(t));
  return r;
}
__device__ __forceinline__ unsigned hcv(f32x2 v) { return pk_f16(v.x, v.y); }
__device__ __forceinline__ f32x2 cexp_p(float al, float be, float pp) {
  const float e = __builtin_amdgcn_exp2f(al * pp), th = __builtin_amdgcn_fractf(be * pp);
  return (f32x2){__builtin_amdgcn_cosf(th), __builtin_amdgcn_sinf(th)} * e;
}
struct PosTab { unsigned short v[2064]; };
constexpr int pos_bf16(int j) {
  if (j < 256) return j;
  int sh = 1; while ((j >> sh) >= 256) ++sh;
  const int q = j >> sh, r = j & ((1 << sh) - 1), h = 1 << (sh - 1);
  return (r > h || (r == h && (q & 1)) ? q + 1 : q) << sh;
}
constexpr PosTab make_postab() {
  PosTab t = {};
  for (int x = 0; x < 2048; ++x) {
    const int p = pos_bf16(2047 - x);
    int col = 0, a = 0;
    if (p < 256) { a = p >> 4; col = p & 15; }
    else if (p < 512) { const int r = p - 256; a = r >> 4; col = 16 + ((r & 15) >> 1); }
    else if (p < 1024) { const int r = p - 512; a = (r & 255) >> 4; col = 24 + 4 * (r >> 8) + ((r & 15) >> 2); }
    else if (p < 2048) { const int r = p - 1024; a = (r & 255) >> 4; col = 32 + 2 * (r >> 8) + ((r & 15) >> 3); }
    else { a = 0; col = 40; }
    t.v[x] = (unsigned short)((col * 16 + a) * 4);
  }
  for (int x = 2048; x < 2064; ++x) t.v[x] = 48 * 64;
  return t;
}
__device__ const PosTab POSTAB = make_postab();

template <int HW>
__device__ __forceinline__ void s4d_conv(LAS char* cb, rsrc_t wsr, int d, int lane, float iscale, float dsk) {
  constexpr int NF = HW ? 7 : 8;
  f32x4 acc[8] = {};
  const int r = lane & 15, h = lane >> 4;
  LAS const char* ab = cb + CH_FT + ((7 - r) & 7) * CH_CS + (255 - (r >> 3) - 2 * (h >> 1) + (h & 1)) * 16 - (64 * 7 + 512 * 7);
  LAS const char* bb = cb + CH_U + 512 + 32 * r - 32 * (h >> 1) + 16 * (h & 1) - 64 * 7;
  u32x4 A[2][8], B[2][8];
#define CONV_LOAD(ar, buf)                                                                                   \
  _Pragma("unroll") for (int k = 0; k < NF; ++k) {                                                           \
    B[buf][k] = *(LAS const u32x4*)(bb + 512 * k + 64 * (7 - (ar)));                                         \
    A[buf][k] = *(LAS const u32x4*)(ab + 512 * (7 - k) + 64 * (7 - (ar)));                                   \
  }
  CONV_LOAD(0, 0);
#pragma unroll
  for (int ar = 0; ar < 8; ++ar) {
    __builtin_amdgcn_sched_barrier(0);
    if (ar < 7) { CONV_LOAD(ar + 1, (ar + 1) & 1); }
#pragma unroll
    for (int al = 0; al < NF; ++al)
#pragma unroll
      for (int be = 0; be < NF; ++be) {
        const int tau = al + be;
        if (HW ? (tau >= 4 && tau <= 6) : (tau <= 3 || tau == 7)) acc[tau] = mfma_h(A[ar & 1][al], B[ar & 1][be], acc[tau]);
      }
#pragma unroll
    for (int i = 0; i < 16; ++i) { __builtin_amdgcn_sched_group_barrier(0x008, 1, 0); __builtin_amdgcn_sched_group_barrier(0x100, 1, 0); }
    __builtin_amdgcn_sched_barrier(0);
  }
#undef CONV_LOAD
  const int q = lane >> 4;
#pragma unroll
  for (int t = 0; t < 8; ++t) {
    if (HW ? (t >= 4 && t <= 6) : (t <= 3 || t == 7)) {
      const f32x4 v = acc[t];
      const int l0 = 256 * t + 16 * r + 4 * q;
      const u32x2 uw = *(LAS const u32x2*)(cb + CH_U + 512 + l0 * 2);
      unsigned o[2];
#pragma unroll
      for (int pp = 0; pp < 2; ++pp) {
        const unsigned uwp = uw[pp];
        const f16x2 ua = __builtin_bit_cast(f16x2, uwp);
        const f32x2 y2 = up_bf(rn_pair((f32x2){v[2 * pp], v[2 * pp + 1]} * iscale)), k2 = up_bf(rn_pair((f32x2){(float)ua[0], (float)ua[1]} * dsk));
        o[pp] = rn_pair(y2 + k2);
      }
      st8(wsr, WS_ST + (d * SEQ + l0) * 2, (u32x2){o[0], o[1]});
    }
  }
}

__device__ __forceinline__ void phaseB_s4d(LAS char* smem, LAS char* cb, const Params& p, rsrc_t wsr, unsigned tag, int rep, bool poller, int d, int hw, int lane) {
  char* ws = p.ws;
  LAS char* cnt = cb + CH_CNT;
  unsigned la_[2], ai_[2], cr_[4], ci_[4], ldt_, dsk_;
  u32x4 pt0, pt1, pt2;
  {
    const unsigned vo = (unsigned)(d * NST + 128 * hw + lane) * 2u, vo2 = (unsigned)(d * NST + ((128 * hw + 128 + lane) & 255)) * 2u, vd = (unsigned)d * 2u;
    asm volatile("global_load_ushort %0, %1, %2" : "=v"(la_[0]) : "v"(vo), "s"(p.logA) : "memory");
    asm volatile("global_load_ushort %0, %1, %2 offset:128" : "=v"(la_[1]) : "v"(vo), "s"(p.logA) : "memory");
    asm volatile("global_load_ushort %0, %1, %2" : "=v"(ai_[0]) : "v"(vo), "s"(p.Aim) : "memory");
    asm volatile("global_load_ushort %0, %1, %2 offset:128" : "=v"(ai_[1]) : "v"(vo), "s"(p.Aim) : "memory");
    asm volatile("global_load_ushort %0, %1, %2" : "=v"(cr_[0]) : "v"(vo), "s"(p.Cre) : "memory");
    asm volatile("global_load_ushort %0, %1, %2 offset:128" : "=v"(cr_[1]) : "v"(vo), "s"(p.Cre) : "memory");
    asm volatile("global_load_ushort %0, %1, %2" : "=v"(cr_[2]) : "v"(vo2), "s"(p.Cre) : "memory");
    asm volatile("global_load_ushort %0, %1, %2 offset:128" : "=v"(cr_[3]) : "v"(vo2), "s"(p.Cre) : "memory");
    asm volatile("global_load_ushort %0, %1, %2" : "=v"(ci_[0]) : "v"(vo), "s"(p.Cim) : "memory");
    asm volatile("global_load_ushort %0, %1, %2 offset:128" : "=v"(ci_[1]) : "v"(vo), "s"(p.Cim) : "memory");
    asm volatile("global_load_ushort %0, %1, %2" : "=v"(ci_[2]) : "v"(vo2), "s"(p.Cim) : "memory");
    asm volatile("global_load_ushort %0, %1, %2 offset:128" : "=v"(ci_[3]) : "v"(vo2), "s"(p.Cim) : "memory");
    asm volatile("global_load_ushort %0, %1, %2" : "=v"(ldt_) : "v"(vd), "s"(p.logdt) : "memory");
    asm volatile("global_load_ushort %0, %1, %2" : "=v"(dsk_) : "v"(vd), "s"(p.Dsk) : "memory");
    const char* pb = (const char*)POSTAB.v + hw * 2048;
    const unsigned vp = lane * 32;
    asm volatile("global_load_dwordx4 %0, %1, %2" : "=v"(pt0) : "v"(vp), "s"(pb) : "memory");
    asm volatile("global_load_dwordx4 %0, %1, %2 offset:16" : "=v"(pt1) : "v"(vp), "s"(pb) : "memory");
    asm volatile("global_load_dwordx4 %0, %1, %2 offset:32" : "=v"(pt2) : "v"(vp), "s"(pb) : "memory");
    if (hw == 0 && lane < 32) *(LAS u32x4*)(cb + CH_U + lane * 16) = (u32x4){0u, 0u, 0u, 0u};
    if (lane < 4) *(LAS u32x4*)(cb + CH_T + hw * CH_TS + 48 * 64 + lane * 16) = (u32x4){0u, 0u, 0u, 0u};
  }
  BAR();
  __builtin_amdgcn_s_setprio(3);
  asm volatile("s_waitcnt vmcnt(0)" : "+v"(la_[0]), "+v"(la_[1]), "+v"(ai_[0]), "+v"(ai_[1]), "+v"(cr_[0]), "+v"(cr_[1]), "+v"(cr_[2]), "+v"(cr_[3]), "+v"(ci_[0]), "+v"(ci_[1]),
               "+v"(ci_[2]), "+v"(ci_[3]), "+v"(ldt_), "+v"(dsk_), "+v"(pt0), "+v"(pt1), "+v"(pt2)::"memory");
  STAMP(1, 0);
  float cr[4], ci[4];
#pragma unroll
  for (int k = 0; k < 4; ++k) { cr[k] = bf2f((unsigned short)cr_[k]); ci[k] = bf2f((unsigned short)ci_[k]); }
  const float dt = rbf(__builtin_amdgcn_exp2f(bf2f((unsigned short)ldt_) * 1.4426950408889634f)), dsk = bf2f((unsigned short)dsk_);
  float sabs = 0.f;
#pragma unroll
  for (int k = 0; k < 4; ++k) sabs += __builtin_fabsf(cr[k]) + __builtin_fabsf(ci[k]);
  sabs = xsum16_32(red16(sabs));
  sabs *= 2.0f * dt;
  int ex = (int)((__float_as_uint(sabs) >> 23) & 0xff) - 126;
  ex = ex < -100 ? -100 : (ex > 100 ? 100 : ex);
  const float scale = __uint_as_float((unsigned)(127 + 15 - ex) << 23), iscale = __uint_as_float((unsigned)(127 - 15 + ex) << 23);
  f32x4 kacc[3] = {};
  LAS char* fw = cb + CH_FT + hw * CH_FW;
#pragma unroll
  for (int ps = 0; ps < 2; ++ps) {
    const float lap = bf2f((unsigned short)la_[ps]), aip = bf2f((unsigned short)ai_[ps]);
    const float crp = cr[ps], cip = ci[ps];
    const float lre = -rbf(__builtin_amdgcn_exp2f(lap * 1.4426950408889634f)) * dt, lim = aip * dt;
    const float al = lre * 1.4426950408889634f, be = lim * 0.15915494309189535f;
    LAS char* wp = fw + lane * 4;
#define PUTH(row, v) *(LAS unsigned*)(wp + (row) * CH_FS) = (v)
    {
      const f32x2 w = cexp_p(al, be, 16.f);
      const float cs = 2.0f * dt * scale;
      const f32x2 c0 = {crp * cs, cip * cs};
      const f32x2 w2 = cmul(w, w), c1 = cmul(c0, w), w4 = cmul(w2, w2), c2 = cmul(c0, w2), c3 = cmul(c1, w2), w8 = cmul(w4, w4), w12 = cmul(w8, w4);
      const unsigned hc[4] = {hcv(c0), hcv(c1), hcv(c2), hcv(c3)}, hw4[3] = {hcv(w4), hcv(w8), hcv(w12)};
#pragma unroll
      for (int j = 0; j < 4; ++j) {
        PUTH(j, hc[j]);
#pragma unroll
        for (int k = 0; k < 3; ++k) PUTH(4 * (k + 1) + j, hmul(hc[j], hw4[k]));
      }
    }
    {
      const f32x2 e1 = cexp_p(al, -be, 1.f);
      const f32x2 e2 = cmul(e1, e1), e3 = cmul(e2, e1), e4 = cmul(e2, e2), e8 = cmul(e4, e4), e12 = cmul(e8, e4);
      unsigned E[16];
      E[0] = 0x00003c00u; E[1] = hcv(e1); E[2] = hcv(e2); E[3] = hcv(e3); E[4] = hcv(e4); E[8] = hcv(e8); E[12] = hcv(e12);
#pragma unroll
      for (int m = 1; m < 4; ++m)
#pragma unroll
        for (int j = 1; j < 4; ++j) E[4 * m + j] = hmul(E[4 * m], E[j]);
#pragma unroll
      for (int k = 0; k < 16; ++k) PUTH(16 + k, E[k]);
      const f32x2 z256 = cexp_p(al, -be, 256.f), z512 = cexp_p(al, -be, 512.f), z1024 = cexp_p(al, -be, 1024.f), z2048 = cexp_p(al, -be, 2048.f);
      const f32x2 z768 = cmul(z512, z256);
      const unsigned h256 = hcv(z256), h512 = hcv(z512), h768 = hcv(z768);
      PUTH(32, h256);
#pragma unroll
      for (int k = 1; k < 8; ++k) PUTH(32 + k, hmul(h256, E[2 * k]));
      PUTH(40, h512); PUTH(44, h768);
#pragma unroll
      for (int k = 1; k < 4; ++k) { PUTH(40 + k, hmul(h512, E[4 * k])); PUTH(44 + k, hmul(h768, E[4 * k])); }
      const unsigned hb[4] = {hcv(z1024), hcv(cmul(z1024, z256)), hcv(cmul(z1024, z512)), hcv(cmul(z1024, z768))};
#pragma unroll
      for (int k = 0; k < 4; ++k) { PUTH(48 + 2 * k, hb[k]); PUTH(49 + 2 * k, hmul(hb[k], E[8])); }
      PUTH(56, hcv(z2048));
    }
#undef PUTH
    asm volatile("s_waitcnt lgkmcnt(0)" ::: "memory");
    LAS const char* rp = fw + (lane & 15) * CH_FS + (lane >> 4) * 16;
    u32x4 fa[4], fb[4][3];
#pragma unroll
    for (int kk = 0; kk < 4; ++kk) {
      fa[kk] = *(LAS const u32x4*)(rp + kk * 64);
#pragma unroll
      for (int t = 0; t < 3; ++t) fb[kk][t] = *(LAS const u32x4*)(rp + (16 + 16 * t) * CH_FS + kk * 64);
    }
#pragma unroll
    for (int kk = 0; kk < 4; ++kk)
#pragma unroll
      for (int t = 0; t < 3; ++t) kacc[t] = mfma_h(fa[kk], fb[kk][t], kacc[t]);
    asm volatile("" ::: "memory");
  }
  STAMP(1, 1);
  u32x4 u0, u1;
  group_wait(poller, wsr, WS_FA + rep * 1024 + 7 * 128 + (lane & 31) * 4, tag, smem + B_CH + CH_CNT + 76);
  {
    const unsigned vu = WS_XT + d * SEQ * 2 + hw * 2048 + lane * 16;
    asm volatile("buffer_load_dwordx4 %0, %1, %2, 0 offen sc1" : "=v"(u0) : "v"(vu), "s"(wsr) : "memory");
    asm volatile("buffer_load_dwordx4 %0, %1, %2, 0 offen offset:1024 sc1" : "=v"(u1) : "v"(vu), "s"(wsr) : "memory");
  }
#pragma unroll
  for (int t = 0; t < 3; ++t) *(LAS f32x4*)(cb + CH_T + hw * CH_TS + (16 * t + (lane & 15)) * 64 + (lane >> 4) * 16) = kacc[t];
  lds_sync(cnt, 2, lane);
  {
    const int y = 2 * (64 * hw + lane);
    const unsigned po[12] = {pt0[0], pt0[1], pt0[2], pt0[3], pt1[0], pt1[1], pt1[2], pt1[3], pt2[0], pt2[1], pt2[2], pt2[3]};
    unsigned W[12], V[11];
#pragma unroll
    for (int k = 0; k < 12; ++k) {
      LAS const char* a0 = cb + CH_T + (po[k] & 0xffffu);
      LAS const char* a1 = cb + CH_T + (po[k] >> 16);
      W[k] = pk_f16(*(LAS const float*)a0 + *(LAS const float*)(a0 + CH_TS), *(LAS const float*)a1 + *(LAS const float*)(a1 + CH_TS));
    }
#pragma unroll
    for (int i = 0; i < 11; ++i) V[i] = __builtin_amdgcn_alignbit(W[i + 1], W[i], 16);
#pragma unroll
    for (int m = 0; m < 4; ++m) {
      *(LAS u32x4*)(cb + CH_FT + (2 * m) * CH_CS + y * 16) = (u32x4){W[m], W[m + 1], W[m + 2], W[m + 3]};
      *(LAS u32x4*)(cb + CH_FT + (2 * m) * CH_CS + y * 16 + 16) = (u32x4){W[m + 4], W[m + 5], W[m + 6], W[m + 7]};
      *(LAS u32x4*)(cb + CH_FT + (2 * m + 1) * CH_CS + y * 16) = (u32x4){V[m], V[m + 1], V[m + 2], V[m + 3]};
      *(LAS u32x4*)(cb + CH_FT + (2 * m + 1) * CH_CS + y * 16 + 16) = (u32x4){V[m + 4], V[m + 5], V[m + 6], V[m + 7]};
    }
    if (hw == 1 && lane < 8) *(LAS u32x4*)(cb + CH_FT + lane * CH_CS + 256 * 16) = (u32x4){0u, 0u, 0u, 0u};
  }
  asm volatile("s_waitcnt vmcnt(0)" : "+v"(u0), "+v"(u1)::"memory");
  *(LAS u32x4*)(cb + CH_U + 512 + hw * 2048 + lane * 16) = u0;
  *(LAS u32x4*)(cb + CH_U + 512 + hw * 2048 + 1024 + lane * 16) = u1;
  lds_sync(cnt, 4, lane);
  STAMP(1, 2);
  if (hw) s4d_conv<1>(cb, wsr, d, lane, iscale, dsk); else s4d_conv<0>(cb, wsr, d, lane, iscale, dsk);
  __builtin_amdgcn_s_setprio(0);
  STAMP(1, 3);
}
#define CODEWARM 0

constexpr int C_PART = 0;
constexpr int C_XS = 65536;
constexpr int C_SS = C_XS + 64 * 136;
constexpr int C_OS = C_SS + 64 * 136;

constexpr int C_GO = C_OS + 64 * 144;
__device__ __forceinline__ void phaseC(LAS char* smem, const Params& p, rsrc_t wsr, unsigned tag, int rep, int T, int Dt, int tid) {
  char* ws = p.ws;
  const int lane = tid & 63;
  const int wave = __builtin_amdgcn_readfirstlane(tid >> 6);
  unsigned fo;
  {
    const int J = lane & 7, bh = 2 * (T >> 3) + (J >> 2) + 8 * ((T & 7) + 8 * (J & 3));
    const int bs = 32 * Dt + ((lane - 8) & 31);
    fo = WS_FB + rep * 1024 + (lane < 8 ? bh : bs) * 4;
  }
  if (wave < 4) {
    const unsigned ho = WS_HF + ((4 * T * 28 + 7 * wave) * 64 + lane) * 16, wo = WS_WOF + ((4 * Dt * 28 + 7 * wave) * 64 + lane) * 16;
    u32x4 hf[7][4], wf[7][4];
    f32x4 acc[4][4];
#pragma unroll
    for (int k = 0; k < 7; ++k)
#pragma unroll
      for (int m = 0; m < 4; ++m) wf[k][m] = ld16(wsr, wo + (m * 28 + k) * 1024);
    __builtin_amdgcn_sched_barrier(0);
    group_wait(false, wsr, fo, tag, smem + C_GO);
    STAMP(2, 0);
#pragma unroll
    for (int n = 0; n < 4; ++n)
#pragma unroll
      for (int m = 0; m < 4; ++m) acc[n][m] = (f32x4){0.f, 0.f, 0.f, 0.f};
#pragma unroll
    for (int k = 0; k < 4; ++k)
#pragma unroll
      for (int m = 0; m < 4; ++m) hf[k][m] = ld16(wsr, ho + (m * 28 + k) * 1024);
    __builtin_amdgcn_sched_barrier(0);
#pragma unroll
    for (int k = 0; k < 7; ++k) {
#pragma unroll
      for (int n = 0; n < 4; ++n)
#pragma unroll
        for (int m = 0; m < 4; ++m) acc[n][m] = mfma_bf(__builtin_bit_cast(s16x8, wf[k][n]), __builtin_bit_cast(s16x8, hf[k][m]), acc[n][m]);
      __builtin_amdgcn_sched_barrier(0);
      if (k + 4 < 7) {
#pragma unroll
        for (int m = 0; m < 4; ++m) hf[k + 4][m] = ld16(wsr, ho + (m * 28 + k + 4) * 1024);
      }
      __builtin_amdgcn_sched_barrier(0);
    }
    STAMP(2, 1);
#pragma unroll
    for (int n = 0; n < 4; ++n)
#pragma unroll
      for (int m = 0; m < 4; ++m) *(LAS f32x4*)(smem + C_PART + ((wave * 4 + m) * 4 + n) * 1024 + lane * 16) = acc[n][m];
  } else {
    const int u = tid - 256, row = u >> 2, c = u & 3;
    const u32x4 x0 = *(const u32x4*)(p.x + (size_t)(64 * T + row) * DM + 64 * Dt + 16 * c);
    const u32x4 x1 = *(const u32x4*)(p.x + (size_t)(64 * T + row) * DM + 64 * Dt + 16 * c + 8);
#pragma unroll
    for (int e = 0; e < 2; ++e) {
      *(LAS u32x2*)(smem + C_XS + row * 136 + c * 32 + e * 8) = (u32x2){x0[2 * e], x0[2 * e + 1]};
      *(LAS u32x2*)(smem + C_XS + row * 136 + c * 32 + 16 + e * 8) = (u32x2){x1[2 * e], x1[2 * e + 1]};
    }
    group_wait(wave == 4, wsr, fo, tag, smem + C_GO);
    const unsigned so = WS_ST + ((64 * Dt + row) * SEQ + 64 * T + 16 * c) * 2;
    const u32x4 s0 = ld16(wsr, so), s1 = ld16(wsr, so + 16);
#pragma unroll
    for (int e = 0; e < 2; ++e) {
      *(LAS u32x2*)(smem + C_SS + row * 136 + c * 32 + e * 8) = (u32x2){s0[2 * e], s0[2 * e + 1]};
      *(LAS u32x2*)(smem + C_SS + row * 136 + c * 32 + 16 + e * 8) = (u32x2){s1[2 * e], s1[2 * e + 1]};
    }
  }
  BAR();
  {
    const int m = wave & 3, l15 = lane & 15, q = lane >> 4, t = 16 * m + l15;
#pragma unroll
    for (int nn = 0; nn < 2; ++nn) {
      const int n = 2 * (wave >> 2) + nn;
      f32x4 o = *(LAS const f32x4*)(smem + C_PART + ((0 * 4 + m) * 4 + n) * 1024 + lane * 16);
#pragma unroll
      for (int sw = 1; sw < 4; ++sw) o += *(LAS const f32x4*)(smem + C_PART + ((sw * 4 + m) * 4 + n) * 1024 + lane * 16);
      const s16x4 sv = tr_read(smem + C_SS + (16 * n + 4 * q + (l15 >> 2)) * 136 + (16 * m + 4 * (lane & 3)) * 2);
      const u32x2 sw2 = __builtin_bit_cast(u32x2, sv);
      const u32x2 xw = *(LAS const u32x2*)(smem + C_XS + t * 136 + (16 * n + 4 * q) * 2);
      unsigned ow[2];
#pragma unroll
      for (int pp = 0; pp < 2; ++pp) {
        const f32x2 v = up_bf(rn_pair((f32x2){o[2 * pp], o[2 * pp + 1]}));
        const f32x2 e = v * -1.4426950408889634f;
        const f32x2 dd = (f32x2){__builtin_amdgcn_exp2f(e.x), __builtin_amdgcn_exp2f(e.y)} + 1.0f;
        const f32x2 g = up_bf(rn_pair(v * (f32x2){__builtin_amdgcn_rcpf(dd.x), __builtin_amdgcn_rcpf(dd.y)}));
        const unsigned s_ = sw2[pp], x_ = xw[pp];
        ow[pp] = rn_pair(up_bf(x_) + up_bf(rn_pair(up_bf(s_) * g)));
      }
      *(LAS u32x2*)(smem + C_OS + t * 144 + (16 * n + 4 * q) * 2) = (u32x2){ow[0], ow[1]};
    }
  }
  BAR();
  {
    const int row = tid >> 3, c = tid & 7;
    *(u32x4*)(p.out + (size_t)(64 * T + row) * DM + 64 * Dt + 8 * c) = *(LAS const u32x4*)(smem + C_OS + row * 144 + c * 16);
  }
}

__global__ __launch_bounds__(512, 2) void mixer_block(Params p) {
  extern __shared__ __attribute__((aligned(16))) char smem_raw[];
  LAS char* smem = (LAS char*)smem_raw;
  char* ws = p.ws;
  const int tid = threadIdx.x, lane = tid & 63;
  const int wave = __builtin_amdgcn_readfirstlane(tid >> 6);
  const int b = blockIdx.x, xcd = b & 7, rr = b >> 3;
  const rsrc_t wsr = mk_rsrc(ws, WS_END);
  STAMP(0, 0);
  const unsigned tag = *(const __attribute__((address_space(4))) unsigned*)(ws + WS_EP) + 1u;
  const int T2 = 8 * (xcd >> 1) + (rr & 7), J2 = 4 * (xcd & 1) + (rr >> 3);
  const bool aux = b >= 224;
  if (!aux) {
    phaseA_gemm(smem, p, wsr, tag, 4 * xcd + rr / 7, rr % 7, T2, tid);
    if (tid < 64) {
      if (lane < 32) *(LAS unsigned*)(smem + B_CH + CH_CNT + lane * 4) = 0u;
      else *(LAS unsigned*)(smem + B_CH + B_CHS + CH_CNT + (lane - 32) * 4) = 0u;
    }
  } else {
    if (tid < 32) *(LAS unsigned*)(smem + B_CH + CH_CNT + tid * 4) = 0u;
    BAR();
    if (wave < 4) {
      phaseA_aux(smem, p, wsr, tag, b - 224, tid);
      const W2Src w2 = w2_src(p, T2, wave, lane);
#pragma unroll
      for (int k = 0; k < 16; ++k) w2_piece(w2, wave, k >> 3, k & 7);
    } else {
#pragma unroll 8
      for (int k = 0; k < 64; ++k) {
        glds16((const char*)p.w_sp + (size_t)(k * 128 + (b - 224) * 4 + (wave - 4)) * 1024, (unsigned)lane * 16u, (unsigned)(AUX_PF + (wave - 4) * 1024));
        if ((k & 7) == 7) WAIT_VM(16);
      }
      if (b == 255 && wave == 4) {
#pragma unroll
        for (int k = 0; k < 4; ++k) poll_flags(wsr, WS_FA + 7 * 1024 + (64 * k + lane) * 4, tag);
        if (lane == 0) st4(wsr, WS_EP, tag);
      }
    }
  }
  STAMP(0, 6);
  if (wave < 4) {
    if (!aux) BAR();
    phaseB_gemm(smem, p, wsr, tag, xcd, wave, lane, T2, J2);
  } else if (!aux) {
    const int sw = wave - 4;
    phaseB_s4d(smem, smem + B_CH + (sw >> 1) * B_CHS, p, wsr, tag, xcd, sw == 0, 2 * b + (sw >> 1), sw & 1, lane);
  }
  WAIT_VM(0);
  BAR();
  if (tid < 8) st4(wsr, WS_FB + tid * 1024 + b * 4, tag);
  STAMP(1, 4);
  if (!aux) {
    if (tid == 64) *(LAS unsigned*)(smem + C_GO) = 0u;
    BAR();
    phaseC(smem, p, wsr, tag, xcd, 4 * xcd + rr / 7, rr % 7, tid);
    STAMP(2, 2);
  }
}

extern "C" void kernel_launch(void* const* d_in, const int* in_sizes, int n_in, void* d_out, int out_size, void* d_ws, size_t ws_size, hipStream_t stream) {
  Params p;
  p.x = (const unsigned short*)d_in[0];
  p.logA = (const unsigned short*)d_in[1];
  p.Aim = (const unsigned short*)d_in[2];
  p.Cre = (const unsigned short*)d_in[3];
  p.Cim = (const unsigned short*)d_in[4];
  p.logdt = (const unsigned short*)d_in[5];
  p.Dsk = (const unsigned short*)d_in[6];
  p.w_in = (const unsigned short*)d_in[8];
  p.w_sp = (const unsigned short*)d_in[9];
  p.b_sp = (const unsigned short*)d_in[10];
  p.w_out = (const unsigned short*)d_in[11];
  p.ws = (char*)d_ws;
  p.out = (unsigned short*)d_out;
  (void)hipFuncSetAttribute((const void*)mixer_block, hipFuncAttributeMaxDynamicSharedMemorySize, 163840);
  mixer_block<<<256, 512, 163840, stream>>>(p);
}
```

```cpp
#include <hip/hip_runtime.h>
#include <hip/hip_bf16.h>
#include <stdint.h>

#define LAS __attribute__((address_space(3)))
#define GAS __attribute__((address_space(1)))
typedef float f32x2 __attribute__((ext_vector_type(2)));
typedef float f32x4 __attribute__((ext_vector_type(4)));
typedef float f32x16 __attribute__((ext_vector_type(16)));
typedef unsigned u32x2 __attribute__((ext_vector_type(2)));
typedef unsigned u32x4 __attribute__((ext_vector_type(4)));
typedef short s16x4 __attribute__((ext_vector_type(4)));
typedef short s16x8 __attribute__((ext_vector_type(8)));
typedef __bf16 bf16x2 __attribute__((ext_vector_type(2)));
typedef __bf16 bf16x8 __attribute__((ext_vector_type(8)));
typedef _Float16 f16x2 __attribute__((ext_vector_type(2)));
typedef _Float16 f16x8 __attribute__((ext_vector_type(8)));

constexpr int SEQ = 2048, DM = 448, FH = 896, NST = 256;

constexpr unsigned WS_Z1D = 0;
constexpr unsigned WS_Z2F = WS_Z1D + SEQ * FH * 2;
constexpr unsigned WS_HF  = WS_Z2F + SEQ * FH * 2;
constexpr unsigned WS_XT  = WS_HF + SEQ * FH * 2;
constexpr unsigned WS_ST  = WS_XT + SEQ * DM * 2;
constexpr unsigned WS_WOF = WS_ST + SEQ * DM * 2;
constexpr unsigned WS_XCH = WS_WOF + FH * DM * 2;
constexpr unsigned WS_XS1 = WS_XCH + 32 * 64 * 8 * 16;
constexpr unsigned WS_FA  = WS_XS1 + 32 * 1024;
constexpr unsigned WS_FZ  = WS_FA + 8192;
constexpr unsigned WS_FB  = WS_FZ + 1024;
constexpr unsigned WS_FS  = WS_FB + 8192;
constexpr unsigned WS_EP  = WS_FS + 8192;
constexpr unsigned WS_END = WS_EP + 256;

constexpr size_t WS_DBG = 32u << 20;
#define STAMP_DECL do {} while (0)
#define STAMP(kern, slot) do {} while (0)
#define STAMP_DUMP() do {} while (0)
#define MARKS_ARG
#define MARKS_PASS

constexpr unsigned ONES_BF = 0x3f803f80u;
constexpr int AUX_SC1 = 16;
__device__ __forceinline__ float bf2f(unsigned short h) { return __uint_as_float((unsigned)h << 16); }
__device__ __forceinline__ float bflo(unsigned w) { return __uint_as_float(w << 16); }
__device__ __forceinline__ float bfhi(unsigned w) { return __uint_as_float(w & 0xffff0000u); }
__device__ __forceinline__ float rbf(float x) { return (float)(__bf16)x; }
__device__ __forceinline__ unsigned pk_f16(float lo, float hi) {
  f16x2 v = {(_Float16)lo, (_Float16)hi};
  return __builtin_bit_cast(unsigned, v);
}
__device__ __forceinline__ f32x2 up_bf(unsigned w) { return (f32x2){bflo(w), bfhi(w)}; }
__device__ __forceinline__ unsigned rn_pair(f32x2 v) {
  unsigned r;
  asm("v_cvt_pk_bf16_f32 %0, %1, %2" : "=v"(r) : "v"(v.x), "v"(v.y));
  return r;
}
template <int CTRL> __device__ __forceinline__ float dpp_add(float v) {
  int t = __builtin_amdgcn_update_dpp(0, __float_as_int(v), CTRL, 0xf, 0xf, true);
  return v + __int_as_float(t);
}
__device__ __forceinline__ float red8(float v) {
  v = dpp_add<0xB1>(v); v = dpp_add<0x4E>(v); v = dpp_add<0x141>(v);
  return v;
}
__device__ __forceinline__ float red16(float v) { return dpp_add<0x140>(red8(v)); }
__device__ __forceinline__ float xsum32(float v) {
  auto b = __builtin_amdgcn_permlane32_swap(__float_as_uint(v), __float_as_uint(v), false, false);
  return __uint_as_float(b[0]) + __uint_as_float(b[1]);
}
__device__ __forceinline__ float xsum16_32(float v) {
  auto a = __builtin_amdgcn_permlane16_swap(__float_as_uint(v), __float_as_uint(v), false, false);
  return xsum32(__uint_as_float(a[0]) + __uint_as_float(a[1]));
}
__device__ __forceinline__ void glds16(const void* sbase, unsigned voff, unsigned lds) {
  asm volatile("s_mov_b32 m0, %2\n\ts_nop 0\n\tglobal_load_lds_dwordx4 %0, %1" ::"v"(voff), "s"(sbase), "s"(lds) : "memory");
}
#define WAIT_VM(n) asm volatile("s_waitcnt vmcnt(%0)" ::"n"(n) : "memory")
#define BAR() do { asm volatile("s_waitcnt lgkmcnt(0)" ::: "memory"); __builtin_amdgcn_s_barrier(); asm volatile("" ::: "memory"); } while (0)
__device__ __forceinline__ s16x4 tr_read(LAS const char* p) {
  typedef short v4i16 __attribute__((ext_vector_type(4)));
  return __builtin_amdgcn_ds_read_tr16_b64_v4i16((LAS v4i16*)p);
}
__device__ __forceinline__ f32x4 mfma_bf(s16x8 a, s16x8 b, f32x4 c) {
  return __builtin_amdgcn_mfma_f32_16x16x32_bf16(__builtin_bit_cast(bf16x8, a), __builtin_bit_cast(bf16x8, b), c, 0, 0, 0);
}
__device__ __forceinline__ void mfma_acc(f32x4& c, u32x4 a, s16x8 b) {
  asm volatile("v_mfma_f32_16x16x32_bf16 %0, %1, %2, %0" : "+v"(c) : "v"(a), "v"(b));
}
__device__ __forceinline__ void mfma32_acc(f32x16& c, s16x8 a, s16x8 b) {
  asm volatile("v_mfma_f32_32x32x16_bf16 %0, %1, %2, %0" : "+v"(c) : "v"(a), "v"(b));
}
#define MFMA_DONE() asm volatile("s_nop 7\n\ts_nop 7" ::: "memory")
__device__ __forceinline__ f32x16 mfma32_bf(s16x8 a, s16x8 b, f32x16 c) {
  return __builtin_amdgcn_mfma_f32_32x32x16_bf16(__builtin_bit_cast(bf16x8, a), __builtin_bit_cast(bf16x8, b), c, 0, 0, 0);
}
__device__ __forceinline__ f32x4 mfma_h(u32x4 a, u32x4 b, f32x4 c) {
  return __builtin_amdgcn_mfma_f32_16x16x32_f16(__builtin_bit_cast(f16x8, a), __builtin_bit_cast(f16x8, b), c, 0, 0, 0);
}
__device__ __forceinline__ float dot2bf(unsigned a, unsigned b, float c) {
  return __builtin_amdgcn_fdot2_f32_bf16(__builtin_bit_cast(bf16x2, a), __builtin_bit_cast(bf16x2, b), c, false);
}
__device__ __forceinline__ void dot2_ones(float& c, unsigned w) { asm volatile("v_dot2c_f32_bf16 %0, 0x3f803f80, %1" : "+v"(c) : "v"(w)); }
__device__ __forceinline__ __amdgpu_buffer_rsrc_t mk_rsrc(const void* p, unsigned bytes) {
  return __builtin_amdgcn_make_buffer_rsrc((void*)p, (short)0, (int)bytes, 0x00020000);
}
typedef __amdgpu_buffer_rsrc_t rsrc_t;
__device__ __forceinline__ void st16(rsrc_t r, unsigned off, u32x4 v) { __builtin_amdgcn_raw_buffer_store_b128(v, r, off, 0, AUX_SC1); }
__device__ __forceinline__ void st8(rsrc_t r, unsigned off, u32x2 v) { __builtin_amdgcn_raw_buffer_store_b64(v, r, off, 0, AUX_SC1); }
__device__ __forceinline__ void st4(rsrc_t r, unsigned off, unsigned v) { __builtin_amdgcn_raw_buffer_store_b32(v, r, off, 0, AUX_SC1); }
__device__ __forceinline__ u32x4 ld16(rsrc_t r, unsigned off) { return __builtin_amdgcn_raw_buffer_load_b128(r, off, 0, AUX_SC1); }
__device__ __forceinline__ void bglds16(rsrc_t rs, unsigned voff, unsigned soff, unsigned lds) {
  asm volatile("s_mov_b32 m0, %3\n\ts_nop 0\n\tbuffer_load_dwordx4 %0, %1, %2 offen lds" ::"v"(voff), "s"(rs), "s"(soff), "s"(lds) : "memory");
}
__device__ __forceinline__ void bload16(u32x4& dst, rsrc_t rs, unsigned voff, unsigned soff) {
  asm volatile("buffer_load_dwordx4 %0, %1, %2, %3 offen sc1" : "=v"(dst) : "v"(voff), "s"(rs), "s"(soff) : "memory");
}
__device__ __forceinline__ void poll_granule(u32x4& gr, rsrc_t rs, unsigned voff, unsigned tag) {
  for (unsigned spins = 0;; ++spins) {
    const bool ok = (gr[1] == tag) && (gr[3] == tag);
    if (__all(ok) || spins > (1u << 20)) break;
    __builtin_amdgcn_s_sleep(1);
    asm volatile("buffer_load_dwordx4 %0, %1, %2, 0 offen sc1\n\ts_waitcnt vmcnt(0)" : "=&v"(gr) : "v"(voff), "s"(rs) : "memory");
  }
}
__device__ __forceinline__ void poll_flags(rsrc_t rs, unsigned voff, unsigned tag) {
  for (unsigned spins = 0;; ++spins) {
    unsigned f;
    asm volatile("buffer_load_dword %0, %1, %2, 0 offen sc1\n\ts_waitcnt vmcnt(0)" : "=&v"(f) : "v"(voff), "s"(rs) : "memory");
    if (__all(f == tag) || spins > (1u << 20)) break;
    __builtin_amdgcn_s_sleep(4);
  }
}
__device__ __forceinline__ void group_wait(bool poller, rsrc_t rs, unsigned voff, unsigned tag, LAS char* word) {
  if (poller) {
    poll_flags(rs, voff, tag);
    *(volatile LAS unsigned*)word = 1u;
  } else {
    while ((unsigned)__builtin_amdgcn_readfirstlane(*(volatile LAS unsigned*)word) == 0u) __builtin_amdgcn_s_sleep(2);
  }
  asm volatile("" ::: "memory");
}
__device__ __forceinline__ void lds_sync(LAS char* cntp, unsigned target, int lane) {
  LAS unsigned* cnt = (LAS unsigned*)cntp;
  asm volatile("s_waitcnt lgkmcnt(0)" ::: "memory");
  if (lane == 0) __hip_atomic_fetch_add(cnt, 1u, __ATOMIC_RELAXED, __HIP_MEMORY_SCOPE_WORKGROUP);
  while ((unsigned)__builtin_amdgcn_readfirstlane(*(volatile LAS unsigned*)cnt) < target) __builtin_amdgcn_s_sleep(1);
  asm volatile("" ::: "memory");
}
template <int V> struct IC { static constexpr int value = V; };
template <int A, int B, class F> __device__ __forceinline__ void static_for(F&& f) {
  if constexpr (A < B) { f(IC<A>{}); static_for<A + 1, B>(f); }
}
__device__ __forceinline__ f32x2 gelu2(f32x2 x) {
  const float c1 = -2.0f * 0.7978845608028654f * 1.4426950408889634f, c2 = c1 * 0.044715f;
  const f32x2 t = x * x;
  const f32x2 a = x * (t * c2 + c1);
  f32x2 d = {__builtin_amdgcn_exp2f(a.x), __builtin_amdgcn_exp2f(a.y)};
  d = d + 1.0f;
  return x * (f32x2){__builtin_amdgcn_rcpf(d.x), __builtin_amdgcn_rcpf(d.y)};
}
__device__ __forceinline__ void ln_scalars(float s, float q, float invn, float& mb, float& rr) {
  const float mean = s * invn;
  float var = q * invn - mean * mean; var = var < 0.f ? 0.f : var;
  mb = rbf(mean);
  rr = __builtin_amdgcn_rcpf(rbf(__builtin_amdgcn_sqrtf(rbf(rbf(var) + 1e-5f))));
}

struct Params {
  const unsigned short *x, *logA, *Aim, *Cre, *Cim, *logdt, *Dsk, *w_in, *w_sp, *b_sp, *w_out;
  char* ws;
  unsigned short* out;
};
#define A_ABL 0
#define TLBWARM 1

constexpr int A_X = 0;
constexpr int A_S = 57344;
constexpr int A_P = A_S;
constexpr int A_W1 = A_S + 2048;
constexpr int A_W = 65536;
constexpr int A_TB = A_W + 65536;
constexpr int A_M2 = A_W + 32768;
constexpr int A_R2 = A_M2 + 256;
constexpr int A_W1B = A_R2 + 256;
constexpr int A_CNT = A_S + 4096;
constexpr int A_SLOT = 16384;
constexpr int B_RING = 0;

struct W2Src { rsrc_t wr; unsigned voff0, voff1; };
__device__ __forceinline__ W2Src w2_src(const Params& p, int T, int wave, int lane) {
  W2Src s;
  s.wr = mk_rsrc((const char*)p.w_sp + (size_t)T * 64 * 4096 + wave * 1024, 64 * 4096 - wave * 1024);
  s.voff0 = (lane >> 3) * 4096 + (((lane & 7) ^ ((lane >> 4) & 3)) * 16);
  s.voff1 = (8 + (lane >> 3)) * 4096 + (((lane & 7) ^ (4 + ((lane >> 4) & 3))) * 16);
  return s;
}
__device__ __forceinline__ void w2_piece(const W2Src& s, int wave, int pair, int piece) {
  bglds16(s.wr, (piece & 1) ? s.voff1 : s.voff0, (unsigned)((piece >> 1) * 65536 + pair * 128), (unsigned)(B_RING + wave * 16384 + (pair & 1) * 8192 + piece * 1024));
}

constexpr int AUX_T = 0;
constexpr int AUX_CNT = B_RING + 65536 + 44160 + 80;
constexpr int AUX_PF = 131072;
__device__ __forceinline__ void phaseA_aux(LAS char* smem, const Params& p, rsrc_t wsr, unsigned tag, int v, int tid) {
  char* ws = p.ws;
  const int lane = tid & 63;
  const int wave = __builtin_amdgcn_readfirstlane(tid >> 6);
  const int row = tid >> 2, c4 = tid & 3;
  u32x4 xv[14];
#pragma unroll
  for (int m = 0; m < 14; ++m) xv[m] = *(const u32x4*)(p.x + (size_t)(64 * v + row) * DM + (c4 + 4 * m) * 8);
  unsigned touch[2];
#pragma unroll
  for (int k = 0; k < 2; ++k) {
    const int i = tid + 256 * k;
    const char* a;
    if (i < 256) a = ws + (size_t)(i < (int)(WS_END >> 16) ? i : 0) * 65536;
    else if (i < 384) a = (const char*)p.w_sp + (size_t)(i - 256) * 65536;
    else if (i < 416) a = (const char*)p.w_in + (size_t)(i - 384 < 24 ? i - 384 : 0) * 65536;
    else if (i < 432) a = (const char*)p.w_out + (size_t)(i - 416 < 12 ? i - 416 : 0) * 65536;
    else if (i < 448) a = ((i >> 2) & 3) == 0 ? (const char*)p.logA + (i & 3) * 57344 : ((i >> 2) & 3) == 1 ? (const char*)p.Aim + (i & 3) * 57344
                        : ((i >> 2) & 3) == 2 ? (const char*)p.Cre + (i & 3) * 57344 : (const char*)p.Cim + (i & 3) * 57344;
    else if (i < 480) a = (const char*)p.out + (size_t)(i - 448 < 28 ? i - 448 : 0) * 65536;
    else a = (const char*)p.x + (size_t)(i - 480 < 28 ? i - 480 : 0) * 65536;
    asm volatile("global_load_dword %0, %1, off" : "=v"(touch[k]) : "v"(a) : "memory");
  }
  float s = 0.f, q = 0.f;
#pragma unroll
  for (int m = 0; m < 14; ++m)
#pragma unroll
    for (int e = 0; e < 4; ++e) { const unsigned w = xv[m][e]; s = dot2bf(w, ONES_BF, s); q = dot2bf(w, w, q); }
  s = dpp_add<0x4E>(dpp_add<0xB1>(s)); q = dpp_add<0x4E>(dpp_add<0xB1>(q));
  float mb, rr;
  ln_scalars(s, q, 1.0f / 448.0f, mb, rr);
  asm volatile("s_waitcnt vmcnt(0)" : "+v"(touch[0]), "+v"(touch[1])::"memory");
  if (c4 == 0) st16(wsr, WS_XS1 + v * 1024 + row * 16, (u32x4){__float_as_uint(mb), tag, __float_as_uint(rr), tag});
  unsigned short wv[7][8];
#pragma unroll
  for (int k = 0; k < 7; ++k) {
    int fr = v * 4 + wave + 128 * k; fr = fr < 784 ? fr : 783;
    const int dt = fr / 28, fb = fr - dt * 28;
    const int dcol = 16 * dt + (lane & 15), qq = lane >> 4;
#pragma unroll
    for (int jj = 0; jj < 8; ++jj) wv[k][jj] = p.w_out[(size_t)(32 * fb + 16 * (jj >> 2) + 4 * qq + (jj & 3)) * DM + dcol];
  }
  LAS char* tb = smem + AUX_T;
#pragma unroll
  for (int m = 0; m < 14; ++m)
#pragma unroll
    for (int e = 0; e < 4; ++e) {
      const unsigned w = xv[m][e];
      const f32x2 n3 = up_bf(rn_pair(up_bf(rn_pair(up_bf(w) - mb)) * rr));
      const int d = (c4 + 4 * m) * 8 + 2 * e;
      *(LAS _Float16*)(tb + d * 136 + row * 2) = (_Float16)n3.x;
      *(LAS _Float16*)(tb + (d + 1) * 136 + row * 2) = (_Float16)n3.y;
    }
  lds_sync(smem + AUX_CNT, 4, lane);
#pragma unroll
  for (int m = 0; m < 14; ++m) {
    const int d = (tid >> 3) + 32 * m, c8 = tid & 7;
    const u32x2 lo = *(LAS const u32x2*)(tb + d * 136 + c8 * 16), hi = *(LAS const u32x2*)(tb + d * 136 + c8 * 16 + 8);
    st16(wsr, WS_XT + (d * SEQ + 64 * v + 8 * c8) * 2, (u32x4){lo[0], lo[1], hi[0], hi[1]});
  }
#pragma unroll
  for (int k = 0; k < 7; ++k) {
    const int fr = v * 4 + wave + 128 * k;
    if (fr < 784)
      st16(wsr, WS_WOF + (fr * 64 + lane) * 16,
           (u32x4){(unsigned)wv[k][0] | ((unsigned)wv[k][1] << 16), (unsigned)wv[k][2] | ((unsigned)wv[k][3] << 16), (unsigned)wv[k][4] | ((unsigned)wv[k][5] << 16),
                   (unsigned)wv[k][6] | ((unsigned)wv[k][7] << 16)});
  }
  WAIT_VM(0);
  lds_sync(smem + AUX_CNT, 8, lane);
  if (tid < 8) st4(wsr, WS_FA + tid * 1024 + (7 * 32 + v) * 4, tag);
}

__device__ __forceinline__ void phaseA_gemm(LAS char* smem, const Params& p, rsrc_t wsr, unsigned tag, int ti, int tj, int T2, int tid MARKS_ARG) {
  char* ws = p.ws;
  const int lane = tid & 63;
  const int wave = __builtin_amdgcn_readfirstlane(tid >> 6);
  const int cg = wave & 3, h = lane >> 5, l31 = lane & 31;
  LAS char* xfer = smem + A_W + cg * 4096 + lane * 16;
  if (wave < 4) {
    LAS const char* ab;
    LAS const char* xb[4];
    {
      const int g4 = lane >> 4, qq = (lane & 15) >> 2, p4 = lane & 3;
      ab = smem + A_W + (8 * (g4 >> 1) + qq) * 256 + (((4 * cg + 2 * (g4 & 1) + (p4 >> 1)) ^ (4 * qq)) * 16) + (p4 & 1) * 8;
      const int sw = (l31 >> 1) & 7;
#pragma unroll
      for (int i = 0; i < 4; ++i) xb[i] = smem + A_X + l31 * 128 + (((2 * i + h) ^ sw) * 16);
    }
    s16x8 xa[8][2], wb[8];
#define A_FRAGS(st)                                                                                                    \
  do {                                                                                                                 \
    const int G_ = (st) >> 2, i_ = (st) & 3, so_ = (G_ % 6) * A_SLOT + i_ * 4096;                                      \
    wb[(st) & 7] = __builtin_shufflevector(tr_read(ab + so_), tr_read(ab + so_ + 1024), 0, 1, 2, 3, 4, 5, 6, 7);       \
    xa[(st) & 7][0] = *(LAS const s16x8*)(xb[i_] + (G_ % 7) * 8192);                                                   \
    xa[(st) & 7][1] = *(LAS const s16x8*)(xb[i_] + (G_ % 7) * 8192 + 4096);                                            \
  } while (0)
    f32x16 a2[2], a1[2];
    float w1a = 0.f, w1b = 0.f;
    u32x4 g1[2];
    const unsigned g1o = WS_XS1 + ti * 1024 + l31 * 16;
    float m1[2], r1[2];
    BAR();
    STAMP(0, 1);
    A_FRAGS(0); A_FRAGS(1); A_FRAGS(2); A_FRAGS(3);
    auto step = [&](auto stc) __attribute__((always_inline)) {
      constexpr int st = decltype(stc)::value;
      constexpr int G = st >> 2, i = st & 3;
      u32x4(&g1r)[2] = g1; const unsigned g1oo = g1o;
      if (i == 0 && G >= 1) {
        if (G == 6 || G == 7 || G == 13) asm volatile("s_waitcnt lgkmcnt(0)" ::: "memory");
        __builtin_amdgcn_s_barrier();
        asm volatile("" ::: "memory");
        if (G == 7) STAMP(0, 2);
      }
      __builtin_amdgcn_sched_barrier(0);
#pragma unroll
      for (int t = 0; t < 2; ++t) {
        const f32x16 zero = {};
        if (st == 0) a2[t] = mfma32_bf(wb[0], xa[0][t], zero); else if (st < 28) mfma32_acc(a2[t], wb[st & 7], xa[st & 7][t]);
        else if (st == 28) a1[t] = mfma32_bf(wb[st & 7], xa[st & 7][t], zero); else mfma32_acc(a1[t], wb[st & 7], xa[st & 7][t]);
      }
      {
        const u32x4 wv = __builtin_bit_cast(u32x4, wb[st & 7]);
#pragma unroll
        for (int e = 0; e < 4; ++e) { const unsigned ww = wv[e]; if (st < 28) dot2_ones(w1a, ww); else dot2_ones(w1b, ww); }
      }
      if constexpr (st + 4 < 56) A_FRAGS(st + 4);
      if constexpr (st == 16) {
        asm volatile("buffer_load_dwordx4 %0, %1, %2, 0 offen sc1" : "=v"(g1r[0]) : "v"(g1oo), "s"(wsr) : "memory");
        asm volatile("buffer_load_dwordx4 %0, %1, %2, 0 offen offset:512 sc1" : "=v"(g1r[1]) : "v"(g1oo), "s"(wsr) : "memory");
      }
      if constexpr (st == 27) {
        MFMA_DONE();
        const float w1s = xsum32(w1a);
        if (h == 0) *(LAS float*)(smem + A_W1 + (32 * cg + l31) * 4) = w1s;
        asm volatile("s_waitcnt vmcnt(0)" : "+v"(g1r[0]), "+v"(g1r[1])::"memory");
        poll_granule(g1r[0], wsr, g1oo, tag);
        poll_granule(g1r[1], wsr, g1oo + 512, tag);
#pragma unroll
        for (int t = 0; t < 2; ++t) { m1[t] = __uint_as_float(g1r[t][0]); r1[t] = __uint_as_float(g1r[t][2]); }
        asm volatile("s_waitcnt lgkmcnt(0)" ::: "memory");
        unsigned pre[2][8];
#pragma unroll
        for (int g = 0; g < 4; ++g) {
          const f32x4 w1 = *(LAS const f32x4*)(smem + A_W1 + (32 * cg + 8 * g + 4 * h) * 4);
#pragma unroll
          for (int t = 0; t < 2; ++t)
#pragma unroll
            for (int pp = 0; pp < 2; ++pp)
              pre[t][2 * g + pp] = rn_pair(((f32x2){a2[t][4 * g + 2 * pp], a2[t][4 * g + 2 * pp + 1]} - (f32x2){w1[2 * pp], w1[2 * pp + 1]} * m1[t]) * r1[t]);
        }
#pragma unroll
        for (int t = 0; t < 2; ++t)
#pragma unroll
          for (int g = 0; g < 2; ++g) *(LAS u32x4*)(xfer + (2 * t + g) * 1024) = (u32x4){pre[t][4 * g], pre[t][4 * g + 1], pre[t][4 * g + 2], pre[t][4 * g + 3]};
      }
      __builtin_amdgcn_sched_barrier(0);
    };
    static_for<0, 56>(step);
#undef A_FRAGS
    MFMA_DONE();
    STAMP(0, 3);
    const float w1s = xsum32(w1b);
    if (h == 0) *(LAS float*)(smem + A_W1B + (32 * cg + l31) * 4) = w1s;
    asm volatile("s_waitcnt lgkmcnt(0)" ::: "memory");
#pragma unroll
    for (int g = 0; g < 4; ++g) {
      const f32x4 w1 = *(LAS const f32x4*)(smem + A_W1B + (32 * cg + 8 * g + 4 * h) * 4);
#pragma unroll
      for (int t = 0; t < 2; ++t) {
        unsigned o[2];
#pragma unroll
        for (int pp = 0; pp < 2; ++pp)
          o[pp] = rn_pair(gelu2(up_bf(rn_pair(((f32x2){a1[t][4 * g + 2 * pp], a1[t][4 * g + 2 * pp + 1]} - (f32x2){w1[2 * pp], w1[2 * pp + 1]} * m1[t]) * r1[t]))));
        const int tt = 4 * ti + 2 * t + (l31 >> 4), ft = 8 * tj + 2 * cg + (g >> 1), lp = (lane & 15) + 16 * (2 * (g & 1) + h);
        st8(wsr, WS_Z1D + ((tt * 56 + ft) * 64 + lp) * 8, (u32x2){o[0], o[1]});
      }
    }
    WAIT_VM(0);
    lds_sync(smem + A_CNT, 4, lane);
    if (tid == 0) st4(wsr, WS_FZ + (tj * 32 + ti) * 4, tag);
    STAMP(0, 4);
  } else {
    unsigned voffx[2], voffw[4];
#pragma unroll
    for (int e = 0; e < 2; ++e) {
      const int r = 16 * cg + 8 * e + (lane >> 3);
      voffx[e] = (unsigned)(r * 896 + (((lane & 7) ^ ((r >> 1) & 7)) * 16));
    }
#pragma unroll
    for (int e = 0; e < 4; ++e) voffw[e] = (unsigned)((16 * cg + 4 * e + (lane >> 4)) * 3584 + (((lane & 15) ^ (4 * (lane >> 4))) * 16) + tj * 256);
    const char* xsrc = (const char*)p.x + (size_t)ti * 64 * 896;
#define A_XCHUNK(g) _Pragma("unroll") for (int e = 0; e < 2; ++e) glds16(xsrc + (g) * 128, voffx[e], (unsigned)(A_X + (g) * 8192 + (2 * cg + e) * 1024))
#define A_WCHUNK(G) _Pragma("unroll") for (int e = 0; e < 4; ++e)                                                     \
    glds16(p.w_in, voffw[e] + (unsigned)(((G) % 7) * 64 * 3584 + ((G) < 7 ? 1792 : 0)), (unsigned)(A_W + ((G) % 6) * A_SLOT + (4 * cg + e) * 1024))
#pragma unroll
    for (int g = 0; g < 3; ++g) { A_XCHUNK(g); A_WCHUNK(g); }
    if (tid < 256 + 4) *(LAS unsigned*)(smem + A_CNT + (tid - 256) * 4) = 0u;
    WAIT_VM(6);
    BAR();
    unsigned zp[2][8];
    float ss[2] = {0.f, 0.f}, sq[2] = {0.f, 0.f};
    u32x4 gr[2];
    const int row = (tid - 256) >> 2, j2 = 2 * (tid & 3);
    const unsigned vo = WS_XCH + ti * 8192 + row * 128 + j2 * 16;
    auto step = [&](auto stc) __attribute__((always_inline)) {
      constexpr int st = decltype(stc)::value;
      constexpr int G = st >> 2, i = st & 3;
      u32x4(&grr)[2] = gr; const unsigned voo = vo;
      if (i == 0 && G >= 1) {
        if (G <= 4) WAIT_VM(6);
        else if (G <= 10) WAIT_VM(8);
        else if (G == 11) asm volatile("s_waitcnt vmcnt(4) lgkmcnt(0)" ::: "memory");
        else if (G == 12) WAIT_VM(0);
        __builtin_amdgcn_s_barrier();
        asm volatile("" ::: "memory");
      }
      __builtin_amdgcn_sched_barrier(0);
      if constexpr (st == 28) {
#pragma unroll
        for (int t = 0; t < 2; ++t)
#pragma unroll
          for (int g = 0; g < 2; ++g) { const u32x4 v = *(LAS const u32x4*)(xfer + (2 * t + g) * 1024); zp[t][4 * g] = v[0]; zp[t][4 * g + 1] = v[1]; zp[t][4 * g + 2] = v[2]; zp[t][4 * g + 3] = v[3]; }
      }
      if constexpr (i == 0) {
        if constexpr (G < 4) { A_XCHUNK(G + 3); A_WCHUNK(G + 3); }
        else if constexpr (G == 4) { A_WCHUNK(7); A_WCHUNK(8); }
        else if constexpr (G <= 9) {
          if constexpr (G == 8) asm volatile("s_waitcnt lgkmcnt(0)" ::: "memory");
          A_WCHUNK(G + 4);
        }
      }
      if constexpr (G >= 7 && G <= 10 && i == 1) {
        constexpr int g = G - 7;
#pragma unroll
        for (int t = 0; t < 2; ++t)
#pragma unroll
          for (int pp = 0; pp < 2; ++pp) {
            const unsigned zz = rn_pair(gelu2(up_bf(zp[t][2 * g + pp])));
            zp[t][2 * g + pp] = zz;
            ss[t] = dot2bf(zz, ONES_BF, ss[t]); sq[t] = dot2bf(zz, zz, sq[t]);
          }
      }
      if constexpr (st == 43) {
#pragma unroll
        for (int t = 0; t < 2; ++t) {
          const float s2 = xsum32(ss[t]), q2 = xsum32(sq[t]);
          if (h == 0) *(LAS f32x2*)(smem + A_P + ((32 * t + l31) * 4 + cg) * 8) = (f32x2){s2, q2};
        }
      }
      if constexpr (st == 44) {
        if (wave == 4) {
          const f32x4 p0 = *(LAS const f32x4*)(smem + A_P + lane * 32), p1 = *(LAS const f32x4*)(smem + A_P + lane * 32 + 16);
          const float s = (p0[0] + p0[2]) + (p1[0] + p1[2]), q = (p0[1] + p0[3]) + (p1[1] + p1[3]);
          u32x4 gq = {__float_as_uint(s), tag, __float_as_uint(q), tag};
          const unsigned vq = WS_XCH + ti * 8192 + lane * 128 + tj * 16;
          asm volatile("buffer_store_dwordx4 %0, %1, %2, 0 offen sc1\n\ts_nop 1" ::"v"(gq), "v"(vq), "s"(wsr) : "memory");
        }
      }
      if constexpr (st == 52) {
        asm volatile("buffer_load_dwordx4 %0, %1, %2, 0 offen sc1" : "=v"(grr[0]) : "v"(voo), "s"(wsr) : "memory");
        const unsigned vo1 = voo + ((tid & 3) == 3 ? 0 : 16);
        asm volatile("buffer_load_dwordx4 %0, %1, %2, 0 offen sc1" : "=v"(grr[1]) : "v"(vo1), "s"(wsr) : "memory");
      }
      __builtin_amdgcn_sched_barrier(0);
    };
    static_for<0, 56>(step);
#undef A_XCHUNK
#undef A_WCHUNK
    asm volatile("s_waitcnt vmcnt(0)" : "+v"(gr[0]), "+v"(gr[1])::"memory");
    poll_granule(gr[0], wsr, vo, tag);
    poll_granule(gr[1], wsr, vo + ((tid & 3) == 3 ? 0 : 16), tag);
    {
      float s = __uint_as_float(gr[0][0]) + ((tid & 3) == 3 ? 0.f : __uint_as_float(gr[1][0])), q = __uint_as_float(gr[0][2]) + ((tid & 3) == 3 ? 0.f : __uint_as_float(gr[1][2]));
      s = dpp_add<0x4E>(dpp_add<0xB1>(s)); q = dpp_add<0x4E>(dpp_add<0xB1>(q));
      float mb, rr2;
      ln_scalars(s, q, 1.0f / 896.0f, mb, rr2);
      if ((tid & 3) == 0) { *(LAS float*)(smem + A_M2 + row * 4) = mb; *(LAS float*)(smem + A_R2 + row * 4) = rr2; }
    }
    lds_sync(smem + A_CNT + 4, 4, lane);
    const int q4 = lane >> 4, l15 = lane & 15;
#pragma unroll
    for (int t = 0; t < 2; ++t) {
      LAS char* tb = smem + A_TB + (2 * cg + t) * 2304;
      const int srow = 32 * t + l31;
      const float mb2 = *(LAS const float*)(smem + A_M2 + srow * 4), rr2 = *(LAS const float*)(smem + A_R2 + srow * 4);
#pragma unroll
      for (int g = 0; g < 4; ++g) {
        unsigned o[2];
#pragma unroll
        for (int pp = 0; pp < 2; ++pp) o[pp] = rn_pair(up_bf(rn_pair(up_bf(zp[t][2 * g + pp]) - mb2)) * rr2);
        *(LAS u32x2*)(tb + l31 * 72 + (8 * g + 4 * h) * 2) = (u32x2){o[0], o[1]};
      }
    }
    asm volatile("s_waitcnt lgkmcnt(0)" ::: "memory");
#pragma unroll
    for (int t = 0; t < 2; ++t)
#pragma unroll
      for (int n = 0; n < 2; ++n) {
        LAS const char* rp = smem + A_TB + (2 * cg + t) * 2304 + (8 * q4 + (l15 >> 2)) * 72 + n * 32 + (lane & 3) * 8;
        const s16x4 lo = tr_read(rp), hi = tr_read(rp + 4 * 72);
        const s16x8 o = __builtin_shufflevector(lo, hi, 0, 1, 2, 3, 4, 5, 6, 7);
        const int ft = 8 * tj + 2 * cg + n, sb = 2 * ti + t;
        st16(wsr, WS_Z2F + ((ft * 64 + sb) * 64 + lane) * 16, __builtin_bit_cast(u32x4, o));
      }
    WAIT_VM(0);
    lds_sync(smem + A_CNT + 8, 4, lane);
    if (tid - 256 < 8) st4(wsr, WS_FA + (tid - 256) * 1024 + (tj * 32 + ti) * 4, tag);
    STAMP(0, 5);
  }
  BAR();
}

constexpr int B_CH = 65536;
constexpr int B_CHS = 49152;
constexpr int CH_FT = 0;
constexpr int CH_FW = 16416, CH_FS = 288;
constexpr int CH_CS = 4160;
constexpr int CH_U = 33280;
constexpr int CH_T = CH_U + 4608;
constexpr int CH_TS = 49 * 64;
constexpr int CH_CNT = CH_T + 2 * CH_TS;

template <int W>
__device__ __forceinline__ void b_epi(LAS char* smem, rsrc_t wsr, f32x4 (&acc)[7][4], const u32x2 (&z1v)[7], float bs, int lane, int tt, int J) {
  LAS char* cnt = smem + B_CH + CH_CNT + 64;
  LAS char* mine = smem + B_RING + W * 16384 + lane * 16;
  LAS char* other = smem + B_RING + (W ^ 2) * 16384 + lane * 16;
  LAS char* o2 = smem + B_RING + (W ^ 3) * 16384 + lane * 16;
  constexpr int K0 = W & 2, S0 = K0 ^ 2;
#pragma unroll
  for (int n = 0; n < 7; ++n) { *(LAS f32x4*)(mine + n * 1024) = acc[n][S0]; *(LAS f32x4*)(mine + (7 + n) * 1024) = acc[n][S0 + 1]; }
  lds_sync(cnt, 4, lane);
#pragma unroll
  for (int n = 0; n < 7; ++n) { acc[n][K0] += *(LAS const f32x4*)(other + n * 1024); acc[n][K0 + 1] += *(LAS const f32x4*)(other + (7 + n) * 1024); }
  asm volatile("s_waitcnt lgkmcnt(0)" ::: "memory");
#pragma unroll
  for (int n = 0; n < 7; ++n) *(LAS f32x4*)(other + n * 1024) = acc[n][W ^ 1];
  lds_sync(cnt, 8, lane);
  unsigned hp[7][2];
#pragma unroll
  for (int n = 0; n < 7; ++n) {
    const f32x4 v = acc[n][W] + *(LAS const f32x4*)(o2 + n * 1024);
#pragma unroll
    for (int pp = 0; pp < 2; ++pp) {
      const f32x2 g0 = up_bf(rn_pair((f32x2){v[2 * pp], v[2 * pp + 1]})) + bs;
      const unsigned zw = z1v[n][pp];
      hp[n][pp] = rn_pair(up_bf(rn_pair(g0)) * up_bf(zw));
    }
  }
  const unsigned hb = WS_HF + ((tt * 28 + (7 * J >> 1)) * 64 + lane) * 16;
  if (!(J & 1)) {
#pragma unroll
    for (int pp = 0; pp < 3; ++pp) st16(wsr, hb + pp * 1024, (u32x4){hp[2 * pp][0], hp[2 * pp][1], hp[2 * pp + 1][0], hp[2 * pp + 1][1]});
    st8(wsr, hb + 3 * 1024, (u32x2){hp[6][0], hp[6][1]});
  } else {
    st8(wsr, hb + 8, (u32x2){hp[0][0], hp[0][1]});
#pragma unroll
    for (int pp = 0; pp < 3; ++pp) st16(wsr, hb + (pp + 1) * 1024, (u32x4){hp[2 * pp + 1][0], hp[2 * pp + 1][1], hp[2 * pp + 2][0], hp[2 * pp + 2][1]});
  }
}

__device__ __forceinline__ void phaseB_gemm(LAS char* smem, const Params& p, rsrc_t wsr, unsigned tag, int rep, int wave, int lane, int T, int J MARKS_ARG) {
  char* ws = p.ws;
  const rsrc_t zs = mk_rsrc(ws + WS_Z2F + (size_t)(7 * J * 64 + 16 * wave) * 1024, 7 * 65536 - 16 * wave * 1024);
  const W2Src w2 = w2_src(p, T, wave, lane);
  const unsigned voffz = lane * 16;
  const unsigned ring = B_RING + wave * 16384;
  LAS const char* wrd0 = smem + ring + (lane & 15) * 128 + (((lane >> 4) ^ ((lane >> 1) & 7)) * 16);
  LAS const char* wrd1 = smem + ring + (lane & 15) * 128 + (((4 + (lane >> 4)) ^ ((lane >> 1) & 7)) * 16);
  u32x4 z[3][7];
  f32x4 acc[7][4];
#pragma unroll
  for (int k = 0; k < 16; ++k) w2_piece(w2, wave, k >> 3, k & 7);
  {
    group_wait(false, wsr, 0u, tag, smem + B_CH + CH_CNT + 72);
  }
  STAMP(1, 0);
#define B_ISSUE_Z(it) _Pragma("unroll") for (int n = 0; n < 7; ++n) bload16(z[(it) % 3][n], zs, voffz, n * 65536 + (it) * 1024)
  B_ISSUE_Z(0); B_ISSUE_Z(1); B_ISSUE_Z(2);
#undef B_ISSUE_Z
  const int tt = 4 * T + wave;
  u32x2 z1v[7];
  unsigned bsw, fz;
#define B_WAITZ(N) asm volatile("s_waitcnt vmcnt(" #N ")" : "+v"(zc[0]), "+v"(zc[1]), "+v"(zc[2]), "+v"(zc[3]), "+v"(zc[4]), "+v"(zc[5]), "+v"(zc[6])::"memory")
#pragma unroll
  for (int it = 0; it < 16; ++it) {
    u32x4(&zc)[7] = z[it % 3];
    if (it <= 1) B_WAITZ(14); else if (it <= 13) B_WAITZ(22); else if (it == 14) B_WAITZ(15); else B_WAITZ(8);
    s16x8 wf[4];
#pragma unroll
    for (int m = 0; m < 4; ++m) wf[m] = *(LAS const s16x8*)(((it & 1) ? wrd1 : wrd0) + ((it >> 1) & 1) * 8192 + m * 2048);
#pragma unroll
    for (int n = 0; n < 7; ++n) {
#pragma unroll
      for (int m = 0; m < 4; ++m) {
        if (it == 0) acc[n][m] = mfma_bf(__builtin_bit_cast(s16x8, zc[n]), wf[m], (f32x4){0.f, 0.f, 0.f, 0.f});
        else mfma_acc(acc[n][m], zc[n], wf[m]);
      }
      __builtin_amdgcn_sched_barrier(0);
      if (it + 3 < 16) {
        bload16(zc[n], zs, voffz, n * 65536 + (it + 3) * 1024);
        if ((it & 1) && n >= 1 && n <= 4) { w2_piece(w2, wave, (it + 3) >> 1, 2 * (n - 1)); w2_piece(w2, wave, (it + 3) >> 1, 2 * (n - 1) + 1); }
      }
      if (it == 9 && n == 6) {
        int ln = lane; asm volatile("" : "+v"(ln));
        const unsigned fzo = WS_FZ + (((ln & 1) ? (112 * J + 111) >> 7 : (112 * J) >> 7) * 32 + T) * 4;
        asm volatile("buffer_load_dword %0, %1, %2, 0 offen sc1" : "=v"(fz) : "v"(fzo), "s"(wsr) : "memory");
      }
      if (it == 13) {
        if (n == 0) {
          asm volatile("" : "+v"(fz));
          if (!__all(fz == tag)) {
            int ln = lane; asm volatile("" : "+v"(ln));
            poll_flags(wsr, WS_FZ + (((ln & 1) ? (112 * J + 111) >> 7 : (112 * J) >> 7) * 32 + T) * 4, tag);
          }
        }
        int l8 = lane; asm volatile("" : "+v"(l8));
        const unsigned vo = WS_Z1D + (tt * 56 + 7 * J + n) * 512 + l8 * 8;
        asm volatile("buffer_load_dwordx2 %0, %1, %2, 0 offen sc1" : "=v"(z1v[n]) : "v"(vo), "s"(wsr) : "memory");
        if (n == 6) {
          const unsigned vb = (unsigned)(16 * tt + (l8 & 15)) * 2u;
          asm volatile("global_load_ushort %0, %1, %2" : "=v"(bsw) : "v"(vb), "s"(p.b_sp) : "memory");
        }
      }
      __builtin_amdgcn_sched_barrier(0);
    }
  }
#undef B_WAITZ
  MFMA_DONE();
  STAMP(1, 1);
  asm volatile("s_waitcnt vmcnt(0)" : "+v"(z1v[0]), "+v"(z1v[1]), "+v"(z1v[2]), "+v"(z1v[3]), "+v"(z1v[4]), "+v"(z1v[5]), "+v"(z1v[6]), "+v"(bsw)::"memory");
  const float bs = bf2f((unsigned short)bsw);
  if (wave == 0) b_epi<0>(smem, wsr, acc, z1v, bs, lane, tt, J);
  else if (wave == 1) b_epi<1>(smem, wsr, acc, z1v, bs, lane, tt, J);
  else if (wave == 2) b_epi<2>(smem, wsr, acc, z1v, bs, lane, tt, J);
  else b_epi<3>(smem, wsr, acc, z1v, bs, lane, tt, J);
  STAMP(1, 2);
}

__device__ __forceinline__ f32x2 cmul(f32x2 a, f32x2 b) {
  f32x2 t, r;
  asm("v_pk_mul_f32 %0, %1, %2 op_sel:[0,0] op_sel_hi:[0,1]" : "=v"(t) : "v"(a), "v"(b));
  asm("v_pk_fma_f32 %0, %1, %2, %3 op_sel:[1,1,0] op_sel_hi:[1,0,1] neg_lo:[1,0,0]" : "=v"(r) : "v"(a), "v"(b), "v"(t));
  return r;
}
__device__ __forceinline__ unsigned hmul(unsigned a, unsigned b) {
  unsigned t, r;
  asm("v_pk_mul_f16 %0, %1, %2 op_sel:[0,0] op_sel_hi:[0,1]" : "=v"(t) : "v"(a), "v"(b));
  asm("v_pk_fma_f16 %0, %1, %2, %3 op_sel:[1,1,0] op_sel_hi:[1,0,1] neg_lo:[1,0,0]" : "=v"(r) : "v"(a), "v"(b), "v"(t));
  return r;
}
__device__ __forceinline__ unsigned hcv(f32x2 v) { return pk_f16(v.x, v.y); }
__device__ __forceinline__ f32x2 cexp_p(float al, float be, float pp) {
  const float e = __builtin_amdgcn_exp2f(al * pp), th = __builtin_amdgcn_fractf(be * pp);
  return (f32x2){__builtin_amdgcn_cosf(th), __builtin_amdgcn_sinf(th)} * e;
}
struct PosTab { unsigned short v[2064]; };
constexpr int pos_bf16(int j) {
  if (j < 256) return j;
  int sh = 1; while ((j >> sh) >= 256) ++sh;
  const int q = j >> sh, r = j & ((1 << sh) - 1), h = 1 << (sh - 1);
  return (r > h || (r == h && (q & 1)) ? q + 1 : q) << sh;
}
constexpr PosTab make_postab() {
  PosTab t = {};
  for (int x = 0; x < 2048; ++x) {
    const int p = pos_bf16(2047 - x);
    int col = 0, a = 0;
    if (p < 256) { a = p >> 4; col = p & 15; }
    else if (p < 512) { const int r = p - 256; a = r >> 4; col = 16 + ((r & 15) >> 1); }
    else if (p < 1024) { const int r = p - 512; a = (r & 255) >> 4; col = 24 + 4 * (r >> 8) + ((r & 15) >> 2); }
    else if (p < 2048) { const int r = p - 1024; a = (r & 255) >> 4; col = 32 + 2 * (r >> 8) + ((r & 15) >> 3); }
    else { a = 0; col = 40; }
    t.v[x] = (unsigned short)((col * 16 + a) * 4);
  }
  for (int x = 2048; x < 2064; ++x) t.v[x] = 48 * 64;
  return t;
}
__device__ const PosTab POSTAB = make_postab();

template <int HW>
__device__ __forceinline__ void s4d_conv(LAS char* cb, rsrc_t wsr, int d, int lane, float iscale, float dsk) {
  constexpr int NF = HW ? 7 : 8;
  f32x4 acc[8] = {};
  const int r = lane & 15, h = lane >> 4;
  LAS const char* ab = cb + CH_FT + ((7 - r) & 7) * CH_CS + (255 - (r >> 3) - 2 * (h >> 1) + (h & 1)) * 16 - (64 * 7 + 512 * 7);
  LAS const char* bb = cb + CH_U + 512 + 32 * r - 32 * (h >> 1) + 16 * (h & 1) - 64 * 7;
  u32x4 A[2][8], B[2][8];
#define CONV_LOAD(ar, buf)                                                                                   \
  _Pragma("unroll") for (int k = 0; k < NF; ++k) {                                                           \
    B[buf][k] = *(LAS const u32x4*)(bb + 512 * k + 64 * (7 - (ar)));                                         \
    A[buf][k] = *(LAS const u32x4*)(ab + 512 * (7 - k) + 64 * (7 - (ar)));                                   \
  }
  CONV_LOAD(0, 0);
#pragma unroll
  for (int ar = 0; ar < 8; ++ar) {
    __builtin_amdgcn_sched_barrier(0);
    if (ar < 7) { CONV_LOAD(ar + 1, (ar + 1) & 1); }
#pragma unroll
    for (int al = 0; al < NF; ++al)
#pragma unroll
      for (int be = 0; be < NF; ++be) {
        const int tau = al + be;
        if (HW ? (tau >= 4 && tau <= 6) : (tau <= 3 || tau == 7)) acc[tau] = mfma_h(A[ar & 1][al], B[ar & 1][be], acc[tau]);
      }
#pragma unroll
    for (int i = 0; i < 16; ++i) { __builtin_amdgcn_sched_group_barrier(0x008, 1, 0); __builtin_amdgcn_sched_group_barrier(0x100, 1, 0); }
    __builtin_amdgcn_sched_barrier(0);
  }
#undef CONV_LOAD
  const int q = lane >> 4;
#pragma unroll
  for (int t = 0; t < 8; ++t) {
    if (HW ? (t >= 4 && t <= 6) : (t <= 3 || t == 7)) {
      const f32x4 v = acc[t];
      const int l0 = 256 * t + 16 * r + 4 * q;
      const u32x2 uw = *(LAS const u32x2*)(cb + CH_U + 512 + l0 * 2);
      unsigned o[2];
#pragma unroll
      for (int pp = 0; pp < 2; ++pp) {
        const unsigned uwp = uw[pp];
        const f16x2 ua = __builtin_bit_cast(f16x2, uwp);
        const f32x2 y2 = up_bf(rn_pair((f32x2){v[2 * pp], v[2 * pp + 1]} * iscale)), k2 = up_bf(rn_pair((f32x2){(float)ua[0], (float)ua[1]} * dsk));
        o[pp] = rn_pair(y2 + k2);
      }
      st8(wsr, WS_ST + (d * SEQ + l0) * 2, (u32x2){o[0], o[1]});
    }
  }
}

__device__ __forceinline__ void poll_gemm2_inputs(LAS char* smem, rsrc_t wsr, unsigned tag, int rep, int J, int lane) {
  const int tj0 = (112 * J) >> 7, tj1 = (112 * J + 111) >> 7;
  group_wait(true, wsr, WS_FA + rep * 1024 + (lane < 32 ? tj0 : tj1) * 128 + (lane & 31) * 4, tag, smem + B_CH + CH_CNT + 72);
}
__device__ __forceinline__ void phaseB_s4d(LAS char* smem, LAS char* cb, const Params& p, rsrc_t wsr, unsigned tag, int rep, bool poller, int J, int d, int hw, int lane MARKS_ARG) {
  char* ws = p.ws;
  LAS char* cnt = cb + CH_CNT;
  unsigned la_[2], ai_[2], cr_[4], ci_[4], ldt_, dsk_;
  u32x4 pt0, pt1, pt2;
  {
    const unsigned vo = (unsigned)(d * NST + 128 * hw + lane) * 2u, vo2 = (unsigned)(d * NST + ((128 * hw + 128 + lane) & 255)) * 2u, vd = (unsigned)d * 2u;
    asm volatile("global_load_ushort %0, %1, %2" : "=v"(la_[0]) : "v"(vo), "s"(p.logA) : "memory");
    asm volatile("global_load_ushort %0, %1, %2 offset:128" : "=v"(la_[1]) : "v"(vo), "s"(p.logA) : "memory");
    asm volatile("global_load_ushort %0, %1, %2" : "=v"(ai_[0]) : "v"(vo), "s"(p.Aim) : "memory");
    asm volatile("global_load_ushort %0, %1, %2 offset:128" : "=v"(ai_[1]) : "v"(vo), "s"(p.Aim) : "memory");
    asm volatile("global_load_ushort %0, %1, %2" : "=v"(cr_[0]) : "v"(vo), "s"(p.Cre) : "memory");
    asm volatile("global_load_ushort %0, %1, %2 offset:128" : "=v"(cr_[1]) : "v"(vo), "s"(p.Cre) : "memory");
    asm volatile("global_load_ushort %0, %1, %2" : "=v"(cr_[2]) : "v"(vo2), "s"(p.Cre) : "memory");
    asm volatile("global_load_ushort %0, %1, %2 offset:128" : "=v"(cr_[3]) : "v"(vo2), "s"(p.Cre) : "memory");
    asm volatile("global_load_ushort %0, %1, %2" : "=v"(ci_[0]) : "v"(vo), "s"(p.Cim) : "memory");
    asm volatile("global_load_ushort %0, %1, %2 offset:128" : "=v"(ci_[1]) : "v"(vo), "s"(p.Cim) : "memory");
    asm volatile("global_load_ushort %0, %1, %2" : "=v"(ci_[2]) : "v"(vo2), "s"(p.Cim) : "memory");
    asm volatile("global_load_ushort %0, %1, %2 offset:128" : "=v"(ci_[3]) : "v"(vo2), "s"(p.Cim) : "memory");
    asm volatile("global_load_ushort %0, %1, %2" : "=v"(ldt_) : "v"(vd), "s"(p.logdt) : "memory");
    asm volatile("global_load_ushort %0, %1, %2" : "=v"(dsk_) : "v"(vd), "s"(p.Dsk) : "memory");
    const char* pb = (const char*)POSTAB.v + hw * 2048;
    const unsigned vp = lane * 32;
    asm volatile("global_load_dwordx4 %0, %1, %2" : "=v"(pt0) : "v"(vp), "s"(pb) : "memory");
    asm volatile("global_load_dwordx4 %0, %1, %2 offset:16" : "=v"(pt1) : "v"(vp), "s"(pb) : "memory");
    asm volatile("global_load_dwordx4 %0, %1, %2 offset:32" : "=v"(pt2) : "v"(vp), "s"(pb) : "memory");
    if (hw == 0 && lane < 32) *(LAS u32x4*)(cb + CH_U + lane * 16) = (u32x4){0u, 0u, 0u, 0u};
    if (lane < 4) *(LAS u32x4*)(cb + CH_T + hw * CH_TS + 48 * 64 + lane * 16) = (u32x4){0u, 0u, 0u, 0u};
  }
  BAR();
  __builtin_amdgcn_s_setprio(3);
  if (J >= 0) poll_gemm2_inputs(smem, wsr, tag, rep, J, lane);
  asm volatile("s_waitcnt vmcnt(0)" : "+v"(la_[0]), "+v"(la_[1]), "+v"(ai_[0]), "+v"(ai_[1]), "+v"(cr_[0]), "+v"(cr_[1]), "+v"(cr_[2]), "+v"(cr_[3]), "+v"(ci_[0]), "+v"(ci_[1]),
               "+v"(ci_[2]), "+v"(ci_[3]), "+v"(ldt_), "+v"(dsk_), "+v"(pt0), "+v"(pt1), "+v"(pt2)::"memory");
  STAMP(1, 0);
  float cr[4], ci[4];
#pragma unroll
  for (int k = 0; k < 4; ++k) { cr[k] = bf2f((unsigned short)cr_[k]); ci[k] = bf2f((unsigned short)ci_[k]); }
  const float dt = rbf(__builtin_amdgcn_exp2f(bf2f((unsigned short)ldt_) * 1.4426950408889634f)), dsk = bf2f((unsigned short)dsk_);
  float sabs = 0.f;
#pragma unroll
  for (int k = 0; k < 4; ++k) sabs += __builtin_fabsf(cr[k]) + __builtin_fabsf(ci[k]);
  sabs = xsum16_32(red16(sabs));
  sabs *= 2.0f * dt;
  int ex = (int)((__float_as_uint(sabs) >> 23) & 0xff) - 126;
  ex = ex < -100 ? -100 : (ex > 100 ? 100 : ex);
  const float scale = __uint_as_float((unsigned)(127 + 15 - ex) << 23), iscale = __uint_as_float((unsigned)(127 - 15 + ex) << 23);
  f32x4 kacc[3] = {};
  LAS char* fw = cb + CH_FT + hw * CH_FW;
#pragma unroll
  for (int ps = 0; ps < 2; ++ps) {
    const float lap = bf2f((unsigned short)la_[ps]), aip = bf2f((unsigned short)ai_[ps]);
    const float crp = cr[ps], cip = ci[ps];
    const float lre = -rbf(__builtin_amdgcn_exp2f(lap * 1.4426950408889634f)) * dt, lim = aip * dt;
    const float al = lre * 1.4426950408889634f, be = lim * 0.15915494309189535f;
    LAS char* wp = fw + lane * 4;
#define PUTH(row, v) *(LAS unsigned*)(wp + (row) * CH_FS) = (v)
    {
      const f32x2 w = cexp_p(al, be, 16.f);
      const float cs = 2.0f * dt * scale;
      const f32x2 c0 = {crp * cs, cip * cs};
      const f32x2 w2 = cmul(w, w), c1 = cmul(c0, w), w4 = cmul(w2, w2), c2 = cmul(c0, w2), c3 = cmul(c1, w2), w8 = cmul(w4, w4), w12 = cmul(w8, w4);
      const unsigned hc[4] = {hcv(c0), hcv(c1), hcv(c2), hcv(c3)}, hw4[3] = {hcv(w4), hcv(w8), hcv(w12)};
#pragma unroll
      for (int j = 0; j < 4; ++j) {
        PUTH(j, hc[j]);
#pragma unroll
        for (int k = 0; k < 3; ++k) PUTH(4 * (k + 1) + j, hmul(hc[j], hw4[k]));
      }
    }
    {
      const f32x2 e1 = cexp_p(al, -be, 1.f);
      const f32x2 e2 = cmul(e1, e1), e3 = cmul(e2, e1), e4 = cmul(e2, e2), e8 = cmul(e4, e4), e12 = cmul(e8, e4);
      unsigned E[16];
      E[0] = 0x00003c00u; E[1] = hcv(e1); E[2] = hcv(e2); E[3] = hcv(e3); E[4] = hcv(e4); E[8] = hcv(e8); E[12] = hcv(e12);
#pragma unroll
      for (int m = 1; m < 4; ++m)
#pragma unroll
        for (int j = 1; j < 4; ++j) E[4 * m + j] = hmul(E[4 * m], E[j]);
#pragma unroll
      for (int k = 0; k < 16; ++k) PUTH(16 + k, E[k]);
      const f32x2 z256 = cexp_p(al, -be, 256.f), z512 = cexp_p(al, -be, 512.f), z1024 = cexp_p(al, -be, 1024.f), z2048 = cexp_p(al, -be, 2048.f);
      const f32x2 z768 = cmul(z512, z256);
      const unsigned h256 = hcv(z256), h512 = hcv(z512), h768 = hcv(z768);
      PUTH(32, h256);
#pragma unroll
      for (int k = 1; k < 8; ++k) PUTH(32 + k, hmul(h256, E[2 * k]));
      PUTH(40, h512); PUTH(44, h768);
#pragma unroll
      for (int k = 1; k < 4; ++k) { PUTH(40 + k, hmul(h512, E[4 * k])); PUTH(44 + k, hmul(h768, E[4 * k])); }
      const unsigned hb[4] = {hcv(z1024), hcv(cmul(z1024, z256)), hcv(cmul(z1024, z512)), hcv(cmul(z1024, z768))};
#pragma unroll
      for (int k = 0; k < 4; ++k) { PUTH(48 + 2 * k, hb[k]); PUTH(49 + 2 * k, hmul(hb[k], E[8])); }
      PUTH(56, hcv(z2048));
    }
#undef PUTH
    asm volatile("s_waitcnt lgkmcnt(0)" ::: "memory");
    LAS const char* rp = fw + (lane & 15) * CH_FS + (lane >> 4) * 16;
    u32x4 fa[4], fb[4][3];
#pragma unroll
    for (int kk = 0; kk < 4; ++kk) {
      fa[kk] = *(LAS const u32x4*)(rp + kk * 64);
#pragma unroll
      for (int t = 0; t < 3; ++t) fb[kk][t] = *(LAS const u32x4*)(rp + (16 + 16 * t) * CH_FS + kk * 64);
    }
#pragma unroll
    for (int kk = 0; kk < 4; ++kk)
#pragma unroll
      for (int t = 0; t < 3; ++t) kacc[t] = mfma_h(fa[kk], fb[kk][t], kacc[t]);
    asm volatile("" ::: "memory");
  }
  STAMP(1, 1);
  u32x4 u0, u1;
  group_wait(poller, wsr, WS_FA + rep * 1024 + 7 * 128 + (lane & 31) * 4, tag, smem + B_CH + CH_CNT + 76);
  {
    const unsigned vu = WS_XT + d * SEQ * 2 + hw * 2048 + lane * 16;
    asm volatile("buffer_load_dwordx4 %0, %1, %2, 0 offen sc1" : "=v"(u0) : "v"(vu), "s"(wsr) : "memory");
    asm volatile("buffer_load_dwordx4 %0, %1, %2, 0 offen offset:1024 sc1" : "=v"(u1) : "v"(vu), "s"(wsr) : "memory");
  }
#pragma unroll
  for (int t = 0; t < 3; ++t) *(LAS f32x4*)(cb + CH_T + hw * CH_TS + (16 * t + (lane & 15)) * 64 + (lane >> 4) * 16) = kacc[t];
  lds_sync(cnt, 2, lane);
  {
    const int y = 2 * (64 * hw + lane);
    const unsigned po[12] = {pt0[0], pt0[1], pt0[2], pt0[3], pt1[0], pt1[1], pt1[2], pt1[3], pt2[0], pt2[1], pt2[2], pt2[3]};
    unsigned W[12], V[11];
#pragma unroll
    for (int k = 0; k < 12; ++k) {
      LAS const char* a0 = cb + CH_T + (po[k] & 0xffffu);
      LAS const char* a1 = cb + CH_T + (po[k] >> 16);
      W[k] = pk_f16(*(LAS const float*)a0 + *(LAS const float*)(a0 + CH_TS), *(LAS const float*)a1 + *(LAS const float*)(a1 + CH_TS));
    }
#pragma unroll
    for (int i = 0; i < 11; ++i) V[i] = __builtin_amdgcn_alignbit(W[i + 1], W[i], 16);
#pragma unroll
    for (int m = 0; m < 4; ++m) {
      *(LAS u32x4*)(cb + CH_FT + (2 * m) * CH_CS + y * 16) = (u32x4){W[m], W[m + 1], W[m + 2], W[m + 3]};
      *(LAS u32x4*)(cb + CH_FT + (2 * m) * CH_CS + y * 16 + 16) = (u32x4){W[m + 4], W[m + 5], W[m + 6], W[m + 7]};
      *(LAS u32x4*)(cb + CH_FT + (2 * m + 1) * CH_CS + y * 16) = (u32x4){V[m], V[m + 1], V[m + 2], V[m + 3]};
      *(LAS u32x4*)(cb + CH_FT + (2 * m + 1) * CH_CS + y * 16 + 16) = (u32x4){V[m + 4], V[m + 5], V[m + 6], V[m + 7]};
    }
    if (hw == 1 && lane < 8) *(LAS u32x4*)(cb + CH_FT + lane * CH_CS + 256 * 16) = (u32x4){0u, 0u, 0u, 0u};
  }
  asm volatile("s_waitcnt vmcnt(0)" : "+v"(u0), "+v"(u1)::"memory");
  *(LAS u32x4*)(cb + CH_U + 512 + hw * 2048 + lane * 16) = u0;
  *(LAS u32x4*)(cb + CH_U + 512 + hw * 2048 + 1024 + lane * 16) = u1;
  lds_sync(cnt, 4, lane);
  STAMP(1, 2);
  if (hw) s4d_conv<1>(cb, wsr, d, lane, iscale, dsk); else s4d_conv<0>(cb, wsr, d, lane, iscale, dsk);
  __builtin_amdgcn_s_setprio(0);
  WAIT_VM(0);
  {
    unsigned old = 0;
    if (lane == 0) old = __hip_atomic_fetch_add((LAS unsigned*)(smem + B_CH + CH_CNT + 84), 1u, __ATOMIC_RELAXED, __HIP_MEMORY_SCOPE_WORKGROUP);
    if (__builtin_amdgcn_readfirstlane(old) == 3u && lane < 8) st4(wsr, WS_FS + lane * 1024 + (d >> 1) * 4, tag);
  }
  STAMP(1, 3);
}

constexpr int C_PART = 0;
constexpr int C_XS = 65536;
constexpr int C_SS = C_XS + 64 * 136;
constexpr int C_OS = C_SS + 64 * 136;

constexpr int C_GO = C_OS + 64 * 144;
__device__ __forceinline__ void phaseC(LAS char* smem, const Params& p, rsrc_t wsr, unsigned tag, int rep, int T, int Dt, int tid MARKS_ARG) {
  char* ws = p.ws;
  const int lane = tid & 63;
  const int wave = __builtin_amdgcn_readfirstlane(tid >> 6);
  unsigned fo;
  {
    const int J = lane & 7, bh = 2 * (T >> 3) + (J >> 2) + 8 * ((T & 7) + 8 * (J & 3));
    const int bs = 32 * Dt + ((lane - 8) & 31);
    fo = (lane < 8 ? WS_FB + bh * 4 : WS_FS + bs * 4) + rep * 1024;
  }
  if (wave < 4) {
    const unsigned ho = WS_HF + ((4 * T * 28 + 7 * wave) * 64 + lane) * 16, wo = WS_WOF + ((4 * Dt * 28 + 7 * wave) * 64 + lane) * 16;
    u32x4 hf[7][4], wf[7][4];
    f32x4 acc[4][4];
#pragma unroll
    for (int k = 0; k < 7; ++k)
#pragma unroll
      for (int m = 0; m < 4; ++m) wf[k][m] = ld16(wsr, wo + (m * 28 + k) * 1024);
    __builtin_amdgcn_sched_barrier(0);
    group_wait(false, wsr, fo, tag, smem + C_GO);
    STAMP(2, 0);
#pragma unroll
    for (int n = 0; n < 4; ++n)
#pragma unroll
      for (int m = 0; m < 4; ++m) acc[n][m] = (f32x4){0.f, 0.f, 0.f, 0.f};
#pragma unroll
    for (int k = 0; k < 4; ++k)
#pragma unroll
      for (int m = 0; m < 4; ++m) hf[k][m] = ld16(wsr, ho + (m * 28 + k) * 1024);
    __builtin_amdgcn_sched_barrier(0);
#pragma unroll
    for (int k = 0; k < 7; ++k) {
#pragma unroll
      for (int n = 0; n < 4; ++n)
#pragma unroll
        for (int m = 0; m < 4; ++m) acc[n][m] = mfma_bf(__builtin_bit_cast(s16x8, wf[k][n]), __builtin_bit_cast(s16x8, hf[k][m]), acc[n][m]);
      __builtin_amdgcn_sched_barrier(0);
      if (k + 4 < 7) {
#pragma unroll
        for (int m = 0; m < 4; ++m) hf[k + 4][m] = ld16(wsr, ho + (m * 28 + k + 4) * 1024);
      }
      __builtin_amdgcn_sched_barrier(0);
    }
    STAMP(2, 1);
#pragma unroll
    for (int n = 0; n < 4; ++n)
#pragma unroll
      for (int m = 0; m < 4; ++m) *(LAS f32x4*)(smem + C_PART + ((wave * 4 + m) * 4 + n) * 1024 + lane * 16) = acc[n][m];
  } else {
    const int u = tid - 256, row = u >> 2, c = u & 3;
    const u32x4 x0 = *(const u32x4*)(p.x + (size_t)(64 * T + row) * DM + 64 * Dt + 16 * c);
    const u32x4 x1 = *(const u32x4*)(p.x + (size_t)(64 * T + row) * DM + 64 * Dt + 16 * c + 8);
#pragma unroll
    for (int e = 0; e < 2; ++e) {
      *(LAS u32x2*)(smem + C_XS + row * 136 + c * 32 + e * 8) = (u32x2){x0[2 * e], x0[2 * e + 1]};
      *(LAS u32x2*)(smem + C_XS + row * 136 + c * 32 + 16 + e * 8) = (u32x2){x1[2 * e], x1[2 * e + 1]};
    }
    group_wait(wave == 4, wsr, fo, tag, smem + C_GO);
    const unsigned so = WS_ST + ((64 * Dt + row) * SEQ + 64 * T + 16 * c) * 2;
    const u32x4 s0 = ld16(wsr, so), s1 = ld16(wsr, so + 16);
#pragma unroll
    for (int e = 0; e < 2; ++e) {
      *(LAS u32x2*)(smem + C_SS + row * 136 + c * 32 + e * 8) = (u32x2){s0[2 * e], s0[2 * e + 1]};
      *(LAS u32x2*)(smem + C_SS + row * 136 + c * 32 + 16 + e * 8) = (u32x2){s1[2 * e], s1[2 * e + 1]};
    }
  }
  BAR();
  {
    const int m = wave & 3, l15 = lane & 15, q = lane >> 4, t = 16 * m + l15;
#pragma unroll
    for (int nn = 0; nn < 2; ++nn) {
      const int n = 2 * (wave >> 2) + nn;
      f32x4 o = *(LAS const f32x4*)(smem + C_PART + ((0 * 4 + m) * 4 + n) * 1024 + lane * 16);
#pragma unroll
      for (int sw = 1; sw < 4; ++sw) o += *(LAS const f32x4*)(smem + C_PART + ((sw * 4 + m) * 4 + n) * 1024 + lane * 16);
      const s16x4 sv = tr_read(smem + C_SS + (16 * n + 4 * q + (l15 >> 2)) * 136 + (16 * m + 4 * (lane & 3)) * 2);
      const u32x2 sw2 = __builtin_bit_cast(u32x2, sv);
      const u32x2 xw = *(LAS const u32x2*)(smem + C_XS + t * 136 + (16 * n + 4 * q) * 2);
      unsigned ow[2];
#pragma unroll
      for (int pp = 0; pp < 2; ++pp) {
        const f32x2 v = up_bf(rn_pair((f32x2){o[2 * pp], o[2 * pp + 1]}));
        const f32x2 e = v * -1.4426950408889634f;
        const f32x2 dd = (f32x2){__builtin_amdgcn_exp2f(e.x), __builtin_amdgcn_exp2f(e.y)} + 1.0f;
        const f32x2 g = up_bf(rn_pair(v * (f32x2){__builtin_amdgcn_rcpf(dd.x), __builtin_amdgcn_rcpf(dd.y)}));
        const unsigned s_ = sw2[pp], x_ = xw[pp];
        ow[pp] = rn_pair(up_bf(x_) + up_bf(rn_pair(up_bf(s_) * g)));
      }
      *(LAS u32x2*)(smem + C_OS + t * 144 + (16 * n + 4 * q) * 2) = (u32x2){ow[0], ow[1]};
    }
  }
  BAR();
  {
    const int row = tid >> 3, c = tid & 7;
    *(u32x4*)(p.out + (size_t)(64 * T + row) * DM + 64 * Dt + 8 * c) = *(LAS const u32x4*)(smem + C_OS + row * 144 + c * 16);
  }
}

__global__ __launch_bounds__(512, 2) void mixer_block(Params p) {
  extern __shared__ __attribute__((aligned(16))) char smem_raw[];
  LAS char* smem = (LAS char*)smem_raw;
  char* ws = p.ws;
  const int tid = threadIdx.x, lane = tid & 63;
  const int wave = __builtin_amdgcn_readfirstlane(tid >> 6);
  const int b = blockIdx.x, xcd = b & 7, rr = b >> 3;
  const rsrc_t wsr = mk_rsrc(ws, WS_END);
  STAMP_DECL;
  STAMP(0, 0);
  const unsigned tag = *(const __attribute__((address_space(4))) unsigned*)(ws + WS_EP) + 1u;
  const int T2 = 8 * (xcd >> 1) + (rr & 7), J2 = 4 * (xcd & 1) + (rr >> 3);
  const bool aux = b >= 224;
  if (!aux) {
    phaseA_gemm(smem, p, wsr, tag, 4 * xcd + rr / 7, rr % 7, T2, tid MARKS_PASS);
    if (tid < 64) {
      if (lane < 32) *(LAS unsigned*)(smem + B_CH + CH_CNT + lane * 4) = 0u;
      else *(LAS unsigned*)(smem + B_CH + B_CHS + CH_CNT + (lane - 32) * 4) = 0u;
    }
  } else {
    if (tid < 32) *(LAS unsigned*)(smem + B_CH + CH_CNT + tid * 4) = 0u;
    BAR();
    if (wave < 4) {
      phaseA_aux(smem, p, wsr, tag, b - 224, tid);
    } else {
      if (wave == 5) poll_gemm2_inputs(smem, wsr, tag, xcd, J2, lane);
      else {
        const int pw = (b - 224) * 3 + (wave == 4 ? 0 : wave - 5);
#pragma unroll 8
        for (int k = 0; k < 88; ++k) {
          int pc = k * 96 + pw; pc = pc < 8192 ? pc : 8191;
          glds16((const char*)p.w_sp + (size_t)pc * 1024, (unsigned)lane * 16u, (unsigned)(AUX_PF + (wave - 4) * 1024));
          if ((k & 7) == 7) WAIT_VM(16);
        }
      }
      if (b == 255 && wave == 4) {
#pragma unroll
        for (int k = 0; k < 4; ++k) poll_flags(wsr, WS_FA + 7 * 1024 + (64 * k + lane) * 4, tag);
        if (lane == 0) st4(wsr, WS_EP, tag);
      }
    }
  }
  STAMP(0, 6);
  if (wave < 4) {
    if (!aux) BAR();
    phaseB_gemm(smem, p, wsr, tag, xcd, wave, lane, T2, J2 MARKS_PASS);
  } else if (!aux) {
    const int sw = wave - 4;
    phaseB_s4d(smem, smem + B_CH + (sw >> 1) * B_CHS, p, wsr, tag, xcd, sw == 0, sw == 1 ? J2 : -1, 2 * b + (sw >> 1), sw & 1, lane MARKS_PASS);
  }
  WAIT_VM(0);
  BAR();
  if (tid < 8) st4(wsr, WS_FB + tid * 1024 + b * 4, tag);
  STAMP(1, 4);
  if (!aux) {
    if (tid == 64) *(LAS unsigned*)(smem + C_GO) = 0u;
    BAR();
    phaseC(smem, p, wsr, tag, xcd, 4 * xcd + rr / 7, rr % 7, tid MARKS_PASS);
    STAMP(2, 2);
  }
  STAMP_DUMP();
}

extern "C" void kernel_launch(void* const* d_in, const int* in_sizes, int n_in, void* d_out, int out_size, void* d_ws, size_t ws_size, hipStream_t stream) {
  Params p;
  p.x = (const unsigned short*)d_in[0];
  p.logA = (const unsigned short*)d_in[1];
  p.Aim = (const unsigned short*)d_in[2];
  p.Cre = (const unsigned short*)d_in[3];
  p.Cim = (const unsigned short*)d_in[4];
  p.logdt = (const unsigned short*)d_in[5];
  p.Dsk = (const unsigned short*)d_in[6];
  p.w_in = (const unsigned short*)d_in[8];
  p.w_sp = (const unsigned short*)d_in[9];
  p.b_sp = (const unsigned short*)d_in[10];
  p.w_out = (const unsigned short*)d_in[11];
  p.ws = (char*)d_ws;
  p.out = (unsigned short*)d_out;
  (void)hipFuncSetAttribute((const void*)mixer_block, hipFuncAttributeMaxDynamicSharedMemorySize, 163840);
  mixer_block<<<256, 512, 163840, stream>>>(p);
}
```

```cpp
#include <hip/hip_runtime.h>
#include <hip/hip_bf16.h>
#include <stdint.h>

#define LAS __attribute__((address_space(3)))
#define GAS __attribute__((address_space(1)))
typedef float f32x2 __attribute__((ext_vector_type(2)));
typedef float f32x4 __attribute__((ext_vector_type(4)));
typedef float f32x16 __attribute__((ext_vector_type(16)));
typedef unsigned u32x2 __attribute__((ext_vector_type(2)));
typedef unsigned u32x4 __attribute__((ext_vector_type(4)));
typedef short s16x4 __attribute__((ext_vector_type(4)));
typedef short s16x8 __attribute__((ext_vector_type(8)));
typedef __bf16 bf16x2 __attribute__((ext_vector_type(2)));
typedef __bf16 bf16x8 __attribute__((ext_vector_type(8)));
typedef _Float16 f16x2 __attribute__((ext_vector_type(2)));
typedef _Float16 f16x8 __attribute__((ext_vector_type(8)));

#define S4D_PRIO 3
#define GEMM_PRIO 1
#define CONV_PRIO 0
#define EPI_PRIO 3
constexpr int SEQ = 2048, DM = 448, FH = 896, NST = 256;

constexpr unsigned WS_Z1D = 0;
constexpr unsigned WS_Z2F = WS_Z1D + SEQ * FH * 2;
constexpr unsigned WS_HF  = WS_Z2F + SEQ * FH * 2;
constexpr unsigned WS_XT  = WS_HF + SEQ * FH * 2;
constexpr unsigned WS_ST  = WS_XT + SEQ * DM * 2;
constexpr unsigned WS_WOF = WS_ST + SEQ * DM * 2;
constexpr unsigned WS_XCH = WS_WOF + FH * DM * 2;
constexpr unsigned WS_XS1 = WS_XCH + 32 * 64 * 8 * 16;
constexpr unsigned WS_FA  = WS_XS1 + 32 * 1024;
constexpr unsigned WS_FZ  = WS_FA + 8192;
constexpr unsigned WS_FB  = WS_FZ + 1024;
constexpr unsigned WS_FS  = WS_FB + 8192;
constexpr unsigned WS_EP  = WS_FS + 8192;
constexpr unsigned WS_END = WS_EP + 256;

constexpr size_t WS_DBG = 32u << 20;
#define STAMP_DECL do {} while (0)
#define STAMP(kern, slot) do {} while (0)
#define STAMP_DUMP() do {} while (0)
#define MARKS_ARG
#define MARKS_PASS

constexpr unsigned ONES_BF = 0x3f803f80u;
constexpr int AUX_SC1 = 16;
__device__ __forceinline__ float bf2f(unsigned short h) { return __uint_as_float((unsigned)h << 16); }
__device__ __forceinline__ float bflo(unsigned w) { return __uint_as_float(w << 16); }
__device__ __forceinline__ float bfhi(unsigned w) { return __uint_as_float(w & 0xffff0000u); }
__device__ __forceinline__ float rbf(float x) { return (float)(__bf16)x; }
__device__ __forceinline__ unsigned pk_f16(float lo, float hi) {
  f16x2 v = {(_Float16)lo, (_Float16)hi};
  return __builtin_bit_cast(unsigned, v);
}
__device__ __forceinline__ f32x2 up_bf(unsigned w) { return (f32x2){bflo(w), bfhi(w)}; }
__device__ __forceinline__ unsigned rn_pair(f32x2 v) {
  unsigned r;
  asm("v_cvt_pk_bf16_f32 %0, %1, %2" : "=v"(r) : "v"(v.x), "v"(v.y));
  return r;
}
template <int CTRL> __device__ __forceinline__ float dpp_add(float v) {
  int t = __builtin_amdgcn_update_dpp(0, __float_as_int(v), CTRL, 0xf, 0xf, true);
  return v + __int_as_float(t);
}
__device__ __forceinline__ float red8(float v) {
  v = dpp_add<0xB1>(v); v = dpp_add<0x4E>(v); v = dpp_add<0x141>(v);
  return v;
}
__device__ __forceinline__ float red16(float v) { return dpp_add<0x140>(red8(v)); }
__device__ __forceinline__ float xsum32(float v) {
  auto b = __builtin_amdgcn_permlane32_swap(__float_as_uint(v), __float_as_uint(v), false, false);
  return __uint_as_float(b[0]) + __uint_as_float(b[1]);
}
__device__ __forceinline__ float xsum16_32(float v) {
  auto a = __builtin_amdgcn_permlane16_swap(__float_as_uint(v), __float_as_uint(v), false, false);
  return xsum32(__uint_as_float(a[0]) + __uint_as_float(a[1]));
}
__device__ __forceinline__ void glds16(const void* sbase, unsigned voff, unsigned lds) {
  asm volatile("s_mov_b32 m0, %2\n\ts_nop 0\n\tglobal_load_lds_dwordx4 %0, %1" ::"v"(voff), "s"(sbase), "s"(lds) : "memory");
}
#define WAIT_VM(n) asm volatile("s_waitcnt vmcnt(%0)" ::"n"(n) : "memory")
#define BAR() do { asm volatile("s_waitcnt lgkmcnt(0)" ::: "memory"); __builtin_amdgcn_s_barrier(); asm volatile("" ::: "memory"); } while (0)
__device__ __forceinline__ s16x4 tr_read(LAS const char* p) {
  typedef short v4i16 __attribute__((ext_vector_type(4)));
  return __builtin_amdgcn_ds_read_tr16_b64_v4i16((LAS v4i16*)p);
}
__device__ __forceinline__ f32x4 mfma_bf(s16x8 a, s16x8 b, f32x4 c) {
  return __builtin_amdgcn_mfma_f32_16x16x32_bf16(__builtin_bit_cast(bf16x8, a), __builtin_bit_cast(bf16x8, b), c, 0, 0, 0);
}
__device__ __forceinline__ void mfma_acc(f32x4& c, u32x4 a, s16x8 b) {
  asm volatile("v_mfma_f32_16x16x32_bf16 %0, %1, %2, %0" : "+v"(c) : "v"(a), "v"(b));
}
#define MFMA_DONE() asm volatile("s_nop 7\n\ts_nop 7" ::: "memory")
__device__ __forceinline__ f32x16 mfma32_bf(s16x8 a, s16x8 b, f32x16 c) {
  return __builtin_amdgcn_mfma_f32_32x32x16_bf16(__builtin_bit_cast(bf16x8, a), __builtin_bit_cast(bf16x8, b), c, 0, 0, 0);
}
__device__ __forceinline__ f32x4 mfma_h(u32x4 a, u32x4 b, f32x4 c) {
  return __builtin_amdgcn_mfma_f32_16x16x32_f16(__builtin_bit_cast(f16x8, a), __builtin_bit_cast(f16x8, b), c, 0, 0, 0);
}
__device__ __forceinline__ float dot2bf(unsigned a, unsigned b, float c) {
  return __builtin_amdgcn_fdot2_f32_bf16(__builtin_bit_cast(bf16x2, a), __builtin_bit_cast(bf16x2, b), c, false);
}
__device__ __forceinline__ __amdgpu_buffer_rsrc_t mk_rsrc(const void* p, unsigned bytes) {
  return __builtin_amdgcn_make_buffer_rsrc((void*)p, (short)0, (int)bytes, 0x00020000);
}
typedef __amdgpu_buffer_rsrc_t rsrc_t;
__device__ __forceinline__ void st16(rsrc_t r, unsigned off, u32x4 v) { __builtin_amdgcn_raw_buffer_store_b128(v, r, off, 0, AUX_SC1); }
__device__ __forceinline__ void st8(rsrc_t r, unsigned off, u32x2 v) { __builtin_amdgcn_raw_buffer_store_b64(v, r, off, 0, AUX_SC1); }
__device__ __forceinline__ void st4(rsrc_t r, unsigned off, unsigned v) { __builtin_amdgcn_raw_buffer_store_b32(v, r, off, 0, AUX_SC1); }
__device__ __forceinline__ u32x4 ld16(rsrc_t r, unsigned off) { return __builtin_amdgcn_raw_buffer_load_b128(r, off, 0, AUX_SC1); }
__device__ __forceinline__ void bglds16(rsrc_t rs, unsigned voff, unsigned soff, unsigned lds) {
  asm volatile("s_mov_b32 m0, %3\n\ts_nop 0\n\tbuffer_load_dwordx4 %0, %1, %2 offen lds" ::"v"(voff), "s"(rs), "s"(soff), "s"(lds) : "memory");
}
__device__ __forceinline__ void bload16(u32x4& dst, rsrc_t rs, unsigned voff, unsigned soff) {
  asm volatile("buffer_load_dwordx4 %0, %1, %2, %3 offen sc1" : "=v"(dst) : "v"(voff), "s"(rs), "s"(soff) : "memory");
}
__device__ __forceinline__ void poll_granule(u32x4& gr, rsrc_t rs, unsigned voff, unsigned tag) {
  for (unsigned spins = 0;; ++spins) {
    const bool ok = (gr[1] == tag) && (gr[3] == tag);
    if (__all(ok) || spins > (1u << 20)) break;
    __builtin_amdgcn_s_sleep(1);
    asm volatile("buffer_load_dwordx4 %0, %1, %2, 0 offen sc1\n\ts_waitcnt vmcnt(0)" : "=&v"(gr) : "v"(voff), "s"(rs) : "memory");
  }
}
__device__ __forceinline__ void poll_flags(rsrc_t rs, unsigned voff, unsigned tag) {
  for (unsigned spins = 0;; ++spins) {
    unsigned f;
    asm volatile("buffer_load_dword %0, %1, %2, 0 offen sc1\n\ts_waitcnt vmcnt(0)" : "=&v"(f) : "v"(voff), "s"(rs) : "memory");
    if (__all(f == tag) || spins > (1u << 20)) break;
    __builtin_amdgcn_s_sleep(4);
  }
}
__device__ __forceinline__ void group_wait(bool poller, rsrc_t rs, unsigned voff, unsigned tag, LAS char* word) {
  if (poller) {
    poll_flags(rs, voff, tag);
    *(volatile LAS unsigned*)word = 1u;
  } else {
    while ((unsigned)__builtin_amdgcn_readfirstlane(*(volatile LAS unsigned*)word) == 0u) __builtin_amdgcn_s_sleep(2);
  }
  asm volatile("" ::: "memory");
}
__device__ __forceinline__ void lds_sync(LAS char* cntp, unsigned target, int lane) {
  LAS unsigned* cnt = (LAS unsigned*)cntp;
  asm volatile("s_waitcnt lgkmcnt(0)" ::: "memory");
  if (lane == 0) __hip_atomic_fetch_add(cnt, 1u, __ATOMIC_RELAXED, __HIP_MEMORY_SCOPE_WORKGROUP);
  while ((unsigned)__builtin_amdgcn_readfirstlane(*(volatile LAS unsigned*)cnt) < target) __builtin_amdgcn_s_sleep(1);
  asm volatile("" ::: "memory");
}
template <int V> struct IC { static constexpr int value = V; };
template <int A, int B, class F> __device__ __forceinline__ void static_for(F&& f) {
  if constexpr (A < B) { f(IC<A>{}); static_for<A + 1, B>(f); }
}
__device__ __forceinline__ f32x2 gelu2(f32x2 x) {
  const float c1 = -2.0f * 0.7978845608028654f * 1.4426950408889634f, c2 = c1 * 0.044715f;
  const f32x2 t = x * x;
  const f32x2 a = x * (t * c2 + c1);
  f32x2 d = {__builtin_amdgcn_exp2f(a.x), __builtin_amdgcn_exp2f(a.y)};
  d = d + 1.0f;
  return x * (f32x2){__builtin_amdgcn_rcpf(d.x), __builtin_amdgcn_rcpf(d.y)};
}
__device__ __forceinline__ void ln_scalars(float s, float q, float invn, float& mb, float& rr) {
  const float mean = s * invn;
  float var = q * invn - mean * mean; var = var < 0.f ? 0.f : var;
  mb = rbf(mean);
  rr = __builtin_amdgcn_rcpf(rbf(__builtin_amdgcn_sqrtf(rbf(rbf(var) + 1e-5f))));
}

struct Params {
  const unsigned short *x, *logA, *Aim, *Cre, *Cim, *logdt, *Dsk, *w_in, *w_sp, *b_sp, *w_out;
  char* ws;
  unsigned short* out;
};
#define TLBWARM 1

constexpr int A_X = 0;
constexpr int A_S = 57344;
constexpr int A_P = A_S;
constexpr int A_W1 = A_S + 2048;
constexpr int A_W = 65536;
constexpr int A_TB = A_W;
constexpr int A_M2 = A_W + 32768;
constexpr int A_R2 = A_M2 + 256;
constexpr int A_W1B = A_R2 + 256;
constexpr int A_SLOT = 16384;
constexpr int B_RING = 0;

struct W2Src { rsrc_t wr; unsigned voff0, voff1; };
__device__ __forceinline__ W2Src w2_src(const Params& p, int T, int wave, int lane) {
  W2Src s;
  s.wr = mk_rsrc((const char*)p.w_sp + (size_t)T * 64 * 4096 + wave * 1024, 64 * 4096 - wave * 1024);
  s.voff0 = (lane >> 3) * 4096 + (((lane & 7) ^ ((lane >> 4) & 3)) * 16);
  s.voff1 = (8 + (lane >> 3)) * 4096 + (((lane & 7) ^ (4 + ((lane >> 4) & 3))) * 16);
  return s;
}
__device__ __forceinline__ void w2_piece(const W2Src& s, int wave, int pair, int piece) {
  bglds16(s.wr, (piece & 1) ? s.voff1 : s.voff0, (unsigned)((piece >> 1) * 65536 + pair * 128), (unsigned)(B_RING + wave * 16384 + (pair & 1) * 8192 + piece * 1024));
}

constexpr int AUX_T = 0;
constexpr int AUX_CNT = B_RING + 65536 + 44160 + 80;
constexpr int AUX_PF = 131072;
__device__ __forceinline__ void phaseA_aux(LAS char* smem, const Params& p, rsrc_t wsr, unsigned tag, int v, int tid) {
  char* ws = p.ws;
  const int lane = tid & 63;
  const int wave = __builtin_amdgcn_readfirstlane(tid >> 6);
  const int row = tid >> 2, c4 = tid & 3;
  u32x4 xv[14];
#pragma unroll
  for (int m = 0; m < 14; ++m) xv[m] = *(const u32x4*)(p.x + (size_t)(64 * v + row) * DM + (c4 + 4 * m) * 8);
  unsigned touch[2];
#pragma unroll
  for (int k = 0; k < 2; ++k) {
    const int i = tid + 256 * k;
    const char* a;
    if (i < 256) a = ws + (size_t)(i < (int)(WS_END >> 16) ? i : 0) * 65536;
    else if (i < 384) a = (const char*)p.w_sp + (size_t)(i - 256) * 65536;
    else if (i < 416) a = (const char*)p.w_in + (size_t)(i - 384 < 24 ? i - 384 : 0) * 65536;
    else if (i < 432) a = (const char*)p.w_out + (size_t)(i - 416 < 12 ? i - 416 : 0) * 65536;
    else if (i < 448) a = ((i >> 2) & 3) == 0 ? (const char*)p.logA + (i & 3) * 57344 : ((i >> 2) & 3) == 1 ? (const char*)p.Aim + (i & 3) * 57344
                        : ((i >> 2) & 3) == 2 ? (const char*)p.Cre + (i & 3) * 57344 : (const char*)p.Cim + (i & 3) * 57344;
    else if (i < 480) a = (const char*)p.out + (size_t)(i - 448 < 28 ? i - 448 : 0) * 65536;
    else a = (const char*)p.x + (size_t)(i - 480 < 28 ? i - 480 : 0) * 65536;
    asm volatile("global_load_dword %0, %1, off" : "=v"(touch[k]) : "v"(a) : "memory");
  }
  float s = 0.f, q = 0.f;
#pragma unroll
  for (int m = 0; m < 14; ++m)
#pragma unroll
    for (int e = 0; e < 4; ++e) { const unsigned w = xv[m][e]; s = dot2bf(w, ONES_BF, s); q = dot2bf(w, w, q); }
  s = dpp_add<0x4E>(dpp_add<0xB1>(s)); q = dpp_add<0x4E>(dpp_add<0xB1>(q));
  float mb, rr;
  ln_scalars(s, q, 1.0f / 448.0f, mb, rr);
  asm volatile("s_waitcnt vmcnt(0)" : "+v"(touch[0]), "+v"(touch[1])::"memory");
  if (c4 == 0) st16(wsr, WS_XS1 + v * 1024 + row * 16, (u32x4){__float_as_uint(mb), tag, __float_as_uint(rr), tag});
  unsigned short wv[7][8];
#pragma unroll
  for (int k = 0; k < 7; ++k) {
    int fr = v * 4 + wave + 128 * k; fr = fr < 784 ? fr : 783;
    const int dt = fr / 28, fb = fr - dt * 28;
    const int dcol = 16 * dt + (lane & 15), qq = lane >> 4;
#pragma unroll
    for (int jj = 0; jj < 8; ++jj) wv[k][jj] = p.w_out[(size_t)(32 * fb + 16 * (jj >> 2) + 4 * qq + (jj & 3)) * DM + dcol];
  }
  LAS char* tb = smem + AUX_T;
#pragma unroll
  for (int m = 0; m < 14; ++m)
#pragma unroll
    for (int e = 0; e < 4; ++e) {
      const unsigned w = xv[m][e];
      const f32x2 n3 = up_bf(rn_pair(up_bf(rn_pair(up_bf(w) - mb)) * rr));
      const int d = (c4 + 4 * m) * 8 + 2 * e;
      *(LAS _Float16*)(tb + d * 136 + row * 2) = (_Float16)n3.x;
      *(LAS _Float16*)(tb + (d + 1) * 136 + row * 2) = (_Float16)n3.y;
    }
  lds_sync(smem + AUX_CNT, 4, lane);
#pragma unroll
  for (int m = 0; m < 14; ++m) {
    const int d = (tid >> 3) + 32 * m, c8 = tid & 7;
    const u32x2 lo = *(LAS const u32x2*)(tb + d * 136 + c8 * 16), hi = *(LAS const u32x2*)(tb + d * 136 + c8 * 16 + 8);
    st16(wsr, WS_XT + (d * SEQ + 64 * v + 8 * c8) * 2, (u32x4){lo[0], lo[1], hi[0], hi[1]});
  }
#pragma unroll
  for (int k = 0; k < 7; ++k) {
    const int fr = v * 4 + wave + 128 * k;
    if (fr < 784)
      st16(wsr, WS_WOF + (fr * 64 + lane) * 16,
           (u32x4){(unsigned)wv[k][0] | ((unsigned)wv[k][1] << 16), (unsigned)wv[k][2] | ((unsigned)wv[k][3] << 16), (unsigned)wv[k][4] | ((unsigned)wv[k][5] << 16),
                   (unsigned)wv[k][6] | ((unsigned)wv[k][7] << 16)});
  }
  WAIT_VM(0);
  lds_sync(smem + AUX_CNT, 8, lane);
  if (tid < 8) st4(wsr, WS_FA + tid * 1024 + (7 * 32 + v) * 4, tag);
}

__device__ __forceinline__ void phaseA_gemm(LAS char* smem, const Params& p, rsrc_t wsr, unsigned tag, int ti, int tj, int T2, int tid MARKS_ARG) {
  char* ws = p.ws;
  const int lane = tid & 63;
  const int wave = __builtin_amdgcn_readfirstlane(tid >> 6);
  const int rh = wave >> 2, cg = wave & 3, h = lane >> 5, l31 = lane & 31;
  const int xrow = 8 * wave + (lane >> 3);
  const unsigned voffx = (unsigned)(xrow * 896 + (((lane & 7) ^ ((xrow >> 1) & 7)) * 16));
  const char* xsrc = (const char*)p.x + (size_t)ti * 64 * 896;
  unsigned voffw[2];
#pragma unroll
  for (int e = 0; e < 2; ++e) voffw[e] = (unsigned)((8 * wave + 4 * e + (lane >> 4)) * 3584 + (((lane & 15) ^ (4 * (lane >> 4))) * 16) + tj * 256);
#define A_XPIECE(g) glds16(xsrc + (g) * 128, voffx, (unsigned)(A_X + (g) * 8192 + wave * 1024))
#define A_WPIECE(G, e) \
  glds16(p.w_in, voffw[e] + (unsigned)(((G) % 7) * 64 * 3584 + ((G) < 7 ? 1792 : 0)), (unsigned)(A_W + ((G) % 6) * A_SLOT + wave * 2048 + (e) * 1024))
#pragma unroll
  for (int g = 0; g < 6; ++g) { A_XPIECE(g); A_WPIECE(g, 0); A_WPIECE(g, 1); }
  A_XPIECE(6);
  LAS const char* ab[2];
  LAS const char* xb[4];
  {
    const int g4 = lane >> 4, qq = (lane & 15) >> 2, p4 = lane & 3;
    const int lb = (8 * (g4 >> 1) + qq) * 256 + (((4 * cg + 2 * (g4 & 1) + (p4 >> 1)) ^ (4 * qq)) * 16) + (p4 & 1) * 8;
    ab[0] = smem + A_W + lb + rh * 4096;
    ab[1] = smem + A_W + lb + (rh ^ 1) * 4096;
    const int row = 32 * rh + l31, sw = (row >> 1) & 7;
#pragma unroll
    for (int i = 0; i < 4; ++i) xb[i] = smem + A_X + row * 128 + (((2 * (i ^ rh) + h) ^ sw) * 16);
  }
  s16x8 xa[28], wb[8];
#define A_FRAGS(st)                                                                                                    \
  do {                                                                                                                 \
    const int G_ = (st) >> 2, i_ = (st) & 3, so_ = (G_ % 6) * A_SLOT + (i_ >> 1) * 8192;                               \
    if ((st) < 28) xa[(st) % 28] = *(LAS const s16x8*)(xb[i_] + G_ * 8192);                                            \
    wb[(st) & 7] = __builtin_shufflevector(tr_read(ab[i_ & 1] + so_), tr_read(ab[i_ & 1] + so_ + 1024), 0, 1, 2, 3, 4, 5, 6, 7); \
  } while (0)
  f32x16 acc2 = {}, acc1 = {};
  float w1a = 0.f, w1b = 0.f;
  u32x4 g1;
  const unsigned g1o = WS_XS1 + ti * 1024 + (32 * rh + l31) * 16;
  unsigned zp[8] = {};
  float m1 = 0.f, r1 = 0.f, ss = 0.f, sq = 0.f;
  u32x4 gr;
  const int row = tid >> 3, jj = tid & 7;
  const unsigned vo = WS_XCH + ti * 8192 + row * 128 + (jj < 7 ? jj : 0) * 16;
  WAIT_VM(13);
  BAR();
  STAMP(0, 1);
  A_FRAGS(0); A_FRAGS(1); A_FRAGS(2); A_FRAGS(3);
  auto step = [&](auto stc) __attribute__((always_inline)) {
    constexpr int st = decltype(stc)::value;
    constexpr int G = st >> 2, i = st & 3;
    if (i == 0 && G >= 1) {
      if (G == 1) WAIT_VM(10); else if (G == 2) WAIT_VM(9); else if (G == 3) WAIT_VM(8); else if (G == 4) WAIT_VM(7); else if (G <= 6) WAIT_VM(6);
      else if (G == 7) asm volatile("s_waitcnt vmcnt(3) lgkmcnt(0)" : "+v"(g1)::"memory");
      else if (G == 8) WAIT_VM(6);
      else if (G == 9) WAIT_VM(6); else if (G == 10) WAIT_VM(4);
      else if (G == 11) asm volatile("s_waitcnt vmcnt(2) lgkmcnt(0)" ::: "memory");
      else if (G == 12) WAIT_VM(0);
      __builtin_amdgcn_s_barrier();
      asm volatile("" ::: "memory");
      if (G == 7) STAMP(0, 2);
    }
    __builtin_amdgcn_sched_barrier(0);
    if constexpr (st < 28) acc2 = mfma32_bf(wb[st & 7], xa[st % 28], acc2); else acc1 = mfma32_bf(wb[st & 7], xa[st % 28], acc1);
    if constexpr (st + 4 < 56) A_FRAGS(st + 4);
    if constexpr ((i & 1) == 0) {
      const u32x4 wv = __builtin_bit_cast(u32x4, wb[st & 7]);
#pragma unroll
      for (int e = 0; e < 4; ++e) { const unsigned ww = wv[e]; if (st < 28) w1a = dot2bf(ww, ONES_BF, w1a); else w1b = dot2bf(ww, ONES_BF, w1b); }
    }
    if constexpr (st == 27) {
      const float w1s = xsum32(w1a);
      if (h == 0) *(LAS float*)(smem + A_W1 + (rh * 128 + 32 * cg + l31) * 4) = w1s;
    }
    if constexpr (st == 28) {
      poll_granule(g1, wsr, g1o, tag);
      m1 = __uint_as_float(g1[0]); r1 = __uint_as_float(g1[2]);
    }
    if constexpr (G >= 7 && G <= 10 && i == 1) {
      constexpr int g = G - 7;
      const f32x4 w1 = *(LAS const f32x4*)(smem + A_W1 + (32 * cg + 8 * g + 4 * h) * 4) + *(LAS const f32x4*)(smem + A_W1 + (128 + 32 * cg + 8 * g + 4 * h) * 4);
#pragma unroll
      for (int pp = 0; pp < 2; ++pp) {
        const f32x2 a2 = {acc2[4 * g + 2 * pp], acc2[4 * g + 2 * pp + 1]}, w2v = {w1[2 * pp], w1[2 * pp + 1]};
        const unsigned zz = rn_pair(gelu2(up_bf(rn_pair((a2 - w2v * m1) * r1))));
        zp[2 * g + pp] = zz;
        ss = dot2bf(zz, ONES_BF, ss); sq = dot2bf(zz, zz, sq);
      }
    }
    if constexpr (st == 43) {
      const float s2 = xsum32(ss), q2 = xsum32(sq);
      if (h == 0) *(LAS f32x2*)(smem + A_P + ((32 * rh + l31) * 4 + cg) * 8) = (f32x2){s2, q2};
    }
    if constexpr (st == 44) {
      if (wave == 0) {
        const f32x4 p0 = *(LAS const f32x4*)(smem + A_P + lane * 32), p1 = *(LAS const f32x4*)(smem + A_P + lane * 32 + 16);
        const float s = (p0[0] + p0[2]) + (p1[0] + p1[2]), q = (p0[1] + p0[3]) + (p1[1] + p1[3]);
        u32x4 gr = {__float_as_uint(s), tag, __float_as_uint(q), tag};
        const unsigned vo = WS_XCH + ti * 8192 + lane * 128 + tj * 16;
        asm volatile("buffer_store_dwordx4 %0, %1, %2, 0 offen sc1\n\ts_nop 1" ::"v"(gr), "v"(vo), "s"(wsr) : "memory");
      }
    }
    __builtin_amdgcn_sched_barrier(0);
    if constexpr ((i & 1) == 0) {
      constexpr int ks = st >> 1;
      if constexpr (ks >= 2 && ks < 18) A_WPIECE(5 + (ks >> 1), ks & 1);
    }
    if constexpr (st == 20) asm volatile("buffer_load_dwordx4 %0, %1, %2, 0 offen sc1" : "=v"(g1) : "v"(g1o), "s"(wsr) : "memory");
    if constexpr (st == 52) asm volatile("buffer_load_dwordx4 %0, %1, %2, 0 offen sc1" : "=v"(gr) : "v"(vo), "s"(wsr) : "memory");
    __builtin_amdgcn_sched_barrier(0);
  };
  static_for<0, 56>(step);
#undef A_FRAGS
#undef A_XPIECE
#undef A_WPIECE
  STAMP(0, 3);
  {
    const float w1s = xsum32(w1b);
    if (h == 0) *(LAS float*)(smem + A_W1B + (rh * 128 + 32 * cg + l31) * 4) = w1s;
  }
  asm volatile("s_waitcnt vmcnt(0)" : "+v"(gr)::"memory");
  poll_granule(gr, wsr, vo, tag);
  {
    const float s = red8(jj < 7 ? __uint_as_float(gr[0]) : 0.f), q = red8(jj < 7 ? __uint_as_float(gr[2]) : 0.f);
    float mb, rr2;
    ln_scalars(s, q, 1.0f / 896.0f, mb, rr2);
    if (jj == 0) { *(LAS float*)(smem + A_M2 + row * 4) = mb; *(LAS float*)(smem + A_R2 + row * 4) = rr2; }
  }
  BAR();
  {
    LAS char* tb = smem + A_TB + wave * 2304;
    const int srow = 32 * rh + l31;
    const float mb2 = *(LAS const float*)(smem + A_M2 + srow * 4), rr2 = *(LAS const float*)(smem + A_R2 + srow * 4);
#pragma unroll
    for (int g = 0; g < 4; ++g) {
      unsigned o[2];
#pragma unroll
      for (int pp = 0; pp < 2; ++pp) o[pp] = rn_pair(up_bf(rn_pair(up_bf(zp[2 * g + pp]) - mb2)) * rr2);
      *(LAS u32x2*)(tb + l31 * 72 + (8 * g + 4 * h) * 2) = (u32x2){o[0], o[1]};
    }
    asm volatile("s_waitcnt lgkmcnt(0)" ::: "memory");
    const int q4 = lane >> 4, l15 = lane & 15;
#pragma unroll
    for (int n = 0; n < 2; ++n) {
      LAS const char* rp = tb + (8 * q4 + (l15 >> 2)) * 72 + n * 32 + (lane & 3) * 8;
      const s16x4 lo = tr_read(rp), hi = tr_read(rp + 4 * 72);
      const s16x8 o = __builtin_shufflevector(lo, hi, 0, 1, 2, 3, 4, 5, 6, 7);
      const int ft = 8 * tj + 2 * cg + n, sb = 2 * ti + rh;
      st16(wsr, WS_Z2F + ((ft * 64 + sb) * 64 + lane) * 16, __builtin_bit_cast(u32x4, o));
    }
  }
  STAMP(0, 4);
  {
    const int tt = 4 * ti + 2 * rh + (l31 >> 4);
    unsigned o[4][2];
    auto part = [&](auto gc) __attribute__((always_inline)) {
      constexpr int g = decltype(gc)::value;
      const f32x4 w1 = *(LAS const f32x4*)(smem + A_W1B + (32 * cg + 8 * g + 4 * h) * 4) + *(LAS const f32x4*)(smem + A_W1B + (128 + 32 * cg + 8 * g + 4 * h) * 4);
#pragma unroll
      for (int pp = 0; pp < 2; ++pp) {
        const f32x2 a2 = {acc1[4 * g + 2 * pp], acc1[4 * g + 2 * pp + 1]}, w2v = {w1[2 * pp], w1[2 * pp + 1]};
        o[g][pp] = rn_pair(gelu2(up_bf(rn_pair((a2 - w2v * m1) * r1))));
      }
    };
    static_for<0, 2>(part);
    __builtin_amdgcn_sched_barrier(0);
    WAIT_VM(0);
    BAR();
    if (tid < 8) st4(wsr, WS_FA + tid * 1024 + (tj * 32 + ti) * 4, tag);
    STAMP(0, 5);
    if (wave < 4) {
      const W2Src w2 = w2_src(p, T2, wave, lane);
#pragma unroll
      for (int k = 0; k < 16; ++k) w2_piece(w2, wave, k >> 3, k & 7);
    }
    static_for<2, 4>(part);
#pragma unroll
    for (int g = 0; g < 4; ++g) {
      const int ft = 8 * tj + 2 * cg + (g >> 1), lp = (lane & 15) + 16 * (2 * (g & 1) + h);
      st8(wsr, WS_Z1D + ((tt * 56 + ft) * 64 + lp) * 8, (u32x2){o[g][0], o[g][1]});
    }
  }
}

constexpr int B_CH = 65536;
constexpr int B_CHS = 49152;
constexpr int CH_FT = 0;
constexpr int CH_FW = 16416, CH_FS = 288;
constexpr int CH_CS = 4160;
constexpr int CH_U = 33280;
constexpr int CH_T = CH_U + 4608;
constexpr int CH_TS = 49 * 64;
constexpr int CH_CNT = CH_T + 2 * CH_TS;

__device__ __forceinline__ void z1_signal(LAS char* smem, rsrc_t wsr, unsigned tag, int fz, int lane) {
  if (fz >= 0) {
    unsigned old = 0;
    if (lane == 0) old = __hip_atomic_fetch_add((LAS unsigned*)(smem + B_CH + CH_CNT + 88), 1u, __ATOMIC_RELAXED, __HIP_MEMORY_SCOPE_WORKGROUP);
    if (__builtin_amdgcn_readfirstlane(old) == 7u && lane == 0) st4(wsr, WS_FZ + fz * 4, tag);
  }
}
template <int W>
__device__ __forceinline__ void b_epi(LAS char* smem, rsrc_t wsr, f32x4 (&acc)[7][4], const u32x2 (&z1v)[7], float bs, int lane, int tt, int J) {
  LAS char* cnt = smem + B_CH + CH_CNT + 64;
  LAS char* mine = smem + B_RING + W * 16384 + lane * 16;
  LAS char* other = smem + B_RING + (W ^ 2) * 16384 + lane * 16;
  LAS char* o2 = smem + B_RING + (W ^ 3) * 16384 + lane * 16;
  constexpr int K0 = W & 2, S0 = K0 ^ 2;
#pragma unroll
  for (int n = 0; n < 7; ++n) { *(LAS f32x4*)(mine + n * 1024) = acc[n][S0]; *(LAS f32x4*)(mine + (7 + n) * 1024) = acc[n][S0 + 1]; }
  lds_sync(cnt, 4, lane);
#pragma unroll
  for (int n = 0; n < 7; ++n) { acc[n][K0] += *(LAS const f32x4*)(other + n * 1024); acc[n][K0 + 1] += *(LAS const f32x4*)(other + (7 + n) * 1024); }
  asm volatile("s_waitcnt lgkmcnt(0)" ::: "memory");
#pragma unroll
  for (int n = 0; n < 7; ++n) *(LAS f32x4*)(other + n * 1024) = acc[n][W ^ 1];
  lds_sync(cnt, 8, lane);
  unsigned hp[7][2];
#pragma unroll
  for (int n = 0; n < 7; ++n) {
    const f32x4 v = acc[n][W] + *(LAS const f32x4*)(o2 + n * 1024);
#pragma unroll
    for (int pp = 0; pp < 2; ++pp) {
      const f32x2 g0 = up_bf(rn_pair((f32x2){v[2 * pp], v[2 * pp + 1]})) + bs;
      const unsigned zw = z1v[n][pp];
      hp[n][pp] = rn_pair(up_bf(rn_pair(g0)) * up_bf(zw));
    }
  }
  const unsigned hb = WS_HF + ((tt * 28 + (7 * J >> 1)) * 64 + lane) * 16;
  if (!(J & 1)) {
#pragma unroll
    for (int pp = 0; pp < 3; ++pp) st16(wsr, hb + pp * 1024, (u32x4){hp[2 * pp][0], hp[2 * pp][1], hp[2 * pp + 1][0], hp[2 * pp + 1][1]});
    st8(wsr, hb + 3 * 1024, (u32x2){hp[6][0], hp[6][1]});
  } else {
    st8(wsr, hb + 8, (u32x2){hp[0][0], hp[0][1]});
#pragma unroll
    for (int pp = 0; pp < 3; ++pp) st16(wsr, hb + (pp + 1) * 1024, (u32x4){hp[2 * pp + 1][0], hp[2 * pp + 1][1], hp[2 * pp + 2][0], hp[2 * pp + 2][1]});
  }
}

__device__ __forceinline__ void phaseB_gemm(LAS char* smem, const Params& p, rsrc_t wsr, unsigned tag, int rep, int fz_, int wave, int lane, int T, int J MARKS_ARG) {
  char* ws = p.ws;
  const rsrc_t zs = mk_rsrc(ws + WS_Z2F + (size_t)(7 * J * 64 + 16 * wave) * 1024, 7 * 65536 - 16 * wave * 1024);
  const W2Src w2 = w2_src(p, T, wave, lane);
  const unsigned voffz = lane * 16;
  const unsigned ring = B_RING + wave * 16384;
  LAS const char* wrd0 = smem + ring + (lane & 15) * 128 + (((lane >> 4) ^ ((lane >> 1) & 7)) * 16);
  LAS const char* wrd1 = smem + ring + (lane & 15) * 128 + (((4 + (lane >> 4)) ^ ((lane >> 1) & 7)) * 16);
  u32x4 z[3][7];
  f32x4 acc[7][4];
  {
    const int tj0 = (112 * J) >> 7, tj1 = (112 * J + 111) >> 7;
    group_wait(wave == 0, wsr, WS_FA + rep * 1024 + (lane < 32 ? tj0 : tj1) * 128 + (lane & 31) * 4, tag, smem + B_CH + CH_CNT + 72);
  }
  __builtin_amdgcn_s_setprio(GEMM_PRIO);
  STAMP(1, 0);
#define B_ISSUE_Z(it) _Pragma("unroll") for (int n = 0; n < 7; ++n) bload16(z[(it) % 3][n], zs, voffz, n * 65536 + (it) * 1024)
  B_ISSUE_Z(0); B_ISSUE_Z(1); B_ISSUE_Z(2);
#undef B_ISSUE_Z
  const int tt = 4 * T + wave;
  u32x2 z1v[7];
  unsigned bsw, fz;
#define B_WAITZ(N) asm volatile("s_waitcnt vmcnt(" #N ")" : "+v"(zc[0]), "+v"(zc[1]), "+v"(zc[2]), "+v"(zc[3]), "+v"(zc[4]), "+v"(zc[5]), "+v"(zc[6])::"memory")
#pragma unroll
  for (int it = 0; it < 16; ++it) {
    u32x4(&zc)[7] = z[it % 3];
    if (it <= 1) B_WAITZ(14); else if (it <= 13) B_WAITZ(22); else if (it == 14) B_WAITZ(15); else B_WAITZ(8);
    if (it == 0) z1_signal(smem, wsr, tag, fz_, lane);
    s16x8 wf[4];
#pragma unroll
    for (int m = 0; m < 4; ++m) wf[m] = *(LAS const s16x8*)(((it & 1) ? wrd1 : wrd0) + ((it >> 1) & 1) * 8192 + m * 2048);
#pragma unroll
    for (int n = 0; n < 7; ++n) {
#pragma unroll
      for (int m = 0; m < 4; ++m) {
        if (it == 0) acc[n][m] = mfma_bf(__builtin_bit_cast(s16x8, zc[n]), wf[m], (f32x4){0.f, 0.f, 0.f, 0.f});
        else mfma_acc(acc[n][m], zc[n], wf[m]);
      }
      __builtin_amdgcn_sched_barrier(0);
      if (it + 3 < 16) {
        bload16(zc[n], zs, voffz, n * 65536 + (it + 3) * 1024);
        if ((it & 1) && n >= 1 && n <= 4) { w2_piece(w2, wave, (it + 3) >> 1, 2 * (n - 1)); w2_piece(w2, wave, (it + 3) >> 1, 2 * (n - 1) + 1); }
      }
      if (it == 9 && n == 6) {
        int ln = lane; asm volatile("" : "+v"(ln));
        const unsigned fzo = WS_FZ + (((ln & 1) ? (112 * J + 111) >> 7 : (112 * J) >> 7) * 32 + T) * 4;
        asm volatile("buffer_load_dword %0, %1, %2, 0 offen sc1" : "=v"(fz) : "v"(fzo), "s"(wsr) : "memory");
      }
      if (it == 13) {
        if (n == 0) {
          asm volatile("" : "+v"(fz));
          if (!__all(fz == tag)) {
            int ln = lane; asm volatile("" : "+v"(ln));
            poll_flags(wsr, WS_FZ + (((ln & 1) ? (112 * J + 111) >> 7 : (112 * J) >> 7) * 32 + T) * 4, tag);
          }
        }
        int l8 = lane; asm volatile("" : "+v"(l8));
        const unsigned vo = WS_Z1D + (tt * 56 + 7 * J + n) * 512 + l8 * 8;
        asm volatile("buffer_load_dwordx2 %0, %1, %2, 0 offen sc1" : "=v"(z1v[n]) : "v"(vo), "s"(wsr) : "memory");
        if (n == 6) {
          const unsigned vb = (unsigned)(16 * tt + (l8 & 15)) * 2u;
          asm volatile("global_load_ushort %0, %1, %2" : "=v"(bsw) : "v"(vb), "s"(p.b_sp) : "memory");
        }
      }
      __builtin_amdgcn_sched_barrier(0);
    }
  }
#undef B_WAITZ
  MFMA_DONE();
  __builtin_amdgcn_s_setprio(EPI_PRIO);
  STAMP(1, 1);
  asm volatile("s_waitcnt vmcnt(0)" : "+v"(z1v[0]), "+v"(z1v[1]), "+v"(z1v[2]), "+v"(z1v[3]), "+v"(z1v[4]), "+v"(z1v[5]), "+v"(z1v[6]), "+v"(bsw)::"memory");
  const float bs = bf2f((unsigned short)bsw);
  if (wave == 0) b_epi<0>(smem, wsr, acc, z1v, bs, lane, tt, J);
  else if (wave == 1) b_epi<1>(smem, wsr, acc, z1v, bs, lane, tt, J);
  else if (wave == 2) b_epi<2>(smem, wsr, acc, z1v, bs, lane, tt, J);
  else b_epi<3>(smem, wsr, acc, z1v, bs, lane, tt, J);
  __builtin_amdgcn_s_setprio(0);
  STAMP(1, 2);
}

__device__ __forceinline__ f32x2 cmul(f32x2 a, f32x2 b) {
  f32x2 t, r;
  asm("v_pk_mul_f32 %0, %1, %2 op_sel:[0,0] op_sel_hi:[0,1]" : "=v"(t) : "v"(a), "v"(b));
  asm("v_pk_fma_f32 %0, %1, %2, %3 op_sel:[1,1,0] op_sel_hi:[1,0,1] neg_lo:[1,0,0]" : "=v"(r) : "v"(a), "v"(b), "v"(t));
  return r;
}
__device__ __forceinline__ unsigned hmul(unsigned a, unsigned b) {
  unsigned t, r;
  asm("v_pk_mul_f16 %0, %1, %2 op_sel:[0,0] op_sel_hi:[0,1]" : "=v"(t) : "v"(a), "v"(b));
  asm("v_pk_fma_f16 %0, %1, %2, %3 op_sel:[1,1,0] op_sel_hi:[1,0,1] neg_lo:[1,0,0]" : "=v"(r) : "v"(a), "v"(b), "v"(t));
  return r;
}
__device__ __forceinline__ unsigned hcv(f32x2 v) { return pk_f16(v.x, v.y); }
__device__ __forceinline__ f32x2 cexp_p(float al, float be, float pp) {
  const float e = __builtin_amdgcn_exp2f(al * pp), th = __builtin_amdgcn_fractf(be * pp);
  return (f32x2){__builtin_amdgcn_cosf(th), __builtin_amdgcn_sinf(th)} * e;
}
struct PosTab { unsigned short v[2064]; };
constexpr int pos_bf16(int j) {
  if (j < 256) return j;
  int sh = 1; while ((j >> sh) >= 256) ++sh;
  const int q = j >> sh, r = j & ((1 << sh) - 1), h = 1 << (sh - 1);
  return (r > h || (r == h && (q & 1)) ? q + 1 : q) << sh;
}
constexpr PosTab make_postab() {
  PosTab t = {};
  for (int x = 0; x < 2048; ++x) {
    const int p = pos_bf16(2047 - x);
    int col = 0, a = 0;
    if (p < 256) { a = p >> 4; col = p & 15; }
    else if (p < 512) { const int r = p - 256; a = r >> 4; col = 16 + ((r & 15) >> 1); }
    else if (p < 1024) { const int r = p - 512; a = (r & 255) >> 4; col = 24 + 4 * (r >> 8) + ((r & 15) >> 2); }
    else if (p < 2048) { const int r = p - 1024; a = (r & 255) >> 4; col = 32 + 2 * (r >> 8) + ((r & 15) >> 3); }
    else { a = 0; col = 40; }
    t.v[x] = (unsigned short)((col * 16 + a) * 4);
  }
  for (int x = 2048; x < 2064; ++x) t.v[x] = 48 * 64;
  return t;
}
__device__ const PosTab POSTAB = make_postab();

template <int HW>
__device__ __forceinline__ void s4d_conv(LAS char* cb, rsrc_t wsr, int d, int lane, float iscale, float dsk) {
  constexpr int NF = HW ? 7 : 8;
  f32x4 acc[8] = {};
  const int r = lane & 15, h = lane >> 4;
  LAS const char* ab = cb + CH_FT + ((7 - r) & 7) * CH_CS + (255 - (r >> 3) - 2 * (h >> 1) + (h & 1)) * 16 - (64 * 7 + 512 * 7);
  LAS const char* bb = cb + CH_U + 512 + 32 * r - 32 * (h >> 1) + 16 * (h & 1) - 64 * 7;
  u32x4 A[2][8], B[2][8];
#define CONV_LOAD(ar, buf)                                                                                   \
  _Pragma("unroll") for (int k = 0; k < NF; ++k) {                                                           \
    B[buf][k] = *(LAS const u32x4*)(bb + 512 * k + 64 * (7 - (ar)));                                         \
    A[buf][k] = *(LAS const u32x4*)(ab + 512 * (7 - k) + 64 * (7 - (ar)));                                   \
  }
  CONV_LOAD(0, 0);
#pragma unroll
  for (int ar = 0; ar < 8; ++ar) {
    __builtin_amdgcn_sched_barrier(0);
    if (ar < 7) { CONV_LOAD(ar + 1, (ar + 1) & 1); }
#pragma unroll
    for (int al = 0; al < NF; ++al)
#pragma unroll
      for (int be = 0; be < NF; ++be) {
        const int tau = al + be;
        if (HW ? (tau >= 4 && tau <= 6) : (tau <= 3 || tau == 7)) acc[tau] = mfma_h(A[ar & 1][al], B[ar & 1][be], acc[tau]);
      }
#pragma unroll
    for (int i = 0; i < 16; ++i) { __builtin_amdgcn_sched_group_barrier(0x008, 1, 0); __builtin_amdgcn_sched_group_barrier(0x100, 1, 0); }
    __builtin_amdgcn_sched_barrier(0);
  }
#undef CONV_LOAD
  const int q = lane >> 4;
#pragma unroll
  for (int t = 0; t < 8; ++t) {
    if (HW ? (t >= 4 && t <= 6) : (t <= 3 || t == 7)) {
      const f32x4 v = acc[t];
      const int l0 = 256 * t + 16 * r + 4 * q;
      const u32x2 uw = *(LAS const u32x2*)(cb + CH_U + 512 + l0 * 2);
      unsigned o[2];
#pragma unroll
      for (int pp = 0; pp < 2; ++pp) {
        const unsigned uwp = uw[pp];
        const f16x2 ua = __builtin_bit_cast(f16x2, uwp);
        const f32x2 y2 = up_bf(rn_pair((f32x2){v[2 * pp], v[2 * pp + 1]} * iscale)), k2 = up_bf(rn_pair((f32x2){(float)ua[0], (float)ua[1]} * dsk));
        o[pp] = rn_pair(y2 + k2);
      }
      st8(wsr, WS_ST + (d * SEQ + l0) * 2, (u32x2){o[0], o[1]});
    }
  }
}

__device__ __forceinline__ void phaseB_s4d(LAS char* smem, LAS char* cb, const Params& p, rsrc_t wsr, unsigned tag, int rep, bool poller, int fz_, int d, int hw, int lane MARKS_ARG) {
  char* ws = p.ws;
  LAS char* cnt = cb + CH_CNT;
  unsigned la_[2], ai_[2], cr_[4], ci_[4], ldt_, dsk_;
  u32x4 pt0, pt1, pt2;
  {
    const unsigned vo = (unsigned)(d * NST + 128 * hw + lane) * 2u, vo2 = (unsigned)(d * NST + ((128 * hw + 128 + lane) & 255)) * 2u, vd = (unsigned)d * 2u;
    asm volatile("global_load_ushort %0, %1, %2" : "=v"(la_[0]) : "v"(vo), "s"(p.logA) : "memory");
    asm volatile("global_load_ushort %0, %1, %2 offset:128" : "=v"(la_[1]) : "v"(vo), "s"(p.logA) : "memory");
    asm volatile("global_load_ushort %0, %1, %2" : "=v"(ai_[0]) : "v"(vo), "s"(p.Aim) : "memory");
    asm volatile("global_load_ushort %0, %1, %2 offset:128" : "=v"(ai_[1]) : "v"(vo), "s"(p.Aim) : "memory");
    asm volatile("global_load_ushort %0, %1, %2" : "=v"(cr_[0]) : "v"(vo), "s"(p.Cre) : "memory");
    asm volatile("global_load_ushort %0, %1, %2 offset:128" : "=v"(cr_[1]) : "v"(vo), "s"(p.Cre) : "memory");
    asm volatile("global_load_ushort %0, %1, %2" : "=v"(cr_[2]) : "v"(vo2), "s"(p.Cre) : "memory");
    asm volatile("global_load_ushort %0, %1, %2 offset:128" : "=v"(cr_[3]) : "v"(vo2), "s"(p.Cre) : "memory");
    asm volatile("global_load_ushort %0, %1, %2" : "=v"(ci_[0]) : "v"(vo), "s"(p.Cim) : "memory");
    asm volatile("global_load_ushort %0, %1, %2 offset:128" : "=v"(ci_[1]) : "v"(vo), "s"(p.Cim) : "memory");
    asm volatile("global_load_ushort %0, %1, %2" : "=v"(ci_[2]) : "v"(vo2), "s"(p.Cim) : "memory");
    asm volatile("global_load_ushort %0, %1, %2 offset:128" : "=v"(ci_[3]) : "v"(vo2), "s"(p.Cim) : "memory");
    asm volatile("global_load_ushort %0, %1, %2" : "=v"(ldt_) : "v"(vd), "s"(p.logdt) : "memory");
    asm volatile("global_load_ushort %0, %1, %2" : "=v"(dsk_) : "v"(vd), "s"(p.Dsk) : "memory");
    const char* pb = (const char*)POSTAB.v + hw * 2048;
    const unsigned vp = lane * 32;
    asm volatile("global_load_dwordx4 %0, %1, %2" : "=v"(pt0) : "v"(vp), "s"(pb) : "memory");
    asm volatile("global_load_dwordx4 %0, %1, %2 offset:16" : "=v"(pt1) : "v"(vp), "s"(pb) : "memory");
    asm volatile("global_load_dwordx4 %0, %1, %2 offset:32" : "=v"(pt2) : "v"(vp), "s"(pb) : "memory");
  }
  BAR();
  if (hw == 0 && lane < 32) *(LAS u32x4*)(cb + CH_U + lane * 16) = (u32x4){0u, 0u, 0u, 0u};
  if (lane < 4) *(LAS u32x4*)(cb + CH_T + hw * CH_TS + 48 * 64 + lane * 16) = (u32x4){0u, 0u, 0u, 0u};
  __builtin_amdgcn_s_setprio(S4D_PRIO);
  asm volatile("s_waitcnt vmcnt(0)" : "+v"(la_[0]), "+v"(la_[1]), "+v"(ai_[0]), "+v"(ai_[1]), "+v"(cr_[0]), "+v"(cr_[1]), "+v"(cr_[2]), "+v"(cr_[3]), "+v"(ci_[0]), "+v"(ci_[1]),
               "+v"(ci_[2]), "+v"(ci_[3]), "+v"(ldt_), "+v"(dsk_), "+v"(pt0), "+v"(pt1), "+v"(pt2)::"memory");
  z1_signal(smem, wsr, tag, fz_, lane);
  STAMP(1, 0);
  float cr[4], ci[4];
#pragma unroll
  for (int k = 0; k < 4; ++k) { cr[k] = bf2f((unsigned short)cr_[k]); ci[k] = bf2f((unsigned short)ci_[k]); }
  const float dt = rbf(__builtin_amdgcn_exp2f(bf2f((unsigned short)ldt_) * 1.4426950408889634f)), dsk = bf2f((unsigned short)dsk_);
  float sabs = 0.f;
#pragma unroll
  for (int k = 0; k < 4; ++k) sabs += __builtin_fabsf(cr[k]) + __builtin_fabsf(ci[k]);
  sabs = xsum16_32(red16(sabs));
  sabs *= 2.0f * dt;
  int ex = (int)((__float_as_uint(sabs) >> 23) & 0xff) - 126;
  ex = ex < -100 ? -100 : (ex > 100 ? 100 : ex);
  const float scale = __uint_as_float((unsigned)(127 + 15 - ex) << 23), iscale = __uint_as_float((unsigned)(127 - 15 + ex) << 23);
  f32x4 kacc[3] = {};
  LAS char* fw = cb + CH_FT + hw * CH_FW;
#pragma unroll
  for (int ps = 0; ps < 2; ++ps) {
    const float lap = bf2f((unsigned short)la_[ps]), aip = bf2f((unsigned short)ai_[ps]);
    const float crp = cr[ps], cip = ci[ps];
    const float lre = -rbf(__builtin_amdgcn_exp2f(lap * 1.4426950408889634f)) * dt, lim = aip * dt;
    const float al = lre * 1.4426950408889634f, be = lim * 0.15915494309189535f;
    LAS char* wp = fw + lane * 4;
#define PUTH(row, v) *(LAS unsigned*)(wp + (row) * CH_FS) = (v)
    {
      const f32x2 w = cexp_p(al, be, 16.f);
      const float cs = 2.0f * dt * scale;
      const f32x2 c0 = {crp * cs, cip * cs};
      const f32x2 w2 = cmul(w, w), c1 = cmul(c0, w), w4 = cmul(w2, w2), c2 = cmul(c0, w2), c3 = cmul(c1, w2), w8 = cmul(w4, w4), w12 = cmul(w8, w4);
      const unsigned hc[4] = {hcv(c0), hcv(c1), hcv(c2), hcv(c3)}, hw4[3] = {hcv(w4), hcv(w8), hcv(w12)};
#pragma unroll
      for (int j = 0; j < 4; ++j) {
        PUTH(j, hc[j]);
#pragma unroll
        for (int k = 0; k < 3; ++k) PUTH(4 * (k + 1) + j, hmul(hc[j], hw4[k]));
      }
    }
    {
      const f32x2 e1 = cexp_p(al, -be, 1.f);
      const f32x2 e2 = cmul(e1, e1), e3 = cmul(e2, e1), e4 = cmul(e2, e2), e8 = cmul(e4, e4), e12 = cmul(e8, e4);
      unsigned E[16];
      E[0] = 0x00003c00u; E[1] = hcv(e1); E[2] = hcv(e2); E[3] = hcv(e3); E[4] = hcv(e4); E[8] = hcv(e8); E[12] = hcv(e12);
#pragma unroll
      for (int m = 1; m < 4; ++m)
#pragma unroll
        for (int j = 1; j < 4; ++j) E[4 * m + j] = hmul(E[4 * m], E[j]);
#pragma unroll
      for (int k = 0; k < 16; ++k) PUTH(16 + k, E[k]);
      const f32x2 z256 = cexp_p(al, -be, 256.f), z512 = cexp_p(al, -be, 512.f), z1024 = cexp_p(al, -be, 1024.f), z2048 = cexp_p(al, -be, 2048.f);
      const f32x2 z768 = cmul(z512, z256);
      const unsigned h256 = hcv(z256), h512 = hcv(z512), h768 = hcv(z768);
      PUTH(32, h256);
#pragma unroll
      for (int k = 1; k < 8; ++k) PUTH(32 + k, hmul(h256, E[2 * k]));
      PUTH(40, h512); PUTH(44, h768);
#pragma unroll
      for (int k = 1; k < 4; ++k) { PUTH(40 + k, hmul(h512, E[4 * k])); PUTH(44 + k, hmul(h768, E[4 * k])); }
      const unsigned hb[4] = {hcv(z1024), hcv(cmul(z1024, z256)), hcv(cmul(z1024, z512)), hcv(cmul(z1024, z768))};
#pragma unroll
      for (int k = 0; k < 4; ++k) { PUTH(48 + 2 * k, hb[k]); PUTH(49 + 2 * k, hmul(hb[k], E[8])); }
      PUTH(56, hcv(z2048));
    }
#undef PUTH
    asm volatile("s_waitcnt lgkmcnt(0)" ::: "memory");
    LAS const char* rp = fw + (lane & 15) * CH_FS + (lane >> 4) * 16;
    u32x4 fa[4], fb[4][3];
#pragma unroll
    for (int kk = 0; kk < 4; ++kk) {
      fa[kk] = *(LAS const u32x4*)(rp + kk * 64);
#pragma unroll
      for (int t = 0; t < 3; ++t) fb[kk][t] = *(LAS const u32x4*)(rp + (16 + 16 * t) * CH_FS + kk * 64);
    }
#pragma unroll
    for (int kk = 0; kk < 4; ++kk)
#pragma unroll
      for (int t = 0; t < 3; ++t) kacc[t] = mfma_h(fa[kk], fb[kk][t], kacc[t]);
    asm volatile("" ::: "memory");
  }
  STAMP(1, 1);
  u32x4 u0, u1;
  group_wait(poller, wsr, WS_FA + rep * 1024 + 7 * 128 + (lane & 31) * 4, tag, smem + B_CH + CH_CNT + 76);
  {
    const unsigned vu = WS_XT + d * SEQ * 2 + hw * 2048 + lane * 16;
    asm volatile("buffer_load_dwordx4 %0, %1, %2, 0 offen sc1" : "=v"(u0) : "v"(vu), "s"(wsr) : "memory");
    asm volatile("buffer_load_dwordx4 %0, %1, %2, 0 offen offset:1024 sc1" : "=v"(u1) : "v"(vu), "s"(wsr) : "memory");
  }
#pragma unroll
  for (int t = 0; t < 3; ++t) *(LAS f32x4*)(cb + CH_T + hw * CH_TS + (16 * t + (lane & 15)) * 64 + (lane >> 4) * 16) = kacc[t];
  lds_sync(cnt, 2, lane);
  {
    const int y = 2 * (64 * hw + lane);
    const unsigned po[12] = {pt0[0], pt0[1], pt0[2], pt0[3], pt1[0], pt1[1], pt1[2], pt1[3], pt2[0], pt2[1], pt2[2], pt2[3]};
    unsigned W[12], V[11];
#pragma unroll
    for (int k = 0; k < 12; ++k) {
      LAS const char* a0 = cb + CH_T + (po[k] & 0xffffu);
      LAS const char* a1 = cb + CH_T + (po[k] >> 16);
      W[k] = pk_f16(*(LAS const float*)a0 + *(LAS const float*)(a0 + CH_TS), *(LAS const float*)a1 + *(LAS const float*)(a1 + CH_TS));
    }
#pragma unroll
    for (int i = 0; i < 11; ++i) V[i] = __builtin_amdgcn_alignbit(W[i + 1], W[i], 16);
#pragma unroll
    for (int m = 0; m < 4; ++m) {
      *(LAS u32x4*)(cb + CH_FT + (2 * m) * CH_CS + y * 16) = (u32x4){W[m], W[m + 1], W[m + 2], W[m + 3]};
      *(LAS u32x4*)(cb + CH_FT + (2 * m) * CH_CS + y * 16 + 16) = (u32x4){W[m + 4], W[m + 5], W[m + 6], W[m + 7]};
      *(LAS u32x4*)(cb + CH_FT + (2 * m + 1) * CH_CS + y * 16) = (u32x4){V[m], V[m + 1], V[m + 2], V[m + 3]};
      *(LAS u32x4*)(cb + CH_FT + (2 * m + 1) * CH_CS + y * 16 + 16) = (u32x4){V[m + 4], V[m + 5], V[m + 6], V[m + 7]};
    }
    if (hw == 1 && lane < 8) *(LAS u32x4*)(cb + CH_FT + lane * CH_CS + 256 * 16) = (u32x4){0u, 0u, 0u, 0u};
  }
  asm volatile("s_waitcnt vmcnt(0)" : "+v"(u0), "+v"(u1)::"memory");
  *(LAS u32x4*)(cb + CH_U + 512 + hw * 2048 + lane * 16) = u0;
  *(LAS u32x4*)(cb + CH_U + 512 + hw * 2048 + 1024 + lane * 16) = u1;
  lds_sync(cnt, 4, lane);
  __builtin_amdgcn_s_setprio(CONV_PRIO);
  STAMP(1, 2);
  if (hw) s4d_conv<1>(cb, wsr, d, lane, iscale, dsk); else s4d_conv<0>(cb, wsr, d, lane, iscale, dsk);
  __builtin_amdgcn_s_setprio(0);
  WAIT_VM(0);
  {
    unsigned old = 0;
    if (lane == 0) old = __hip_atomic_fetch_add((LAS unsigned*)(smem + B_CH + CH_CNT + 84), 1u, __ATOMIC_RELAXED, __HIP_MEMORY_SCOPE_WORKGROUP);
    if (__builtin_amdgcn_readfirstlane(old) == 3u && lane < 8) st4(wsr, WS_FS + lane * 1024 + (d >> 1) * 4, tag);
  }
  STAMP(1, 3);
}

constexpr int C_PART = 0;
constexpr int C_XS = 65536;
constexpr int C_SS = C_XS + 64 * 136;
constexpr int C_OS = C_SS + 64 * 136;

constexpr int C_GO = C_OS + 64 * 144;
__device__ __forceinline__ void phaseC(LAS char* smem, const Params& p, rsrc_t wsr, unsigned tag, int rep, int T, int Dt, int tid MARKS_ARG) {
  char* ws = p.ws;
  const int lane = tid & 63;
  const int wave = __builtin_amdgcn_readfirstlane(tid >> 6);
  unsigned fo;
  {
    const int J = lane & 7, bh = 2 * (T >> 3) + (J >> 2) + 8 * ((T & 7) + 8 * (J & 3));
    const int bs = 32 * Dt + ((lane - 8) & 31);
    fo = (lane < 8 ? WS_FB + bh * 4 : WS_FS + bs * 4) + rep * 1024;
  }
  if (wave < 4) {
    const unsigned ho = WS_HF + ((4 * T * 28 + 7 * wave) * 64 + lane) * 16, wo = WS_WOF + ((4 * Dt * 28 + 7 * wave) * 64 + lane) * 16;
    u32x4 hf[7][4], wf[7][4];
    f32x4 acc[4][4];
#pragma unroll
    for (int k = 0; k < 7; ++k)
#pragma unroll
      for (int m = 0; m < 4; ++m) wf[k][m] = ld16(wsr, wo + (m * 28 + k) * 1024);
    __builtin_amdgcn_sched_barrier(0);
    group_wait(false, wsr, fo, tag, smem + C_GO);
    STAMP(2, 0);
#pragma unroll
    for (int n = 0; n < 4; ++n)
#pragma unroll
      for (int m = 0; m < 4; ++m) acc[n][m] = (f32x4){0.f, 0.f, 0.f, 0.f};
#pragma unroll
    for (int k = 0; k < 4; ++k)
#pragma unroll
      for (int m = 0; m < 4; ++m) hf[k][m] = ld16(wsr, ho + (m * 28 + k) * 1024);
    __builtin_amdgcn_sched_barrier(0);
#pragma unroll
    for (int k = 0; k < 7; ++k) {
#pragma unroll
      for (int n = 0; n < 4; ++n)
#pragma unroll
        for (int m = 0; m < 4; ++m) acc[n][m] = mfma_bf(__builtin_bit_cast(s16x8, wf[k][n]), __builtin_bit_cast(s16x8, hf[k][m]), acc[n][m]);
      __builtin_amdgcn_sched_barrier(0);
      if (k + 4 < 7) {
#pragma unroll
        for (int m = 0; m < 4; ++m) hf[k + 4][m] = ld16(wsr, ho + (m * 28 + k + 4) * 1024);
      }
      __builtin_amdgcn_sched_barrier(0);
    }
    STAMP(2, 1);
#pragma unroll
    for (int n = 0; n < 4; ++n)
#pragma unroll
      for (int m = 0; m < 4; ++m) *(LAS f32x4*)(smem + C_PART + ((wave * 4 + m) * 4 + n) * 1024 + lane * 16) = acc[n][m];
  } else {
    const int u = tid - 256, row = u >> 2, c = u & 3;
    const u32x4 x0 = *(const u32x4*)(p.x + (size_t)(64 * T + row) * DM + 64 * Dt + 16 * c);
    const u32x4 x1 = *(const u32x4*)(p.x + (size_t)(64 * T + row) * DM + 64 * Dt + 16 * c + 8);
#pragma unroll
    for (int e = 0; e < 2; ++e) {
      *(LAS u32x2*)(smem + C_XS + row * 136 + c * 32 + e * 8) = (u32x2){x0[2 * e], x0[2 * e + 1]};
      *(LAS u32x2*)(smem + C_XS + row * 136 + c * 32 + 16 + e * 8) = (u32x2){x1[2 * e], x1[2 * e + 1]};
    }
    group_wait(wave == 4, wsr, fo, tag, smem + C_GO);
    const unsigned so = WS_ST + ((64 * Dt + row) * SEQ + 64 * T + 16 * c) * 2;
    const u32x4 s0 = ld16(wsr, so), s1 = ld16(wsr, so + 16);
#pragma unroll
    for (int e = 0; e < 2; ++e) {
      *(LAS u32x2*)(smem + C_SS + row * 136 + c * 32 + e * 8) = (u32x2){s0[2 * e], s0[2 * e + 1]};
      *(LAS u32x2*)(smem + C_SS + row * 136 + c * 32 + 16 + e * 8) = (u32x2){s1[2 * e], s1[2 * e + 1]};
    }
  }
  BAR();
  {
    const int m = wave & 3, l15 = lane & 15, q = lane >> 4, t = 16 * m + l15;
#pragma unroll
    for (int nn = 0; nn < 2; ++nn) {
      const int n = 2 * (wave >> 2) + nn;
      f32x4 o = *(LAS const f32x4*)(smem + C_PART + ((0 * 4 + m) * 4 + n) * 1024 + lane * 16);
#pragma unroll
      for (int sw = 1; sw < 4; ++sw) o += *(LAS const f32x4*)(smem + C_PART + ((sw * 4 + m) * 4 + n) * 1024 + lane * 16);
      const s16x4 sv = tr_read(smem + C_SS + (16 * n + 4 * q + (l15 >> 2)) * 136 + (16 * m + 4 * (lane & 3)) * 2);
      const u32x2 sw2 = __builtin_bit_cast(u32x2, sv);
      const u32x2 xw = *(LAS const u32x2*)(smem + C_XS + t * 136 + (16 * n + 4 * q) * 2);
      unsigned ow[2];
#pragma unroll
      for (int pp = 0; pp < 2; ++pp) {
        const f32x2 v = up_bf(rn_pair((f32x2){o[2 * pp], o[2 * pp + 1]}));
        const f32x2 e = v * -1.4426950408889634f;
        const f32x2 dd = (f32x2){__builtin_amdgcn_exp2f(e.x), __builtin_amdgcn_exp2f(e.y)} + 1.0f;
        const f32x2 g = up_bf(rn_pair(v * (f32x2){__builtin_amdgcn_rcpf(dd.x), __builtin_amdgcn_rcpf(dd.y)}));
        const unsigned s_ = sw2[pp], x_ = xw[pp];
        ow[pp] = rn_pair(up_bf(x_) + up_bf(rn_pair(up_bf(s_) * g)));
      }
      *(LAS u32x2*)(smem + C_OS + t * 144 + (16 * n + 4 * q) * 2) = (u32x2){ow[0], ow[1]};
    }
  }
  BAR();
  {
    const int row = tid >> 3, c = tid & 7;
    *(u32x4*)(p.out + (size_t)(64 * T + row) * DM + 64 * Dt + 8 * c) = *(LAS const u32x4*)(smem + C_OS + row * 144 + c * 16);
  }
}

__global__ __launch_bounds__(512, 2) void mixer_block(Params p) {
  extern __shared__ __attribute__((aligned(16))) char smem_raw[];
  LAS char* smem = (LAS char*)smem_raw;
  char* ws = p.ws;
  const int tid = threadIdx.x, lane = tid & 63;
  const int wave = __builtin_amdgcn_readfirstlane(tid >> 6);
  const int b = blockIdx.x, xcd = b & 7, rr = b >> 3;
  const rsrc_t wsr = mk_rsrc(ws, WS_END);
  STAMP_DECL;
  STAMP(0, 0);
  const unsigned tag = *(const __attribute__((address_space(4))) unsigned*)(ws + WS_EP) + 1u;
  const int T2 = 8 * (xcd >> 1) + (rr & 7), J2 = 4 * (xcd & 1) + (rr >> 3);
  const bool aux = b >= 224;
  if (!aux) {
    phaseA_gemm(smem, p, wsr, tag, 4 * xcd + rr / 7, rr % 7, T2, tid MARKS_PASS);
    if (tid < 64) {
      if (lane < 32) *(LAS unsigned*)(smem + B_CH + CH_CNT + lane * 4) = 0u;
      else *(LAS unsigned*)(smem + B_CH + B_CHS + CH_CNT + (lane - 32) * 4) = 0u;
    }
  } else {
    if (tid < 32) *(LAS unsigned*)(smem + B_CH + CH_CNT + tid * 4) = 0u;
    BAR();
    if (wave < 4) {
      phaseA_aux(smem, p, wsr, tag, b - 224, tid);
      const W2Src w2 = w2_src(p, T2, wave, lane);
#pragma unroll
      for (int k = 0; k < 16; ++k) w2_piece(w2, wave, k >> 3, k & 7);
    } else {
#pragma unroll 8
      for (int k = 0; k < 64; ++k) {
        glds16((const char*)p.w_sp + (size_t)(k * 128 + (b - 224) * 4 + (wave - 4)) * 1024, (unsigned)lane * 16u, (unsigned)(AUX_PF + (wave - 4) * 1024));
        if ((k & 7) == 7) WAIT_VM(16);
      }
      if (b == 255 && wave == 4) {
#pragma unroll
        for (int k = 0; k < 4; ++k) poll_flags(wsr, WS_FA + 7 * 1024 + (64 * k + lane) * 4, tag);
        if (lane == 0) st4(wsr, WS_EP, tag);
      }
    }
  }
  STAMP(0, 6);
  if (wave < 4) {
    if (!aux) BAR();
    phaseB_gemm(smem, p, wsr, tag, xcd, aux ? -1 : (rr % 7) * 32 + 4 * xcd + rr / 7, wave, lane, T2, J2 MARKS_PASS);
  } else if (!aux) {
    const int sw = wave - 4;
    phaseB_s4d(smem, smem + B_CH + (sw >> 1) * B_CHS, p, wsr, tag, xcd, sw == 0, (rr % 7) * 32 + 4 * xcd + rr / 7, 2 * b + (sw >> 1), sw & 1, lane MARKS_PASS);
  }
  WAIT_VM(0);
  BAR();
  if (tid < 8) st4(wsr, WS_FB + tid * 1024 + b * 4, tag);
  STAMP(1, 4);
  if (!aux) {
    if (tid == 64) *(LAS unsigned*)(smem + C_GO) = 0u;
    BAR();
    phaseC(smem, p, wsr, tag, xcd, 4 * xcd + rr / 7, rr % 7, tid MARKS_PASS);
    STAMP(2, 2);
  }
  STAMP_DUMP();
}

extern "C" void kernel_launch(void* const* d_in, const int* in_sizes, int n_in, void* d_out, int out_size, void* d_ws, size_t ws_size, hipStream_t stream) {
  Params p;
  p.x = (const unsigned short*)d_in[0];
  p.logA = (const unsigned short*)d_in[1];
  p.Aim = (const unsigned short*)d_in[2];
  p.Cre = (const unsigned short*)d_in[3];
  p.Cim = (const unsigned short*)d_in[4];
  p.logdt = (const unsigned short*)d_in[5];
  p.Dsk = (const unsigned short*)d_in[6];
  p.w_in = (const unsigned short*)d_in[8];
  p.w_sp = (const unsigned short*)d_in[9];
  p.b_sp = (const unsigned short*)d_in[10];
  p.w_out = (const unsigned short*)d_in[11];
  p.ws = (char*)d_ws;
  p.out = (unsigned short*)d_out;
  (void)hipFuncSetAttribute((const void*)mixer_block, hipFuncAttributeMaxDynamicSharedMemorySize, 163840);
  mixer_block<<<256, 512, 163840, stream>>>(p);
}
```

```cpp
#include <hip/hip_runtime.h>
#include <hip/hip_bf16.h>
#include <stdint.h>

#define LAS __attribute__((address_space(3)))
#define GAS __attribute__((address_space(1)))
typedef float f32x2 __attribute__((ext_vector_type(2)));
typedef float f32x4 __attribute__((ext_vector_type(4)));
typedef float f32x16 __attribute__((ext_vector_type(16)));
typedef unsigned u32x2 __attribute__((ext_vector_type(2)));
typedef unsigned u32x4 __attribute__((ext_vector_type(4)));
typedef short s16x4 __attribute__((ext_vector_type(4)));
typedef short s16x8 __attribute__((ext_vector_type(8)));
typedef __bf16 bf16x2 __attribute__((ext_vector_type(2)));
typedef __bf16 bf16x8 __attribute__((ext_vector_type(8)));
typedef _Float16 f16x2 __attribute__((ext_vector_type(2)));
typedef _Float16 f16x8 __attribute__((ext_vector_type(8)));

#define S4D_PRIO 3
#define GEMM_PRIO 1
#define CONV_PRIO 0
#define EPI_PRIO 3
constexpr int SEQ = 2048, DM = 448, FH = 896, NST = 256;

constexpr unsigned WS_Z1D = 0;
constexpr unsigned WS_Z2F = WS_Z1D + SEQ * FH * 2;
constexpr unsigned WS_HF  = WS_Z2F + SEQ * FH * 2;
constexpr unsigned WS_XT  = WS_HF + SEQ * FH * 2;
constexpr unsigned WS_ST  = WS_XT + SEQ * DM * 2;
constexpr unsigned WS_WOF = WS_ST + SEQ * DM * 2;
constexpr unsigned WS_XCH = WS_WOF + FH * DM * 2;
constexpr unsigned WS_XS1 = WS_XCH + 32 * 64 * 8 * 16;
constexpr unsigned WS_FA  = WS_XS1 + 32 * 1024;
constexpr unsigned WS_FZ  = WS_FA + 8192;
constexpr unsigned WS_FB  = WS_FZ + 1024;
constexpr unsigned WS_FS  = WS_FB + 8192;
constexpr unsigned WS_EP  = WS_FS + 8192;
constexpr unsigned WS_END = WS_EP + 256;

constexpr size_t WS_DBG = 32u << 20;
#define STAMP_DECL do {} while (0)
#define STAMP(kern, slot) do {} while (0)
#define STAMP_DUMP() do {} while (0)
#define MARKS_ARG
#define MARKS_PASS

constexpr unsigned ONES_BF = 0x3f803f80u;
constexpr int AUX_SC1 = 16;
__device__ __forceinline__ float bf2f(unsigned short h) { return __uint_as_float((unsigned)h << 16); }
__device__ __forceinline__ float bflo(unsigned w) { return __uint_as_float(w << 16); }
__device__ __forceinline__ float bfhi(unsigned w) { return __uint_as_float(w & 0xffff0000u); }
__device__ __forceinline__ float rbf(float x) { return (float)(__bf16)x; }
__device__ __forceinline__ unsigned pk_f16(float lo, float hi) {
  f16x2 v = {(_Float16)lo, (_Float16)hi};
  return __builtin_bit_cast(unsigned, v);
}
__device__ __forceinline__ f32x2 up_bf(unsigned w) { return (f32x2){bflo(w), bfhi(w)}; }
__device__ __forceinline__ unsigned rn_pair(f32x2 v) {
  unsigned r;
  asm("v_cvt_pk_bf16_f32 %0, %1, %2" : "=v"(r) : "v"(v.x), "v"(v.y));
  return r;
}
template <int CTRL> __device__ __forceinline__ float dpp_add(float v) {
  int t = __builtin_amdgcn_update_dpp(0, __float_as_int(v), CTRL, 0xf, 0xf, true);
  return v + __int_as_float(t);
}
__device__ __forceinline__ float red8(float v) {
  v = dpp_add<0xB1>(v); v = dpp_add<0x4E>(v); v = dpp_add<0x141>(v);
  return v;
}
__device__ __forceinline__ float red16(float v) { return dpp_add<0x140>(red8(v)); }
__device__ __forceinline__ float xsum32(float v) {
  auto b = __builtin_amdgcn_permlane32_swap(__float_as_uint(v), __float_as_uint(v), false, false);
  return __uint_as_float(b[0]) + __uint_as_float(b[1]);
}
__device__ __forceinline__ float xsum16_32(float v) {
  auto a = __builtin_amdgcn_permlane16_swap(__float_as_uint(v), __float_as_uint(v), false, false);
  return xsum32(__uint_as_float(a[0]) + __uint_as_float(a[1]));
}
__device__ __forceinline__ void glds16(const void* sbase, unsigned voff, unsigned lds) {
  asm volatile("s_mov_b32 m0, %2\n\ts_nop 0\n\tglobal_load_lds_dwordx4 %0, %1" ::"v"(voff), "s"(sbase), "s"(lds) : "memory");
}
#define WAIT_VM(n) asm volatile("s_waitcnt vmcnt(%0)" ::"n"(n) : "memory")
#define BAR() do { asm volatile("s_waitcnt lgkmcnt(0)" ::: "memory"); __builtin_amdgcn_s_barrier(); asm volatile("" ::: "memory"); } while (0)
__device__ __forceinline__ s16x4 tr_read(LAS const char* p) {
  typedef short v4i16 __attribute__((ext_vector_type(4)));
  return __builtin_amdgcn_ds_read_tr16_b64_v4i16((LAS v4i16*)p);
}
__device__ __forceinline__ f32x4 mfma_bf(s16x8 a, s16x8 b, f32x4 c) {
  return __builtin_amdgcn_mfma_f32_16x16x32_bf16(__builtin_bit_cast(bf16x8, a), __builtin_bit_cast(bf16x8, b), c, 0, 0, 0);
}
__device__ __forceinline__ void mfma_acc(f32x4& c, u32x4 a, s16x8 b) {
  asm volatile("v_mfma_f32_16x16x32_bf16 %0, %1, %2, %0" : "+v"(c) : "v"(a), "v"(b));
}
#define MFMA_DONE() asm volatile("s_nop 7\n\ts_nop 7" ::: "memory")
__device__ __forceinline__ f32x16 mfma32_bf(s16x8 a, s16x8 b, f32x16 c) {
  return __builtin_amdgcn_mfma_f32_32x32x16_bf16(__builtin_bit_cast(bf16x8, a), __builtin_bit_cast(bf16x8, b), c, 0, 0, 0);
}
__device__ __forceinline__ f32x4 mfma_h(u32x4 a, u32x4 b, f32x4 c) {
  return __builtin_amdgcn_mfma_f32_16x16x32_f16(__builtin_bit_cast(f16x8, a), __builtin_bit_cast(f16x8, b), c, 0, 0, 0);
}
__device__ __forceinline__ float dot2bf(unsigned a, unsigned b, float c) {
  return __builtin_amdgcn_fdot2_f32_bf16(__builtin_bit_cast(bf16x2, a), __builtin_bit_cast(bf16x2, b), c, false);
}
__device__ __forceinline__ __amdgpu_buffer_rsrc_t mk_rsrc(const void* p, unsigned bytes) {
  return __builtin_amdgcn_make_buffer_rsrc((void*)p, (short)0, (int)bytes, 0x00020000);
}
typedef __amdgpu_buffer_rsrc_t rsrc_t;
__device__ __forceinline__ void st16(rsrc_t r, unsigned off, u32x4 v) { __builtin_amdgcn_raw_buffer_store_b128(v, r, off, 0, AUX_SC1); }
__device__ __forceinline__ void st8(rsrc_t r, unsigned off, u32x2 v) { __builtin_amdgcn_raw_buffer_store_b64(v, r, off, 0, AUX_SC1); }
__device__ __forceinline__ void st4(rsrc_t r, unsigned off, unsigned v) { __builtin_amdgcn_raw_buffer_store_b32(v, r, off, 0, AUX_SC1); }
__device__ __forceinline__ u32x4 ld16(rsrc_t r, unsigned off) { return __builtin_amdgcn_raw_buffer_load_b128(r, off, 0, AUX_SC1); }
__device__ __forceinline__ void bglds16(rsrc_t rs, unsigned voff, unsigned soff, unsigned lds) {
  asm volatile("s_mov_b32 m0, %3\n\ts_nop 0\n\tbuffer_load_dwordx4 %0, %1, %2 offen lds" ::"v"(voff), "s"(rs), "s"(soff), "s"(lds) : "memory");
}
__device__ __forceinline__ void bload16(u32x4& dst, rsrc_t rs, unsigned voff, unsigned soff) {
  asm volatile("buffer_load_dwordx4 %0, %1, %2, %3 offen sc1" : "=v"(dst) : "v"(voff), "s"(rs), "s"(soff) : "memory");
}
__device__ __forceinline__ void poll_granule(u32x4& gr, rsrc_t rs, unsigned voff, unsigned tag) {
  for (unsigned spins = 0;; ++spins) {
    const bool ok = (gr[1] == tag) && (gr[3] == tag);
    if (__all(ok) || spins > (1u << 20)) break;
    __builtin_amdgcn_s_sleep(1);
    asm volatile("buffer_load_dwordx4 %0, %1, %2, 0 offen sc1\n\ts_waitcnt vmcnt(0)" : "=&v"(gr) : "v"(voff), "s"(rs) : "memory");
  }
}
__device__ __forceinline__ void poll_flags(rsrc_t rs, unsigned voff, unsigned tag) {
  for (unsigned spins = 0;; ++spins) {
    unsigned f;
    asm volatile("buffer_load_dword %0, %1, %2, 0 offen sc1\n\ts_waitcnt vmcnt(0)" : "=&v"(f) : "v"(voff), "s"(rs) : "memory");
    if (__all(f == tag) || spins > (1u << 20)) break;
    __builtin_amdgcn_s_sleep(4);
  }
}
__device__ __forceinline__ void group_wait(bool poller, rsrc_t rs, unsigned voff, unsigned tag, LAS char* word) {
  if (poller) {
    poll_flags(rs, voff, tag);
    *(volatile LAS unsigned*)word = 1u;
  } else {
    while ((unsigned)__builtin_amdgcn_readfirstlane(*(volatile LAS unsigned*)word) == 0u) __builtin_amdgcn_s_sleep(2);
  }
  asm volatile("" ::: "memory");
}
__device__ __forceinline__ void lds_sync(LAS char* cntp, unsigned target, int lane) {
  LAS unsigned* cnt = (LAS unsigned*)cntp;
  asm volatile("s_waitcnt lgkmcnt(0)" ::: "memory");
  if (lane == 0) __hip_atomic_fetch_add(cnt, 1u, __ATOMIC_RELAXED, __HIP_MEMORY_SCOPE_WORKGROUP);
  while ((unsigned)__builtin_amdgcn_readfirstlane(*(volatile LAS unsigned*)cnt) < target) __builtin_amdgcn_s_sleep(1);
  asm volatile("" ::: "memory");
}
template <int V> struct IC { static constexpr int value = V; };
template <int A, int B, class F> __device__ __forceinline__ void static_for(F&& f) {
  if constexpr (A < B) { f(IC<A>{}); static_for<A + 1, B>(f); }
}
__device__ __forceinline__ f32x2 gelu2(f32x2 x) {
  const float c1 = -2.0f * 0.7978845608028654f * 1.4426950408889634f, c2 = c1 * 0.044715f;
  const f32x2 t = x * x;
  const f32x2 a = x * (t * c2 + c1);
  f32x2 d = {__builtin_amdgcn_exp2f(a.x), __builtin_amdgcn_exp2f(a.y)};
  d = d + 1.0f;
  return x * (f32x2){__builtin_amdgcn_rcpf(d.x), __builtin_amdgcn_rcpf(d.y)};
}
__device__ __forceinline__ void ln_scalars(float s, float q, float invn, float& mb, float& rr) {
  const float mean = s * invn;
  float var = q * invn - mean * mean; var = var < 0.f ? 0.f : var;
  mb = rbf(mean);
  rr = __builtin_amdgcn_rcpf(rbf(__builtin_amdgcn_sqrtf(rbf(rbf(var) + 1e-5f))));
}

struct Params {
  const unsigned short *x, *logA, *Aim, *Cre, *Cim, *logdt, *Dsk, *w_in, *w_sp, *b_sp, *w_out;
  char* ws;
  unsigned short* out;
};
#define TLBWARM 1

constexpr int A_X = 0;
constexpr int A_S = 57344;
constexpr int A_P = A_S;
constexpr int A_W1 = A_S + 2048;
constexpr int A_W = 65536;
constexpr int A_TB = A_W;
constexpr int A_M2 = A_W + 32768;
constexpr int A_R2 = A_M2 + 256;
constexpr int A_W1B = A_R2 + 256;
constexpr int A_SLOT = 16384;
constexpr int B_RING = 0;

struct W2Src { rsrc_t wr; unsigned voff0, voff1; };
__device__ __forceinline__ W2Src w2_src(const Params& p, int T, int wave, int lane) {
  W2Src s;
  s.wr = mk_rsrc((const char*)p.w_sp + (size_t)T * 64 * 4096 + wave * 1024, 64 * 4096 - wave * 1024);
  s.voff0 = (lane >> 3) * 4096 + (((lane & 7) ^ ((lane >> 4) & 3)) * 16);
  s.voff1 = (8 + (lane >> 3)) * 4096 + (((lane & 7) ^ (4 + ((lane >> 4) & 3))) * 16);
  return s;
}
__device__ __forceinline__ void w2_piece(const W2Src& s, int wave, int pair, int piece) {
  bglds16(s.wr, (piece & 1) ? s.voff1 : s.voff0, (unsigned)((piece >> 1) * 65536 + pair * 128), (unsigned)(B_RING + wave * 16384 + (pair & 1) * 8192 + piece * 1024));
}

constexpr int AUX_T = 0;
constexpr int AUX_CNT = B_RING + 65536 + 44160 + 80;
constexpr int AUX_PF = 131072;
__device__ __forceinline__ void phaseA_aux(LAS char* smem, const Params& p, rsrc_t wsr, unsigned tag, int v, int tid) {
  char* ws = p.ws;
  const int lane = tid & 63;
  const int wave = __builtin_amdgcn_readfirstlane(tid >> 6);
  const int row = tid >> 2, c4 = tid & 3;
  u32x4 xv[14];
#pragma unroll
  for (int m = 0; m < 14; ++m) xv[m] = *(const u32x4*)(p.x + (size_t)(64 * v + row) * DM + (c4 + 4 * m) * 8);
  unsigned touch[2];
#pragma unroll
  for (int k = 0; k < 2; ++k) {
    const int i = tid + 256 * k;
    const char* a;
    if (i < 256) a = ws + (size_t)(i < (int)(WS_END >> 16) ? i : 0) * 65536;
    else if (i < 384) a = (const char*)p.w_sp + (size_t)(i - 256) * 65536;
    else if (i < 416) a = (const char*)p.w_in + (size_t)(i - 384 < 24 ? i - 384 : 0) * 65536;
    else if (i < 432) a = (const char*)p.w_out + (size_t)(i - 416 < 12 ? i - 416 : 0) * 65536;
    else if (i < 448) a = ((i >> 2) & 3) == 0 ? (const char*)p.logA + (i & 3) * 57344 : ((i >> 2) & 3) == 1 ? (const char*)p.Aim + (i & 3) * 57344
                        : ((i >> 2) & 3) == 2 ? (const char*)p.Cre + (i & 3) * 57344 : (const char*)p.Cim + (i & 3) * 57344;
    else if (i < 480) a = (const char*)p.out + (size_t)(i - 448 < 28 ? i - 448 : 0) * 65536;
    else a = (const char*)p.x + (size_t)(i - 480 < 28 ? i - 480 : 0) * 65536;
    asm volatile("global_load_dword %0, %1, off" : "=v"(touch[k]) : "v"(a) : "memory");
  }
  float s = 0.f, q = 0.f;
#pragma unroll
  for (int m = 0; m < 14; ++m)
#pragma unroll
    for (int e = 0; e < 4; ++e) { const unsigned w = xv[m][e]; s = dot2bf(w, ONES_BF, s); q = dot2bf(w, w, q); }
  s = dpp_add<0x4E>(dpp_add<0xB1>(s)); q = dpp_add<0x4E>(dpp_add<0xB1>(q));
  float mb, rr;
  ln_scalars(s, q, 1.0f / 448.0f, mb, rr);
  asm volatile("s_waitcnt vmcnt(0)" : "+v"(touch[0]), "+v"(touch[1])::"memory");
  if (c4 == 0) st16(wsr, WS_XS1 + v * 1024 + row * 16, (u32x4){__float_as_uint(mb), tag, __float_as_uint(rr), tag});
  unsigned short wv[7][8];
#pragma unroll
  for (int k = 0; k < 7; ++k) {
    int fr = v * 4 + wave + 128 * k; fr = fr < 784 ? fr : 783;
    const int dt = fr / 28, fb = fr - dt * 28;
    const int dcol = 16 * dt + (lane & 15), qq = lane >> 4;
#pragma unroll
    for (int jj = 0; jj < 8; ++jj) wv[k][jj] = p.w_out[(size_t)(32 * fb + 16 * (jj >> 2) + 4 * qq + (jj & 3)) * DM + dcol];
  }
  LAS char* tb = smem + AUX_T;
#pragma unroll
  for (int m = 0; m < 14; ++m)
#pragma unroll
    for (int e = 0; e < 4; ++e) {
      const unsigned w = xv[m][e];
      const f32x2 n3 = up_bf(rn_pair(up_bf(rn_pair(up_bf(w) - mb)) * rr));
      const int d = (c4 + 4 * m) * 8 + 2 * e;
      *(LAS _Float16*)(tb + d * 136 + row * 2) = (_Float16)n3.x;
      *(LAS _Float16*)(tb + (d + 1) * 136 + row * 2) = (_Float16)n3.y;
    }
  lds_sync(smem + AUX_CNT, 4, lane);
#pragma unroll
  for (int m = 0; m < 14; ++m) {
    const int d = (tid >> 3) + 32 * m, c8 = tid & 7;
    const u32x2 lo = *(LAS const u32x2*)(tb + d * 136 + c8 * 16), hi = *(LAS const u32x2*)(tb + d * 136 + c8 * 16 + 8);
    st16(wsr, WS_XT + (d * SEQ + 64 * v + 8 * c8) * 2, (u32x4){lo[0], lo[1], hi[0], hi[1]});
  }
#pragma unroll
  for (int k = 0; k < 7; ++k) {
    const int fr = v * 4 + wave + 128 * k;
    if (fr < 784)
      st16(wsr, WS_WOF + (fr * 64 + lane) * 16,
           (u32x4){(unsigned)wv[k][0] | ((unsigned)wv[k][1] << 16), (unsigned)wv[k][2] | ((unsigned)wv[k][3] << 16), (unsigned)wv[k][4] | ((unsigned)wv[k][5] << 16),
                   (unsigned)wv[k][6] | ((unsigned)wv[k][7] << 16)});
  }
  WAIT_VM(0);
  lds_sync(smem + AUX_CNT, 8, lane);
  if (tid < 8) st4(wsr, WS_FA + tid * 1024 + (7 * 32 + v) * 4, tag);
}

__device__ __forceinline__ void phaseA_gemm(LAS char* smem, const Params& p, rsrc_t wsr, unsigned tag, int ti, int tj, int T2, int tid MARKS_ARG) {
  char* ws = p.ws;
  const int lane = tid & 63;
  const int wave = __builtin_amdgcn_readfirstlane(tid >> 6);
  const int rh = wave >> 2, cg = wave & 3, h = lane >> 5, l31 = lane & 31;
  const int xrow = 8 * wave + (lane >> 3);
  const unsigned voffx = (unsigned)(xrow * 896 + (((lane & 7) ^ ((xrow >> 1) & 7)) * 16));
  const char* xsrc = (const char*)p.x + (size_t)ti * 64 * 896;
  unsigned voffw[2];
#pragma unroll
  for (int e = 0; e < 2; ++e) voffw[e] = (unsigned)((8 * wave + 4 * e + (lane >> 4)) * 3584 + (((lane & 15) ^ (4 * (lane >> 4))) * 16) + tj * 256);
#define A_XPIECE(g) glds16(xsrc + (g) * 128, voffx, (unsigned)(A_X + (g) * 8192 + wave * 1024))
#define A_WPIECE(G, e) \
  glds16(p.w_in, voffw[e] + (unsigned)(((G) % 7) * 64 * 3584 + ((G) < 7 ? 1792 : 0)), (unsigned)(A_W + ((G) % 6) * A_SLOT + wave * 2048 + (e) * 1024))
#pragma unroll
  for (int g = 0; g < 6; ++g) { A_XPIECE(g); A_WPIECE(g, 0); A_WPIECE(g, 1); }
  A_XPIECE(6);
  LAS const char* ab[2];
  LAS const char* xb[4];
  {
    const int g4 = lane >> 4, qq = (lane & 15) >> 2, p4 = lane & 3;
    const int lb = (8 * (g4 >> 1) + qq) * 256 + (((4 * cg + 2 * (g4 & 1) + (p4 >> 1)) ^ (4 * qq)) * 16) + (p4 & 1) * 8;
    ab[0] = smem + A_W + lb + rh * 4096;
    ab[1] = smem + A_W + lb + (rh ^ 1) * 4096;
    const int row = 32 * rh + l31, sw = (row >> 1) & 7;
#pragma unroll
    for (int i = 0; i < 4; ++i) xb[i] = smem + A_X + row * 128 + (((2 * (i ^ rh) + h) ^ sw) * 16);
  }
  s16x8 xa[28], wb[8];
#define A_FRAGS(st)                                                                                                    \
  do {                                                                                                                 \
    const int G_ = (st) >> 2, i_ = (st) & 3, so_ = (G_ % 6) * A_SLOT + (i_ >> 1) * 8192;                               \
    if ((st) < 28) xa[(st) % 28] = *(LAS const s16x8*)(xb[i_] + G_ * 8192);                                            \
    wb[(st) & 7] = __builtin_shufflevector(tr_read(ab[i_ & 1] + so_), tr_read(ab[i_ & 1] + so_ + 1024), 0, 1, 2, 3, 4, 5, 6, 7); \
  } while (0)
  f32x16 acc2 = {}, acc1 = {};
  float w1a = 0.f, w1b = 0.f;
  u32x4 g1;
  const unsigned g1o = WS_XS1 + ti * 1024 + (32 * rh + l31) * 16;
  unsigned zp[8] = {};
  float m1 = 0.f, r1 = 0.f, ss = 0.f, sq = 0.f;
  u32x4 gr;
  const int row = tid >> 3, jj = tid & 7;
  const unsigned vo = WS_XCH + ti * 8192 + row * 128 + (jj < 7 ? jj : 0) * 16;
  WAIT_VM(13);
  BAR();
  STAMP(0, 1);
  A_FRAGS(0); A_FRAGS(1); A_FRAGS(2); A_FRAGS(3);
  auto step = [&](auto stc) __attribute__((always_inline)) {
    constexpr int st = decltype(stc)::value;
    constexpr int G = st >> 2, i = st & 3;
    if (i == 0 && G >= 1) {
      if (G == 1) WAIT_VM(10); else if (G == 2) WAIT_VM(9); else if (G == 3) WAIT_VM(8); else if (G == 4) WAIT_VM(7); else if (G <= 6) WAIT_VM(6);
      else if (G == 7) asm volatile("s_waitcnt vmcnt(3) lgkmcnt(0)" : "+v"(g1)::"memory");
      else if (G == 8) WAIT_VM(6);
      else if (G == 9) WAIT_VM(6); else if (G == 10) WAIT_VM(4);
      else if (G == 11) asm volatile("s_waitcnt vmcnt(2) lgkmcnt(0)" ::: "memory");
      else if (G == 12) WAIT_VM(0);
      __builtin_amdgcn_s_barrier();
      asm volatile("" ::: "memory");
      if (G == 7) STAMP(0, 2);
    }
    __builtin_amdgcn_sched_barrier(0);
    if constexpr (st < 28) acc2 = mfma32_bf(wb[st & 7], xa[st % 28], acc2); else acc1 = mfma32_bf(wb[st & 7], xa[st % 28], acc1);
    if constexpr (st + 4 < 56) A_FRAGS(st + 4);
    if constexpr ((i & 1) == 0) {
      const u32x4 wv = __builtin_bit_cast(u32x4, wb[st & 7]);
#pragma unroll
      for (int e = 0; e < 4; ++e) { const unsigned ww = wv[e]; if (st < 28) w1a = dot2bf(ww, ONES_BF, w1a); else w1b = dot2bf(ww, ONES_BF, w1b); }
    }
    if constexpr (st == 27) {
      const float w1s = xsum32(w1a);
      if (h == 0) *(LAS float*)(smem + A_W1 + (rh * 128 + 32 * cg + l31) * 4) = w1s;
    }
    if constexpr (st == 28) {
      poll_granule(g1, wsr, g1o, tag);
      m1 = __uint_as_float(g1[0]); r1 = __uint_as_float(g1[2]);
    }
    if constexpr (G >= 7 && G <= 10 && i == 1) {
      constexpr int g = G - 7;
      const f32x4 w1 = *(LAS const f32x4*)(smem + A_W1 + (32 * cg + 8 * g + 4 * h) * 4) + *(LAS const f32x4*)(smem + A_W1 + (128 + 32 * cg + 8 * g + 4 * h) * 4);
#pragma unroll
      for (int pp = 0; pp < 2; ++pp) {
        const f32x2 a2 = {acc2[4 * g + 2 * pp], acc2[4 * g + 2 * pp + 1]}, w2v = {w1[2 * pp], w1[2 * pp + 1]};
        const unsigned zz = rn_pair(gelu2(up_bf(rn_pair((a2 - w2v * m1) * r1))));
        zp[2 * g + pp] = zz;
        ss = dot2bf(zz, ONES_BF, ss); sq = dot2bf(zz, zz, sq);
      }
    }
    if constexpr (st == 43) {
      const float s2 = xsum32(ss), q2 = xsum32(sq);
      if (h == 0) *(LAS f32x2*)(smem + A_P + ((32 * rh + l31) * 4 + cg) * 8) = (f32x2){s2, q2};
    }
    if constexpr (st == 44) {
      if (wave == 0) {
        const f32x4 p0 = *(LAS const f32x4*)(smem + A_P + lane * 32), p1 = *(LAS const f32x4*)(smem + A_P + lane * 32 + 16);
        const float s = (p0[0] + p0[2]) + (p1[0] + p1[2]), q = (p0[1] + p0[3]) + (p1[1] + p1[3]);
        u32x4 gr = {__float_as_uint(s), tag, __float_as_uint(q), tag};
        const unsigned vo = WS_XCH + ti * 8192 + lane * 128 + tj * 16;
        asm volatile("buffer_store_dwordx4 %0, %1, %2, 0 offen sc1\n\ts_nop 1" ::"v"(gr), "v"(vo), "s"(wsr) : "memory");
      }
    }
    __builtin_amdgcn_sched_barrier(0);
    if constexpr ((i & 1) == 0) {
      constexpr int ks = st >> 1;
      if constexpr (ks >= 2 && ks < 18) A_WPIECE(5 + (ks >> 1), ks & 1);
    }
    if constexpr (st == 20) asm volatile("buffer_load_dwordx4 %0, %1, %2, 0 offen sc1" : "=v"(g1) : "v"(g1o), "s"(wsr) : "memory");
    if constexpr (st == 52) asm volatile("buffer_load_dwordx4 %0, %1, %2, 0 offen sc1" : "=v"(gr) : "v"(vo), "s"(wsr) : "memory");
    __builtin_amdgcn_sched_barrier(0);
  };
  static_for<0, 56>(step);
#undef A_FRAGS
#undef A_XPIECE
#undef A_WPIECE
  STAMP(0, 3);
  {
    const float w1s = xsum32(w1b);
    if (h == 0) *(LAS float*)(smem + A_W1B + (rh * 128 + 32 * cg + l31) * 4) = w1s;
  }
  asm volatile("s_waitcnt vmcnt(0)" : "+v"(gr)::"memory");
  poll_granule(gr, wsr, vo, tag);
  {
    const float s = red8(jj < 7 ? __uint_as_float(gr[0]) : 0.f), q = red8(jj < 7 ? __uint_as_float(gr[2]) : 0.f);
    float mb, rr2;
    ln_scalars(s, q, 1.0f / 896.0f, mb, rr2);
    if (jj == 0) { *(LAS float*)(smem + A_M2 + row * 4) = mb; *(LAS float*)(smem + A_R2 + row * 4) = rr2; }
  }
  BAR();
  {
    LAS char* tb = smem + A_TB + wave * 2304;
    const int srow = 32 * rh + l31;
    const float mb2 = *(LAS const float*)(smem + A_M2 + srow * 4), rr2 = *(LAS const float*)(smem + A_R2 + srow * 4);
#pragma unroll
    for (int g = 0; g < 4; ++g) {
      unsigned o[2];
#pragma unroll
      for (int pp = 0; pp < 2; ++pp) o[pp] = rn_pair(up_bf(rn_pair(up_bf(zp[2 * g + pp]) - mb2)) * rr2);
      *(LAS u32x2*)(tb + l31 * 72 + (8 * g + 4 * h) * 2) = (u32x2){o[0], o[1]};
    }
    asm volatile("s_waitcnt lgkmcnt(0)" ::: "memory");
    const int q4 = lane >> 4, l15 = lane & 15;
#pragma unroll
    for (int n = 0; n < 2; ++n) {
      LAS const char* rp = tb + (8 * q4 + (l15 >> 2)) * 72 + n * 32 + (lane & 3) * 8;
      const s16x4 lo = tr_read(rp), hi = tr_read(rp + 4 * 72);
      const s16x8 o = __builtin_shufflevector(lo, hi, 0, 1, 2, 3, 4, 5, 6, 7);
      const int ft = 8 * tj + 2 * cg + n, sb = 2 * ti + rh;
      st16(wsr, WS_Z2F + ((ft * 64 + sb) * 64 + lane) * 16, __builtin_bit_cast(u32x4, o));
    }
  }
  STAMP(0, 4);
  {
    const int tt = 4 * ti + 2 * rh + (l31 >> 4);
    unsigned o[4][2];
    auto part = [&](auto gc) __attribute__((always_inline)) {
      constexpr int g = decltype(gc)::value;
      const f32x4 w1 = *(LAS const f32x4*)(smem + A_W1B + (32 * cg + 8 * g + 4 * h) * 4) + *(LAS const f32x4*)(smem + A_W1B + (128 + 32 * cg + 8 * g + 4 * h) * 4);
#pragma unroll
      for (int pp = 0; pp < 2; ++pp) {
        const f32x2 a2 = {acc1[4 * g + 2 * pp], acc1[4 * g + 2 * pp + 1]}, w2v = {w1[2 * pp], w1[2 * pp + 1]};
        o[g][pp] = rn_pair(gelu2(up_bf(rn_pair((a2 - w2v * m1) * r1))));
      }
    };
    static_for<0, 2>(part);
    __builtin_amdgcn_sched_barrier(0);
    WAIT_VM(0);
    BAR();
    if (tid < 8) st4(wsr, WS_FA + tid * 1024 + (tj * 32 + ti) * 4, tag);
    STAMP(0, 5);
    if (wave < 4) {
      const W2Src w2 = w2_src(p, T2, wave, lane);
#pragma unroll
      for (int k = 0; k < 16; ++k) w2_piece(w2, wave, k >> 3, k & 7);
    }
    static_for<2, 4>(part);
#pragma unroll
    for (int g = 0; g < 4; ++g) {
      const int ft = 8 * tj + 2 * cg + (g >> 1), lp = (lane & 15) + 16 * (2 * (g & 1) + h);
      st8(wsr, WS_Z1D + ((tt * 56 + ft) * 64 + lp) * 8, (u32x2){o[g][0], o[g][1]});
    }
  }
}

constexpr int B_CH = 65536;
constexpr int B_CHS = 49152;
constexpr int CH_FT = 0;
constexpr int CH_FW = 16416, CH_FS = 288;
constexpr int CH_CS = 4160;
constexpr int CH_U = 33280;
constexpr int CH_T = CH_U + 4608;
constexpr int CH_TS = 49 * 64;
constexpr int CH_CNT = CH_T + 2 * CH_TS;

__device__ __forceinline__ void z1_signal(LAS char* smem, rsrc_t wsr, unsigned tag, int fz, int lane) {
  if (fz >= 0) {
    unsigned old = 0;
    if (lane == 0) old = __hip_atomic_fetch_add((LAS unsigned*)(smem + B_CH + CH_CNT + 88), 1u, __ATOMIC_RELAXED, __HIP_MEMORY_SCOPE_WORKGROUP);
    if (__builtin_amdgcn_readfirstlane(old) == 7u && lane == 0) st4(wsr, WS_FZ + fz * 4, tag);
  }
}
template <int W>
__device__ __forceinline__ void b_epi(LAS char* smem, rsrc_t wsr, f32x4 (&acc)[7][4], const u32x2 (&z1v)[7], float bs, int lane, int tt, int J) {
  LAS char* cnt = smem + B_CH + CH_CNT + 64;
  LAS char* mine = smem + B_RING + W * 16384 + lane * 16;
  LAS char* other = smem + B_RING + (W ^ 2) * 16384 + lane * 16;
  LAS char* o2 = smem + B_RING + (W ^ 3) * 16384 + lane * 16;
  constexpr int K0 = W & 2, S0 = K0 ^ 2;
#pragma unroll
  for (int n = 0; n < 7; ++n) { *(LAS f32x4*)(mine + n * 1024) = acc[n][S0]; *(LAS f32x4*)(mine + (7 + n) * 1024) = acc[n][S0 + 1]; }
  lds_sync(cnt, 4, lane);
#pragma unroll
  for (int n = 0; n < 7; ++n) { acc[n][K0] += *(LAS const f32x4*)(other + n * 1024); acc[n][K0 + 1] += *(LAS const f32x4*)(other + (7 + n) * 1024); }
  asm volatile("s_waitcnt lgkmcnt(0)" ::: "memory");
#pragma unroll
  for (int n = 0; n < 7; ++n) *(LAS f32x4*)(other + n * 1024) = acc[n][W ^ 1];
  lds_sync(cnt, 8, lane);
  unsigned hp[7][2];
#pragma unroll
  for (int n = 0; n < 7; ++n) {
    const f32x4 v = acc[n][W] + *(LAS const f32x4*)(o2 + n * 1024);
#pragma unroll
    for (int pp = 0; pp < 2; ++pp) {
      const f32x2 g0 = up_bf(rn_pair((f32x2){v[2 * pp], v[2 * pp + 1]})) + bs;
      const unsigned zw = z1v[n][pp];
      hp[n][pp] = rn_pair(up_bf(rn_pair(g0)) * up_bf(zw));
    }
  }
  const unsigned hb = WS_HF + ((tt * 28 + (7 * J >> 1)) * 64 + lane) * 16;
  if (!(J & 1)) {
#pragma unroll
    for (int pp = 0; pp < 3; ++pp) st16(wsr, hb + pp * 1024, (u32x4){hp[2 * pp][0], hp[2 * pp][1], hp[2 * pp + 1][0], hp[2 * pp + 1][1]});
    st8(wsr, hb + 3 * 1024, (u32x2){hp[6][0], hp[6][1]});
  } else {
    st8(wsr, hb + 8, (u32x2){hp[0][0], hp[0][1]});
#pragma unroll
    for (int pp = 0; pp < 3; ++pp) st16(wsr, hb + (pp + 1) * 1024, (u32x4){hp[2 * pp + 1][0], hp[2 * pp + 1][1], hp[2 * pp + 2][0], hp[2 * pp + 2][1]});
  }
}

__device__ __forceinline__ void phaseB_gemm(LAS char* smem, const Params& p, rsrc_t wsr, unsigned tag, int rep, int fz_, int wave, int lane, int T, int J MARKS_ARG) {
  char* ws = p.ws;
  const rsrc_t zs = mk_rsrc(ws + WS_Z2F + (size_t)(7 * J * 64 + 16 * wave) * 1024, 7 * 65536 - 16 * wave * 1024);
  const W2Src w2 = w2_src(p, T, wave, lane);
  const unsigned voffz = lane * 16;
  const unsigned ring = B_RING + wave * 16384;
  LAS const char* wrd0 = smem + ring + (lane & 15) * 128 + (((lane >> 4) ^ ((lane >> 1) & 7)) * 16);
  LAS const char* wrd1 = smem + ring + (lane & 15) * 128 + (((4 + (lane >> 4)) ^ ((lane >> 1) & 7)) * 16);
  u32x4 z[3][7];
  f32x4 acc[7][4];
  {
    const int tj0 = (112 * J) >> 7, tj1 = (112 * J + 111) >> 7;
    group_wait(wave == 0, wsr, WS_FA + rep * 1024 + (lane < 32 ? tj0 : tj1) * 128 + (lane & 31) * 4, tag, smem + B_CH + CH_CNT + 72);
  }
  __builtin_amdgcn_s_setprio(GEMM_PRIO);
  STAMP(1, 0);
#define B_ISSUE_Z(it) _Pragma("unroll") for (int n = 0; n < 7; ++n) bload16(z[(it) % 3][n], zs, voffz, n * 65536 + (it) * 1024)
  B_ISSUE_Z(0); B_ISSUE_Z(1); B_ISSUE_Z(2);
#undef B_ISSUE_Z
  const int tt = 4 * T + wave;
  u32x2 z1v[7];
  unsigned bsw, fz;
#define B_WAITZ(N) asm volatile("s_waitcnt vmcnt(" #N ")" : "+v"(zc[0]), "+v"(zc[1]), "+v"(zc[2]), "+v"(zc[3]), "+v"(zc[4]), "+v"(zc[5]), "+v"(zc[6])::"memory")
#pragma unroll
  for (int it = 0; it < 16; ++it) {
    u32x4(&zc)[7] = z[it % 3];
    if (it <= 1) B_WAITZ(14); else if (it <= 13) B_WAITZ(22); else if (it == 14) B_WAITZ(15); else B_WAITZ(8);
    if (it == 0) z1_signal(smem, wsr, tag, fz_, lane);
    s16x8 wf[4];
#pragma unroll
    for (int m = 0; m < 4; ++m) wf[m] = *(LAS const s16x8*)(((it & 1) ? wrd1 : wrd0) + ((it >> 1) & 1) * 8192 + m * 2048);
#pragma unroll
    for (int n = 0; n < 7; ++n) {
#pragma unroll
      for (int m = 0; m < 4; ++m) {
        if (it == 0) acc[n][m] = mfma_bf(__builtin_bit_cast(s16x8, zc[n]), wf[m], (f32x4){0.f, 0.f, 0.f, 0.f});
        else mfma_acc(acc[n][m], zc[n], wf[m]);
      }
      __builtin_amdgcn_sched_barrier(0);
      if (it + 3 < 16) {
        bload16(zc[n], zs, voffz, n * 65536 + (it + 3) * 1024);
        if ((it & 1) && n >= 1 && n <= 4) { w2_piece(w2, wave, (it + 3) >> 1, 2 * (n - 1)); w2_piece(w2, wave, (it + 3) >> 1, 2 * (n - 1) + 1); }
      }
      if (it == 9 && n == 6) {
        int ln = lane; asm volatile("" : "+v"(ln));
        const unsigned fzo = WS_FZ + (((ln & 1) ? (112 * J + 111) >> 7 : (112 * J) >> 7) * 32 + T) * 4;
        asm volatile("buffer_load_dword %0, %1, %2, 0 offen sc1" : "=v"(fz) : "v"(fzo), "s"(wsr) : "memory");
      }
      if (it == 13) {
        if (n == 0) {
          asm volatile("" : "+v"(fz));
          if (!__all(fz == tag)) {
            int ln = lane; asm volatile("" : "+v"(ln));
            poll_flags(wsr, WS_FZ + (((ln & 1) ? (112 * J + 111) >> 7 : (112 * J) >> 7) * 32 + T) * 4, tag);
          }
        }
        int l8 = lane; asm volatile("" : "+v"(l8));
        const unsigned vo = WS_Z1D + (tt * 56 + 7 * J + n) * 512 + l8 * 8;
        asm volatile("buffer_load_dwordx2 %0, %1, %2, 0 offen sc1" : "=v"(z1v[n]) : "v"(vo), "s"(wsr) : "memory");
        if (n == 6) {
          const unsigned vb = (unsigned)(16 * tt + (l8 & 15)) * 2u;
          asm volatile("global_load_ushort %0, %1, %2" : "=v"(bsw) : "v"(vb), "s"(p.b_sp) : "memory");
        }
      }
      __builtin_amdgcn_sched_barrier(0);
    }
  }
#undef B_WAITZ
  MFMA_DONE();
  __builtin_amdgcn_s_setprio(EPI_PRIO);
  STAMP(1, 1);
  asm volatile("s_waitcnt vmcnt(0)" : "+v"(z1v[0]), "+v"(z1v[1]), "+v"(z1v[2]), "+v"(z1v[3]), "+v"(z1v[4]), "+v"(z1v[5]), "+v"(z1v[6]), "+v"(bsw)::"memory");
  const float bs = bf2f((unsigned short)bsw);
  if (wave == 0) b_epi<0>(smem, wsr, acc, z1v, bs, lane, tt, J);
  else if (wave == 1) b_epi<1>(smem, wsr, acc, z1v, bs, lane, tt, J);
  else if (wave == 2) b_epi<2>(smem, wsr, acc, z1v, bs, lane, tt, J);
  else b_epi<3>(smem, wsr, acc, z1v, bs, lane, tt, J);
  __builtin_amdgcn_s_setprio(0);
  STAMP(1, 2);
}

__device__ __forceinline__ f32x2 cmul(f32x2 a, f32x2 b) {
  f32x2 t, r;
  asm("v_pk_mul_f32 %0, %1, %2 op_sel:[0,0] op_sel_hi:[0,1]" : "=v"(t) : "v"(a), "v"(b));
  asm("v_pk_fma_f32 %0, %1, %2, %3 op_sel:[1,1,0] op_sel_hi:[1,0,1] neg_lo:[1,0,0]" : "=v"(r) : "v"(a), "v"(b), "v"(t));
  return r;
}
__device__ __forceinline__ unsigned hmul(unsigned a, unsigned b) {
  unsigned t, r;
  asm("v_pk_mul_f16 %0, %1, %2 op_sel:[0,0] op_sel_hi:[0,1]" : "=v"(t) : "v"(a), "v"(b));
  asm("v_pk_fma_f16 %0, %1, %2, %3 op_sel:[1,1,0] op_sel_hi:[1,0,1] neg_lo:[1,0,0]" : "=v"(r) : "v"(a), "v"(b), "v"(t));
  return r;
}
__device__ __forceinline__ unsigned hcv(f32x2 v) { return pk_f16(v.x, v.y); }
__device__ __forceinline__ f32x2 cexp_p(float al, float be, float pp) {
  const float e = __builtin_amdgcn_exp2f(al * pp), th = __builtin_amdgcn_fractf(be * pp);
  return (f32x2){__builtin_amdgcn_cosf(th), __builtin_amdgcn_sinf(th)} * e;
}
struct PosTab { unsigned short v[2064]; };
constexpr int pos_bf16(int j) {
  if (j < 256) return j;
  int sh = 1; while ((j >> sh) >= 256) ++sh;
  const int q = j >> sh, r = j & ((1 << sh) - 1), h = 1 << (sh - 1);
  return (r > h || (r == h && (q & 1)) ? q + 1 : q) << sh;
}
constexpr PosTab make_postab() {
  PosTab t = {};
  for (int x = 0; x < 2048; ++x) {
    const int p = pos_bf16(2047 - x);
    int col = 0, a = 0;
    if (p < 256) { a = p >> 4; col = p & 15; }
    else if (p < 512) { const int r = p - 256; a = r >> 4; col = 16 + ((r & 15) >> 1); }
    else if (p < 1024) { const int r = p - 512; a = (r & 255) >> 4; col = 24 + 4 * (r >> 8) + ((r & 15) >> 2); }
    else if (p < 2048) { const int r = p - 1024; a = (r & 255) >> 4; col = 32 + 2 * (r >> 8) + ((r & 15) >> 3); }
    else { a = 0; col = 40; }
    t.v[x] = (unsigned short)((col * 16 + a) * 4);
  }
  for (int x = 2048; x < 2064; ++x) t.v[x] = 48 * 64;
  return t;
}
__device__ const PosTab POSTAB = make_postab();

template <int HW>
__device__ __forceinline__ void s4d_conv(LAS char* cb, rsrc_t wsr, int d, int lane, float iscale, float dsk) {
  constexpr int NF = HW ? 7 : 8;
  f32x4 acc[8] = {};
  const int r = lane & 15, h = lane >> 4;
  LAS const char* ab = cb + CH_FT + ((7 - r) & 7) * CH_CS + (255 - (r >> 3) - 2 * (h >> 1) + (h & 1)) * 16 - (64 * 7 + 512 * 7);
  LAS const char* bb = cb + CH_U + 512 + 32 * r - 32 * (h >> 1) + 16 * (h & 1) - 64 * 7;
  u32x4 A[2][8], B[2][8];
#define CONV_LOAD(ar, buf)                                                                                   \
  _Pragma("unroll") for (int k = 0; k < NF; ++k) {                                                           \
    B[buf][k] = *(LAS const u32x4*)(bb + 512 * k + 64 * (7 - (ar)));                                         \
    A[buf][k] = *(LAS const u32x4*)(ab + 512 * (7 - k) + 64 * (7 - (ar)));                                   \
  }
  CONV_LOAD(0, 0);
#pragma unroll
  for (int ar = 0; ar < 8; ++ar) {
    __builtin_amdgcn_sched_barrier(0);
    if (ar < 7) { CONV_LOAD(ar + 1, (ar + 1) & 1); }
#pragma unroll
    for (int al = 0; al < NF; ++al)
#pragma unroll
      for (int be = 0; be < NF; ++be) {
        const int tau = al + be;
        if (HW ? (tau >= 4 && tau <= 6) : (tau <= 3 || tau == 7)) acc[tau] = mfma_h(A[ar & 1][al], B[ar & 1][be], acc[tau]);
      }
#pragma unroll
    for (int i = 0; i < 16; ++i) { __builtin_amdgcn_sched_group_barrier(0x008, 1, 0); __builtin_amdgcn_sched_group_barrier(0x100, 1, 0); }
    __builtin_amdgcn_sched_barrier(0);
  }
#undef CONV_LOAD
  const int q = lane >> 4;
#pragma unroll
  for (int t = 0; t < 8; ++t) {
    if (HW ? (t >= 4 && t <= 6) : (t <= 3 || t == 7)) {
      const f32x4 v = acc[t];
      const int l0 = 256 * t + 16 * r + 4 * q;
      const u32x2 uw = *(LAS const u32x2*)(cb + CH_U + 512 + l0 * 2);
      unsigned o[2];
#pragma unroll
      for (int pp = 0; pp < 2; ++pp) {
        const unsigned uwp = uw[pp];
        const f16x2 ua = __builtin_bit_cast(f16x2, uwp);
        const f32x2 y2 = up_bf(rn_pair((f32x2){v[2 * pp], v[2 * pp + 1]} * iscale)), k2 = up_bf(rn_pair((f32x2){(float)ua[0], (float)ua[1]} * dsk));
        o[pp] = rn_pair(y2 + k2);
      }
      st8(wsr, WS_ST + (d * SEQ + l0) * 2, (u32x2){o[0], o[1]});
    }
  }
}

__device__ __forceinline__ void phaseB_s4d(LAS char* smem, LAS char* cb, const Params& p, rsrc_t wsr, unsigned tag, int rep, bool poller, int fz_, int d, int hw, int lane MARKS_ARG) {
  char* ws = p.ws;
  LAS char* cnt = cb + CH_CNT;
  unsigned la_[2], ai_[2], cr_[4], ci_[4], ldt_, dsk_;
  u32x4 pt0, pt1, pt2;
  {
    const unsigned vo = (unsigned)(d * NST + 128 * hw + lane) * 2u, vo2 = (unsigned)(d * NST + ((128 * hw + 128 + lane) & 255)) * 2u, vd = (unsigned)d * 2u;
    asm volatile("global_load_ushort %0, %1, %2" : "=v"(la_[0]) : "v"(vo), "s"(p.logA) : "memory");
    asm volatile("global_load_ushort %0, %1, %2 offset:128" : "=v"(la_[1]) : "v"(vo), "s"(p.logA) : "memory");
    asm volatile("global_load_ushort %0, %1, %2" : "=v"(ai_[0]) : "v"(vo), "s"(p.Aim) : "memory");
    asm volatile("global_load_ushort %0, %1, %2 offset:128" : "=v"(ai_[1]) : "v"(vo), "s"(p.Aim) : "memory");
    asm volatile("global_load_ushort %0, %1, %2" : "=v"(cr_[0]) : "v"(vo), "s"(p.Cre) : "memory");
    asm volatile("global_load_ushort %0, %1, %2 offset:128" : "=v"(cr_[1]) : "v"(vo), "s"(p.Cre) : "memory");
    asm volatile("global_load_ushort %0, %1, %2" : "=v"(cr_[2]) : "v"(vo2), "s"(p.Cre) : "memory");
    asm volatile("global_load_ushort %0, %1, %2 offset:128" : "=v"(cr_[3]) : "v"(vo2), "s"(p.Cre) : "memory");
    asm volatile("global_load_ushort %0, %1, %2" : "=v"(ci_[0]) : "v"(vo), "s"(p.Cim) : "memory");
    asm volatile("global_load_ushort %0, %1, %2 offset:128" : "=v"(ci_[1]) : "v"(vo), "s"(p.Cim) : "memory");
    asm volatile("global_load_ushort %0, %1, %2" : "=v"(ci_[2]) : "v"(vo2), "s"(p.Cim) : "memory");
    asm volatile("global_load_ushort %0, %1, %2 offset:128" : "=v"(ci_[3]) : "v"(vo2), "s"(p.Cim) : "memory");
    asm volatile("global_load_ushort %0, %1, %2" : "=v"(ldt_) : "v"(vd), "s"(p.logdt) : "memory");
    asm volatile("global_load_ushort %0, %1, %2" : "=v"(dsk_) : "v"(vd), "s"(p.Dsk) : "memory");
    const char* pb = (const char*)POSTAB.v + hw * 2048;
    const unsigned vp = lane * 32;
    asm volatile("global_load_dwordx4 %0, %1, %2" : "=v"(pt0) : "v"(vp), "s"(pb) : "memory");
    asm volatile("global_load_dwordx4 %0, %1, %2 offset:16" : "=v"(pt1) : "v"(vp), "s"(pb) : "memory");
    asm volatile("global_load_dwordx4 %0, %1, %2 offset:32" : "=v"(pt2) : "v"(vp), "s"(pb) : "memory");
  }
  BAR();
  if (hw == 0 && lane < 32) *(LAS u32x4*)(cb + CH_U + lane * 16) = (u32x4){0u, 0u, 0u, 0u};
  if (lane < 4) *(LAS u32x4*)(cb + CH_T + hw * CH_TS + 48 * 64 + lane * 16) = (u32x4){0u, 0u, 0u, 0u};
  __builtin_amdgcn_s_setprio(S4D_PRIO);
  asm volatile("s_waitcnt vmcnt(0)" : "+v"(la_[0]), "+v"(la_[1]), "+v"(ai_[0]), "+v"(ai_[1]), "+v"(cr_[0]), "+v"(cr_[1]), "+v"(cr_[2]), "+v"(cr_[3]), "+v"(ci_[0]), "+v"(ci_[1]),
               "+v"(ci_[2]), "+v"(ci_[3]), "+v"(ldt_), "+v"(dsk_), "+v"(pt0), "+v"(pt1), "+v"(pt2)::"memory");
  z1_signal(smem, wsr, tag, fz_, lane);
  STAMP(1, 0);
  float cr[4], ci[4];
#pragma unroll
  for (int k = 0; k < 4; ++k) { cr[k] = bf2f((unsigned short)cr_[k]); ci[k] = bf2f((unsigned short)ci_[k]); }
  const float dt = rbf(__builtin_amdgcn_exp2f(bf2f((unsigned short)ldt_) * 1.4426950408889634f)), dsk = bf2f((unsigned short)dsk_);
  float sabs = 0.f;
#pragma unroll
  for (int k = 0; k < 4; ++k) sabs += __builtin_fabsf(cr[k]) + __builtin_fabsf(ci[k]);
  sabs = xsum16_32(red16(sabs));
  sabs *= 2.0f * dt;
  int ex = (int)((__float_as_uint(sabs) >> 23) & 0xff) - 126;
  ex = ex < -100 ? -100 : (ex > 100 ? 100 : ex);
  const float scale = __uint_as_float((unsigned)(127 + 15 - ex) << 23), iscale = __uint_as_float((unsigned)(127 - 15 + ex) << 23);
  f32x4 kacc[3] = {};
  LAS char* fw = cb + CH_FT + hw * CH_FW;
#pragma unroll
  for (int ps = 0; ps < 2; ++ps) {
    const float lap = bf2f((unsigned short)la_[ps]), aip = bf2f((unsigned short)ai_[ps]);
    const float crp = cr[ps], cip = ci[ps];
    const float lre = -rbf(__builtin_amdgcn_exp2f(lap * 1.4426950408889634f)) * dt, lim = aip * dt;
    const float al = lre * 1.4426950408889634f, be = lim * 0.15915494309189535f;
    LAS char* wp = fw + lane * 4;
#define PUTH(row, v) *(LAS unsigned*)(wp + (row) * CH_FS) = (v)
    {
      const f32x2 w = cexp_p(al, be, 16.f);
      const float cs = 2.0f * dt * scale;
      const f32x2 c0 = {crp * cs, cip * cs};
      const f32x2 w2 = cmul(w, w), c1 = cmul(c0, w), w4 = cmul(w2, w2), c2 = cmul(c0, w2), c3 = cmul(c1, w2), w8 = cmul(w4, w4), w12 = cmul(w8, w4);
      const unsigned hc[4] = {hcv(c0), hcv(c1), hcv(c2), hcv(c3)}, hw4[3] = {hcv(w4), hcv(w8), hcv(w12)};
#pragma unroll
      for (int j = 0; j < 4; ++j) {
        PUTH(j, hc[j]);
#pragma unroll
        for (int k = 0; k < 3; ++k) PUTH(4 * (k + 1) + j, hmul(hc[j], hw4[k]));
      }
    }
    {
      const f32x2 e1 = cexp_p(al, -be, 1.f);
      const f32x2 e2 = cmul(e1, e1), e3 = cmul(e2, e1), e4 = cmul(e2, e2), e8 = cmul(e4, e4), e12 = cmul(e8, e4);
      unsigned E[16];
      E[0] = 0x00003c00u; E[1] = hcv(e1); E[2] = hcv(e2); E[3] = hcv(e3); E[4] = hcv(e4); E[8] = hcv(e8); E[12] = hcv(e12);
#pragma unroll
      for (int m = 1; m < 4; ++m)
#pragma unroll
        for (int j = 1; j < 4; ++j) E[4 * m + j] = hmul(E[4 * m], E[j]);
#pragma unroll
      for (int k = 0; k < 16; ++k) PUTH(16 + k, E[k]);
      const f32x2 z256 = cexp_p(al, -be, 256.f), z512 = cexp_p(al, -be, 512.f), z1024 = cexp_p(al, -be, 1024.f), z2048 = cexp_p(al, -be, 2048.f);
      const f32x2 z768 = cmul(z512, z256);
      const unsigned h256 = hcv(z256), h512 = hcv(z512), h768 = hcv(z768);
      PUTH(32, h256);
#pragma unroll
      for (int k = 1; k < 8; ++k) PUTH(32 + k, hmul(h256, E[2 * k]));
      PUTH(40, h512); PUTH(44, h768);
#pragma unroll
      for (int k = 1; k < 4; ++k) { PUTH(40 + k, hmul(h512, E[4 * k])); PUTH(44 + k, hmul(h768, E[4 * k])); }
      const unsigned hb[4] = {hcv(z1024), hcv(cmul(z1024, z256)), hcv(cmul(z1024, z512)), hcv(cmul(z1024, z768))};
#pragma unroll
      for (int k = 0; k < 4; ++k) { PUTH(48 + 2 * k, hb[k]); PUTH(49 + 2 * k, hmul(hb[k], E[8])); }
      PUTH(56, hcv(z2048));
    }
#undef PUTH
    asm volatile("s_waitcnt lgkmcnt(0)" ::: "memory");
    LAS const char* rp = fw + (lane & 15) * CH_FS + (lane >> 4) * 16;
    u32x4 fa[4], fb[4][3];
#pragma unroll
    for (int kk = 0; kk < 4; ++kk) {
      fa[kk] = *(LAS const u32x4*)(rp + kk * 64);
#pragma unroll
      for (int t = 0; t < 3; ++t) fb[kk][t] = *(LAS const u32x4*)(rp + (16 + 16 * t) * CH_FS + kk * 64);
    }
#pragma unroll
    for (int kk = 0; kk < 4; ++kk)
#pragma unroll
      for (int t = 0; t < 3; ++t) kacc[t] = mfma_h(fa[kk], fb[kk][t], kacc[t]);
    asm volatile("" ::: "memory");
  }
  STAMP(1, 1);
  u32x4 u0, u1;
  group_wait(poller, wsr, WS_FA + rep * 1024 + 7 * 128 + (lane & 31) * 4, tag, smem + B_CH + CH_CNT + 76);
  {
    const unsigned vu = WS_XT + d * SEQ * 2 + hw * 2048 + lane * 16;
    asm volatile("buffer_load_dwordx4 %0, %1, %2, 0 offen sc1" : "=v"(u0) : "v"(vu), "s"(wsr) : "memory");
    asm volatile("buffer_load_dwordx4 %0, %1, %2, 0 offen offset:1024 sc1" : "=v"(u1) : "v"(vu), "s"(wsr) : "memory");
  }
#pragma unroll
  for (int t = 0; t < 3; ++t) *(LAS f32x4*)(cb + CH_T + hw * CH_TS + (16 * t + (lane & 15)) * 64 + (lane >> 4) * 16) = kacc[t];
  lds_sync(cnt, 2, lane);
  {
    const int y = 2 * (64 * hw + lane);
    const unsigned po[12] = {pt0[0], pt0[1], pt0[2], pt0[3], pt1[0], pt1[1], pt1[2], pt1[3], pt2[0], pt2[1], pt2[2], pt2[3]};
    unsigned W[12], V[11];
#pragma unroll
    for (int k = 0; k < 12; ++k) {
      LAS const char* a0 = cb + CH_T + (po[k] & 0xffffu);
      LAS const char* a1 = cb + CH_T + (po[k] >> 16);
      W[k] = pk_f16(*(LAS const float*)a0 + *(LAS const float*)(a0 + CH_TS), *(LAS const float*)a1 + *(LAS const float*)(a1 + CH_TS));
    }
#pragma unroll
    for (int i = 0; i < 11; ++i) V[i] = __builtin_amdgcn_alignbit(W[i + 1], W[i], 16);
#pragma unroll
    for (int m = 0; m < 4; ++m) {
      *(LAS u32x4*)(cb + CH_FT + (2 * m) * CH_CS + y * 16) = (u32x4){W[m], W[m + 1], W[m + 2], W[m + 3]};
      *(LAS u32x4*)(cb + CH_FT + (2 * m) * CH_CS + y * 16 + 16) = (u32x4){W[m + 4], W[m + 5], W[m + 6], W[m + 7]};
      *(LAS u32x4*)(cb + CH_FT + (2 * m + 1) * CH_CS + y * 16) = (u32x4){V[m], V[m + 1], V[m + 2], V[m + 3]};
      *(LAS u32x4*)(cb + CH_FT + (2 * m + 1) * CH_CS + y * 16 + 16) = (u32x4){V[m + 4], V[m + 5], V[m + 6], V[m + 7]};
    }
    if (hw == 1 && lane < 8) *(LAS u32x4*)(cb + CH_FT + lane * CH_CS + 256 * 16) = (u32x4){0u, 0u, 0u, 0u};
  }
  asm volatile("s_waitcnt vmcnt(0)" : "+v"(u0), "+v"(u1)::"memory");
  *(LAS u32x4*)(cb + CH_U + 512 + hw * 2048 + lane * 16) = u0;
  *(LAS u32x4*)(cb + CH_U + 512 + hw * 2048 + 1024 + lane * 16) = u1;
  lds_sync(cnt, 4, lane);
  __builtin_amdgcn_s_setprio(CONV_PRIO);
  STAMP(1, 2);
  if (hw) s4d_conv<1>(cb, wsr, d, lane, iscale, dsk); else s4d_conv<0>(cb, wsr, d, lane, iscale, dsk);
  __builtin_amdgcn_s_setprio(0);
  WAIT_VM(0);
  {
    unsigned old = 0;
    if (lane == 0) old = __hip_atomic_fetch_add((LAS unsigned*)(smem + B_CH + CH_CNT + 84), 1u, __ATOMIC_RELAXED, __HIP_MEMORY_SCOPE_WORKGROUP);
    if (__builtin_amdgcn_readfirstlane(old) == 3u && lane < 8) st4(wsr, WS_FS + lane * 1024 + (d >> 1) * 4, tag);
  }
  STAMP(1, 3);
}

constexpr int C_PART = 0;
constexpr int C_XS = 65536;
constexpr int C_SS = C_XS + 64 * 136;
constexpr int C_OS = C_SS + 64 * 136;

constexpr int C_GO = C_OS + 64 * 144;
__device__ __forceinline__ void phaseC(LAS char* smem, const Params& p, rsrc_t wsr, unsigned tag, int rep, int T, int Dt, int tid MARKS_ARG) {
  char* ws = p.ws;
  const int lane = tid & 63;
  const int wave = __builtin_amdgcn_readfirstlane(tid >> 6);
  unsigned fo;
  {
    const int J = lane & 7, bh = 2 * (T >> 3) + (J >> 2) + 8 * ((T & 7) + 8 * (J & 3));
    const int bs = 32 * Dt + ((lane - 8) & 31);
    fo = (lane < 8 ? WS_FB + bh * 4 : WS_FS + bs * 4) + rep * 1024;
  }
  if (wave < 4) {
    const unsigned ho = WS_HF + ((4 * T * 28 + 7 * wave) * 64 + lane) * 16, wo = WS_WOF + ((4 * Dt * 28 + 7 * wave) * 64 + lane) * 16;
    u32x4 hf[7][4], wf[7][4];
    f32x4 acc[4][4];
#pragma unroll
    for (int k = 0; k < 7; ++k)
#pragma unroll
      for (int m = 0; m < 4; ++m) wf[k][m] = ld16(wsr, wo + (m * 28 + k) * 1024);
    __builtin_amdgcn_sched_barrier(0);
    group_wait(false, wsr, fo, tag, smem + C_GO);
    STAMP(2, 0);
#pragma unroll
    for (int n = 0; n < 4; ++n)
#pragma unroll
      for (int m = 0; m < 4; ++m) acc[n][m] = (f32x4){0.f, 0.f, 0.f, 0.f};
#pragma unroll
    for (int k = 0; k < 4; ++k)
#pragma unroll
      for (int m = 0; m < 4; ++m) hf[k][m] = ld16(wsr, ho + (m * 28 + k) * 1024);
    __builtin_amdgcn_sched_barrier(0);
#pragma unroll
    for (int k = 0; k < 7; ++k) {
#pragma unroll
      for (int n = 0; n < 4; ++n)
#pragma unroll
        for (int m = 0; m < 4; ++m) acc[n][m] = mfma_bf(__builtin_bit_cast(s16x8, wf[k][n]), __builtin_bit_cast(s16x8, hf[k][m]), acc[n][m]);
      __builtin_amdgcn_sched_barrier(0);
      if (k + 4 < 7) {
#pragma unroll
        for (int m = 0; m < 4; ++m) hf[k + 4][m] = ld16(wsr, ho + (m * 28 + k + 4) * 1024);
      }
      __builtin_amdgcn_sched_barrier(0);
    }
    STAMP(2, 1);
#pragma unroll
    for (int n = 0; n < 4; ++n)
#pragma unroll
      for (int m = 0; m < 4; ++m) *(LAS f32x4*)(smem + C_PART + ((wave * 4 + m) * 4 + n) * 1024 + lane * 16) = acc[n][m];
  } else {
    const int u = tid - 256, row = u >> 2, c = u & 3;
    const u32x4 x0 = *(const u32x4*)(p.x + (size_t)(64 * T + row) * DM + 64 * Dt + 16 * c);
    const u32x4 x1 = *(const u32x4*)(p.x + (size_t)(64 * T + row) * DM + 64 * Dt + 16 * c + 8);
#pragma unroll
    for (int e = 0; e < 2; ++e) {
      *(LAS u32x2*)(smem + C_XS + row * 136 + c * 32 + e * 8) = (u32x2){x0[2 * e], x0[2 * e + 1]};
      *(LAS u32x2*)(smem + C_XS + row * 136 + c * 32 + 16 + e * 8) = (u32x2){x1[2 * e], x1[2 * e + 1]};
    }
    group_wait(wave == 4, wsr, fo, tag, smem + C_GO);
    const unsigned so = WS_ST + ((64 * Dt + row) * SEQ + 64 * T + 16 * c) * 2;
    const u32x4 s0 = ld16(wsr, so), s1 = ld16(wsr, so + 16);
#pragma unroll
    for (int e = 0; e < 2; ++e) {
      *(LAS u32x2*)(smem + C_SS + row * 136 + c * 32 + e * 8) = (u32x2){s0[2 * e], s0[2 * e + 1]};
      *(LAS u32x2*)(smem + C_SS + row * 136 + c * 32 + 16 + e * 8) = (u32x2){s1[2 * e], s1[2 * e + 1]};
    }
  }
  BAR();
  {
    const int m = wave & 3, l15 = lane & 15, q = lane >> 4, t = 16 * m + l15;
#pragma unroll
    for (int nn = 0; nn < 2; ++nn) {
      const int n = 2 * (wave >> 2) + nn;
      f32x4 o = *(LAS const f32x4*)(smem + C_PART + ((0 * 4 + m) * 4 + n) * 1024 + lane * 16);
#pragma unroll
      for (int sw = 1; sw < 4; ++sw) o += *(LAS const f32x4*)(smem + C_PART + ((sw * 4 + m) * 4 + n) * 1024 + lane * 16);
      const s16x4 sv = tr_read(smem + C_SS + (16 * n + 4 * q + (l15 >> 2)) * 136 + (16 * m + 4 * (lane & 3)) * 2);
      const u32x2 sw2 = __builtin_bit_cast(u32x2, sv);
      const u32x2 xw = *(LAS const u32x2*)(smem + C_XS + t * 136 + (16 * n + 4 * q) * 2);
      unsigned ow[2];
#pragma unroll
      for (int pp = 0; pp < 2; ++pp) {
        const f32x2 v = up_bf(rn_pair((f32x2){o[2 * pp], o[2 * pp + 1]}));
        const f32x2 e = v * -1.4426950408889634f;
        const f32x2 dd = (f32x2){__builtin_amdgcn_exp2f(e.x), __builtin_amdgcn_exp2f(e.y)} + 1.0f;
        const f32x2 g = up_bf(rn_pair(v * (f32x2){__builtin_amdgcn_rcpf(dd.x), __builtin_amdgcn_rcpf(dd.y)}));
        const unsigned s_ = sw2[pp], x_ = xw[pp];
        ow[pp] = rn_pair(up_bf(x_) + up_bf(rn_pair(up_bf(s_) * g)));
      }
      *(LAS u32x2*)(smem + C_OS + t * 144 + (16 * n + 4 * q) * 2) = (u32x2){ow[0], ow[1]};
    }
  }
  BAR();
  {
    const int row = tid >> 3, c = tid & 7;
    *(u32x4*)(p.out + (size_t)(64 * T + row) * DM + 64 * Dt + 8 * c) = *(LAS const u32x4*)(smem + C_OS + row * 144 + c * 16);
  }
}

__global__ __launch_bounds__(512, 2) void mixer_block(Params p) {
  extern __shared__ __attribute__((aligned(16))) char smem_raw[];
  LAS char* smem = (LAS char*)smem_raw;
  char* ws = p.ws;
  const int tid = threadIdx.x, lane = tid & 63;
  const int wave = __builtin_amdgcn_readfirstlane(tid >> 6);
  const int b = blockIdx.x, xcd = b & 7, rr = b >> 3;
  const rsrc_t wsr = mk_rsrc(ws, WS_END);
  STAMP_DECL;
  STAMP(0, 0);
  const unsigned tag = *(const __attribute__((address_space(4))) unsigned*)(ws + WS_EP) + 1u;
  const int T2 = 8 * (xcd >> 1) + (rr & 7), J2 = 4 * (xcd & 1) + (rr >> 3);
  const bool aux = b >= 224;
  if (!aux) {
    phaseA_gemm(smem, p, wsr, tag, 4 * xcd + rr / 7, rr % 7, T2, tid MARKS_PASS);
    if (tid < 64) {
      if (lane < 32) *(LAS unsigned*)(smem + B_CH + CH_CNT + lane * 4) = 0u;
      else *(LAS unsigned*)(smem + B_CH + B_CHS + CH_CNT + (lane - 32) * 4) = 0u;
    }
  } else {
    if (tid < 32) *(LAS unsigned*)(smem + B_CH + CH_CNT + tid * 4) = 0u;
    BAR();
    if (wave < 4) {
      phaseA_aux(smem, p, wsr, tag, b - 224, tid);
      const W2Src w2 = w2_src(p, T2, wave, lane);
#pragma unroll
      for (int k = 0; k < 16; ++k) w2_piece(w2, wave, k >> 3, k & 7);
    } else {
      const int pw = (b - 224) * 4 + (wave - 4);
      const unsigned pl = (unsigned)(AUX_PF + (wave - 4) * 1024);
#pragma unroll
      for (int k = 0; k < 7; ++k) {
        const int pc = k * 128 + pw;
        glds16((const char*)p.w_in + (size_t)(pc >> 1) * 3584 + (pc & 1) * 896, (unsigned)(lane < 56 ? lane : 55) * 16u, pl);
      }
#pragma unroll
      for (int k = 0; k < 7; ++k) {
        const int pc = k * 128 + pw, a = pc / 224, o = pc - a * 224;
        const unsigned short* src = a == 0 ? p.logA : a == 1 ? p.Aim : a == 2 ? p.Cre : p.Cim;
        glds16((const char*)src + (size_t)o * 1024, (unsigned)lane * 16u, pl);
      }
#pragma unroll 8
      for (int k = 0; k < 64; ++k) {
        glds16((const char*)p.w_sp + (size_t)(k * 128 + pw) * 1024, (unsigned)lane * 16u, pl);
        if ((k & 7) == 7) WAIT_VM(16);
      }
      if (b == 255 && wave == 4) {
#pragma unroll
        for (int k = 0; k < 4; ++k) poll_flags(wsr, WS_FA + 7 * 1024 + (64 * k + lane) * 4, tag);
        if (lane == 0) st4(wsr, WS_EP, tag);
      }
    }
  }
  STAMP(0, 6);
  if (wave < 4) {
    if (!aux) BAR();
    phaseB_gemm(smem, p, wsr, tag, xcd, aux ? -1 : (rr % 7) * 32 + 4 * xcd + rr / 7, wave, lane, T2, J2 MARKS_PASS);
  } else if (!aux) {
    const int sw = wave - 4;
    phaseB_s4d(smem, smem + B_CH + (sw >> 1) * B_CHS, p, wsr, tag, xcd, sw == 0, (rr % 7) * 32 + 4 * xcd + rr / 7, 2 * b + (sw >> 1), sw & 1, lane MARKS_PASS);
  }
  WAIT_VM(0);
  BAR();
  if (tid < 8) st4(wsr, WS_FB + tid * 1024 + b * 4, tag);
  STAMP(1, 4);
  if (!aux) {
    if (tid == 64) *(LAS unsigned*)(smem + C_GO) = 0u;
    BAR();
    phaseC(smem, p, wsr, tag, xcd, 4 * xcd + rr / 7, rr % 7, tid MARKS_PASS);
    STAMP(2, 2);
  }
  STAMP_DUMP();
}

extern "C" void kernel_launch(void* const* d_in, const int* in_sizes, int n_in, void* d_out, int out_size, void* d_ws, size_t ws_size, hipStream_t stream) {
  Params p;
  p.x = (const unsigned short*)d_in[0];
  p.logA = (const unsigned short*)d_in[1];
  p.Aim = (const unsigned short*)d_in[2];
  p.Cre = (const unsigned short*)d_in[3];
  p.Cim = (const unsigned short*)d_in[4];
  p.logdt = (const unsigned short*)d_in[5];
  p.Dsk = (const unsigned short*)d_in[6];
  p.w_in = (const unsigned short*)d_in[8];
  p.w_sp = (const unsigned short*)d_in[9];
  p.b_sp = (const unsigned short*)d_in[10];
  p.w_out = (const unsigned short*)d_in[11];
  p.ws = (char*)d_ws;
  p.out = (unsigned short*)d_out;
  (void)hipFuncSetAttribute((const void*)mixer_block, hipFuncAttributeMaxDynamicSharedMemorySize, 163840);
  mixer_block<<<256, 512, 163840, stream>>>(p);
}
```

```cpp
#include <hip/hip_runtime.h>
#include <hip/hip_bf16.h>
#include <stdint.h>

#define LAS __attribute__((address_space(3)))
#define GAS __attribute__((address_space(1)))
typedef float f32x2 __attribute__((ext_vector_type(2)));
typedef float f32x4 __attribute__((ext_vector_type(4)));
typedef unsigned u32x2 __attribute__((ext_vector_type(2)));
typedef unsigned u32x4 __attribute__((ext_vector_type(4)));
typedef short s16x4 __attribute__((ext_vector_type(4)));
typedef short s16x8 __attribute__((ext_vector_type(8)));
typedef __bf16 bf16x2 __attribute__((ext_vector_type(2)));
typedef __bf16 bf16x8 __attribute__((ext_vector_type(8)));
typedef _Float16 f16x2 __attribute__((ext_vector_type(2)));
typedef _Float16 f16x8 __attribute__((ext_vector_type(8)));

constexpr int SEQ = 2048, DM = 448, FH = 896, FF = 1792, NST = 256;

constexpr size_t WS_Z1D = 0;
constexpr size_t WS_Z2F = WS_Z1D + (size_t)SEQ * FH * 2;
constexpr size_t WS_HF  = WS_Z2F + (size_t)SEQ * FH * 2;
constexpr size_t WS_XT  = WS_HF + (size_t)SEQ * FH * 2;
constexpr size_t WS_ST  = WS_XT + (size_t)SEQ * DM * 2;
constexpr size_t WS_WOF = WS_ST + (size_t)SEQ * DM * 2;
constexpr size_t WS_XCH = WS_WOF + (size_t)FH * DM * 2;
constexpr size_t WS_EP  = WS_XCH + 32 * 64 * 8 * 16;
constexpr size_t WS_XS1 = WS_EP + 32 * 128;
constexpr size_t WS_END = WS_XS1 + 32 * 1024;

constexpr size_t WS_DBG = 32u << 20;
#define STAMP(kern, slot) do {} while (0)
#define MARK_DECL do {} while (0)
#define MARK(i) do {} while (0)
#define MARK_DUMP(kern) do {} while (0)
__device__ __forceinline__ float bf2f(unsigned short h) { return __uint_as_float((unsigned)h << 16); }
__device__ __forceinline__ float bflo(unsigned w) { return __uint_as_float(w << 16); }
__device__ __forceinline__ float bfhi(unsigned w) { return __uint_as_float(w & 0xffff0000u); }
__device__ __forceinline__ unsigned pk_bf16(float lo, float hi) {
  bf16x2 v = {(__bf16)lo, (__bf16)hi};
  return __builtin_bit_cast(unsigned, v);
}
__device__ __forceinline__ float rbf(float x) { return (float)(__bf16)x; }
__device__ __forceinline__ unsigned pk_f16(float lo, float hi) {
  f16x2 v = {(_Float16)lo, (_Float16)hi};
  return __builtin_bit_cast(unsigned, v);
}
__device__ __forceinline__ float gelu_tanh(float x) {
  const float c1 = -2.0f * 0.7978845608028654f * 1.4426950408889634f, c2 = c1 * 0.044715f;
  float t = x * x;
  float a = x * __builtin_fmaf(t, c2, c1);
  float e = __builtin_amdgcn_exp2f(a);
  return x * __builtin_amdgcn_rcpf(1.0f + e);
}
template <int CTRL> __device__ __forceinline__ float dpp_add(float v) {
  int t = __builtin_amdgcn_update_dpp(0, __float_as_int(v), CTRL, 0xf, 0xf, true);
  return v + __int_as_float(t);
}
__device__ __forceinline__ float red16(float v) {
  v = dpp_add<0xB1>(v);
  v = dpp_add<0x4E>(v);
  v = dpp_add<0x141>(v);
  v = dpp_add<0x140>(v);
  return v;
}
__device__ __forceinline__ float red8(float v) {
  v = dpp_add<0xB1>(v); v = dpp_add<0x4E>(v); v = dpp_add<0x141>(v);
  return v;
}
__device__ __forceinline__ void glds16(const void* sbase, unsigned voff, unsigned lds) {
  asm volatile("s_mov_b32 m0, %2\n\ts_nop 0\n\tglobal_load_lds_dwordx4 %0, %1" ::"v"(voff), "s"(sbase), "s"(lds) : "memory");
}
#define WAIT_VM(n) asm volatile("s_waitcnt vmcnt(%0)" ::"n"(n) : "memory")
#define WAIT_LGKM0() asm volatile("s_waitcnt lgkmcnt(0)" ::: "memory")
#define BAR() do { asm volatile("s_waitcnt lgkmcnt(0)" ::: "memory"); __builtin_amdgcn_s_barrier(); asm volatile("" ::: "memory"); } while (0)
__device__ __forceinline__ s16x4 tr_read(LAS const char* p) {
  typedef short v4i16 __attribute__((ext_vector_type(4)));
  return __builtin_amdgcn_ds_read_tr16_b64_v4i16((LAS v4i16*)p);
}
__device__ __forceinline__ f32x4 mfma_bf(s16x8 a, s16x8 b, f32x4 c) {
  return __builtin_amdgcn_mfma_f32_16x16x32_bf16(__builtin_bit_cast(bf16x8, a), __builtin_bit_cast(bf16x8, b), c, 0, 0, 0);
}
__device__ __forceinline__ float dot2bf(unsigned a, unsigned b, float c) {
  return __builtin_amdgcn_fdot2_f32_bf16(__builtin_bit_cast(bf16x2, a), __builtin_bit_cast(bf16x2, b), c, false);
}

constexpr int K1_X = 0;
constexpr int K1_W = 57344;
constexpr int K1_SLOT = 16384;
constexpr int K1_S = K1_W + 6 * K1_SLOT;
constexpr int K1_P = K1_S;
constexpr int K1_M2 = K1_S + 2048;
constexpr int K1_R2 = K1_M2 + 256;
constexpr int K1_W1 = K1_R2 + 256;
constexpr int K1_LDS = 163840;
constexpr unsigned ONES_BF = 0x3f803f80u;
typedef float f32x16 __attribute__((ext_vector_type(16)));

__device__ __forceinline__ float xsum32(float v) {
  auto b = __builtin_amdgcn_permlane32_swap(__float_as_uint(v), __float_as_uint(v), false, false);
  return __uint_as_float(b[0]) + __uint_as_float(b[1]);
}
__device__ __forceinline__ float xsum16_32(float v) {
  auto a = __builtin_amdgcn_permlane16_swap(__float_as_uint(v), __float_as_uint(v), false, false);
  return xsum32(__uint_as_float(a[0]) + __uint_as_float(a[1]));
}
__device__ __forceinline__ f32x2 up_bf(unsigned w) { return (f32x2){bflo(w), bfhi(w)}; }
__device__ __forceinline__ unsigned rn_pair(f32x2 v) {
  unsigned r;
  asm("v_cvt_pk_bf16_f32 %0, %1, %2" : "=v"(r) : "v"(v.x), "v"(v.y));
  return r;
}
__device__ __forceinline__ f32x2 gelu2(f32x2 x) {
  const float c1 = -2.0f * 0.7978845608028654f * 1.4426950408889634f, c2 = c1 * 0.044715f;
  const f32x2 t = x * x;
  const f32x2 a = x * (t * c2 + c1);
  f32x2 d = {__builtin_amdgcn_exp2f(a.x), __builtin_amdgcn_exp2f(a.y)};
  d = d + 1.0f;
  return x * (f32x2){__builtin_amdgcn_rcpf(d.x), __builtin_amdgcn_rcpf(d.y)};
}
__device__ __forceinline__ void ln_scalars(float s, float q, float invn, float& mb, float& rr) {
  const float mean = s * invn;
  float var = q * invn - mean * mean; var = var < 0.f ? 0.f : var;
  mb = rbf(mean);
  rr = __builtin_amdgcn_rcpf(rbf(__builtin_amdgcn_sqrtf(rbf(rbf(var) + 1e-5f))));
}
__device__ __forceinline__ f32x16 mfma32_bf(s16x8 a, s16x8 b, f32x16 c) {
  return __builtin_amdgcn_mfma_f32_32x32x16_bf16(__builtin_bit_cast(bf16x8, a), __builtin_bit_cast(bf16x8, b), c, 0, 0, 0);
}
__device__ __forceinline__ void poll_granule(u32x4& gr, const char* base, unsigned voff, unsigned tag) {
  for (unsigned spins = 0;; ++spins) {
    const bool ok = (gr[1] == tag) && (gr[3] == tag);
    if (__all(ok) || spins > (1u << 22)) break;
    __builtin_amdgcn_s_sleep(1);
    asm volatile("global_load_dwordx4 %0, %1, %2 sc1\n\ts_waitcnt vmcnt(0)" : "=&v"(gr) : "v"(voff), "s"(base) : "memory");
  }
}

template <int V> struct IC { static constexpr int value = V; };
template <int A, int B, class F> __device__ __forceinline__ void static_for(F&& f) {
  if constexpr (A < B) { f(IC<A>{}); static_for<A + 1, B>(f); }
}
__device__ __forceinline__ void k1_xt(LAS char* smem, const unsigned short* __restrict__ x, const unsigned short* __restrict__ w_sp, char* __restrict__ ws, int v, int tid) {
  const int row = tid >> 3, c8 = tid & 7;
  const unsigned tag = __hip_atomic_load((GAS unsigned*)(ws + WS_EP + v * 128), __ATOMIC_RELAXED, __HIP_MEMORY_SCOPE_AGENT) + 1u;
  u32x4 xv[7];
#pragma unroll
  for (int m = 0; m < 7; ++m) xv[m] = *(const u32x4*)(x + (size_t)(64 * v + row) * DM + (c8 + 8 * m) * 8);
  float s = 0.f, q = 0.f;
#pragma unroll
  for (int m = 0; m < 7; ++m)
#pragma unroll
    for (int e = 0; e < 4; ++e) { const unsigned w = xv[m][e]; s = dot2bf(w, ONES_BF, s); q = dot2bf(w, w, q); }
  s = red8(s); q = red8(q);
  float mb, rr;
  ln_scalars(s, q, 1.0f / 448.0f, mb, rr);
  if (c8 == 0) {
    u32x4 gr = {__float_as_uint(mb), tag, __float_as_uint(rr), tag};
    const char* gp = ws + WS_XS1 + (size_t)v * 1024;
    const unsigned vo = row * 16;
    asm volatile("global_store_dwordx4 %0, %1, %2 sc1\n\ts_nop 1" ::"v"(vo), "v"(gr), "s"(gp) : "memory");
  }
#pragma unroll
  for (int m = 0; m < 7; ++m)
#pragma unroll
    for (int e = 0; e < 4; ++e) {
      const unsigned w = xv[m][e];
      const f32x2 n3 = up_bf(rn_pair(up_bf(rn_pair(up_bf(w) - mb)) * rr));
      const int d = (c8 + 8 * m) * 8 + 2 * e;
      *(LAS _Float16*)(smem + d * 144 + row * 2) = (_Float16)n3.x;
      *(LAS _Float16*)(smem + (d + 1) * 144 + row * 2) = (_Float16)n3.y;
    }
  __syncthreads();
#pragma unroll
  for (int m = 0; m < 7; ++m) {
    const int d = row + 64 * m;
    *(u32x4*)(ws + WS_XT + ((size_t)d * SEQ + 64 * v + 8 * c8) * 2) = *(LAS const u32x4*)(smem + d * 144 + c8 * 16);
  }
}

__global__ __launch_bounds__(512, 2) void k1_gemm1(const unsigned short* __restrict__ x, const unsigned short* __restrict__ w_in, const unsigned short* __restrict__ w_sp, char* __restrict__ ws) {
  extern __shared__ __attribute__((aligned(16))) char smem_raw[];
  LAS char* smem = (LAS char*)smem_raw;
  const int tid = threadIdx.x, lane = tid & 63;
  const int wave = __builtin_amdgcn_readfirstlane(tid >> 6);
  const int b = blockIdx.x;
  if (b >= 224) { k1_xt(smem, x, w_sp, ws, b - 224, tid); return; }
  const int xcd = b & 7, rr = b >> 3;
  const int ti = 4 * xcd + rr / 7, tj = rr % 7, rh = wave >> 2, cg = wave & 3;
  const int h = lane >> 5, l31 = lane & 31;

  STAMP(0, 0);
  unsigned ep;
  {
    const char* epp = ws + WS_EP + ti * 128;
    unsigned zero = 0;
    asm volatile("global_load_dword %0, %1, %2 sc1" : "=v"(ep) : "v"(zero), "s"(epp) : "memory");
  }
  const int xrow = 8 * wave + (lane >> 3);
  const unsigned voffx = (unsigned)(xrow * 896 + (((lane & 7) ^ ((xrow >> 1) & 7)) * 16));
  const char* xsrc = (const char*)x + (size_t)ti * 64 * 896;
  unsigned voffw[2];
#pragma unroll
  for (int e = 0; e < 2; ++e) voffw[e] = (unsigned)((8 * wave + 4 * e + (lane >> 4)) * 3584 + (((lane & 15) ^ (4 * (lane >> 4))) * 16) + tj * 256);
#define K1_XPIECE(g) glds16(xsrc + (g) * 128, voffx, (unsigned)(K1_X + (g) * 8192 + wave * 1024))
#define K1_WPIECE(G, e) \
  glds16(w_in, voffw[e] + (unsigned)(((G) % 7) * 64 * 3584 + ((G) < 7 ? 1792 : 0)), (unsigned)(K1_W + ((G) % 6) * K1_SLOT + wave * 2048 + (e) * 1024))
#pragma unroll
  for (int g = 0; g < 3; ++g) { K1_XPIECE(g); K1_WPIECE(g, 0); K1_WPIECE(g, 1); }
  LAS const char* ab[2];
  LAS const char* xb[4];
  {
    const int g4 = lane >> 4, qq = (lane & 15) >> 2, p4 = lane & 3;
    const int lb = (8 * (g4 >> 1) + qq) * 256 + (((4 * cg + 2 * (g4 & 1) + (p4 >> 1)) ^ (4 * qq)) * 16) + (p4 & 1) * 8;
    ab[0] = smem + K1_W + lb + rh * 4096;
    ab[1] = smem + K1_W + lb + (rh ^ 1) * 4096;
    const int row = 32 * rh + l31, sw = (row >> 1) & 7;
#pragma unroll
    for (int i = 0; i < 4; ++i) xb[i] = smem + K1_X + row * 128 + (((2 * (i ^ rh) + h) ^ sw) * 16);
  }
  s16x8 xa[28], wb[8];
#define K1_FRAGS(st)                                                                                                   \
  do {                                                                                                                 \
    const int G_ = (st) >> 2, i_ = (st) & 3, so_ = (G_ % 6) * K1_SLOT + (i_ >> 1) * 8192;                              \
    if ((st) < 28) xa[(st) % 28] = *(LAS const s16x8*)(xb[i_] + G_ * 8192);                                            \
    wb[(st) & 7] = __builtin_shufflevector(tr_read(ab[i_ & 1] + so_), tr_read(ab[i_ & 1] + so_ + 1024), 0, 1, 2, 3, 4, 5, 6, 7); \
  } while (0)
  f32x16 acc2 = {}, acc1 = {};
  float w1a = 0.f, w1b = 0.f;
  u32x4 g1;
  const char* g1p = ws + WS_XS1 + (size_t)ti * 1024;
  const unsigned g1o = (unsigned)(32 * rh + l31) * 16;
  unsigned zp[8];
  float m1 = 0.f, r1 = 0.f;
  asm volatile("s_waitcnt vmcnt(3)" : "+v"(ep)::"memory");
  const unsigned tag = ep + 1u;
  BAR();
  STAMP(0, 1);
  K1_FRAGS(0); K1_FRAGS(1); K1_FRAGS(2); K1_FRAGS(3);
  float ss = 0.f, sq = 0.f;
  auto step = [&](auto stc) __attribute__((always_inline)) {
    constexpr int st = decltype(stc)::value;
    constexpr int G = st >> 2, i = st & 3;
    if (i == 0 && G >= 1) {
      if (G == 1) WAIT_VM(6); else if (G == 2) WAIT_VM(9); else if (G == 3) WAIT_VM(8); else if (G == 4) WAIT_VM(7); else if (G <= 6) WAIT_VM(6);
      else if (G == 7) asm volatile("s_waitcnt vmcnt(3) lgkmcnt(0)" : "+v"(g1)::"memory");
      else if (G == 8) WAIT_VM(6);
      else if (G == 9) WAIT_VM(7); else if (G == 10) WAIT_VM(9);
      else if (G == 11) asm volatile("s_waitcnt vmcnt(7) lgkmcnt(0)" ::: "memory");
      else if (G == 12) WAIT_VM(5);
      __builtin_amdgcn_s_barrier();
      asm volatile("" ::: "memory");
      if (G == 7) STAMP(0, 3);
    }
    __builtin_amdgcn_sched_barrier(0);
    if constexpr (st < 28) acc2 = mfma32_bf(wb[st & 7], xa[st % 28], acc2); else acc1 = mfma32_bf(wb[st & 7], xa[st % 28], acc1);
    if constexpr (st + 4 < 56) K1_FRAGS(st + 4);
    if constexpr ((i & 1) == 0) {
      const u32x4 wv = __builtin_bit_cast(u32x4, wb[st & 7]);
#pragma unroll
      for (int e = 0; e < 4; ++e) { const unsigned ww = wv[e]; if (st < 28) w1a = dot2bf(ww, ONES_BF, w1a); else w1b = dot2bf(ww, ONES_BF, w1b); }
    }
    if constexpr (st == 27) {
      const float w1s = xsum32(w1a);
      if (h == 0) *(LAS float*)(smem + K1_W1 + (rh * 128 + 32 * cg + l31) * 4) = w1s;
    }
    if constexpr (st == 28) {
      poll_granule(g1, g1p, g1o, tag);
      m1 = __uint_as_float(g1[0]); r1 = __uint_as_float(g1[2]);
    }
    if constexpr (G >= 7 && G <= 10 && i == 1) {
      constexpr int g = G - 7;
      const f32x4 w1 = *(LAS const f32x4*)(smem + K1_W1 + (32 * cg + 8 * g + 4 * h) * 4) + *(LAS const f32x4*)(smem + K1_W1 + (128 + 32 * cg + 8 * g + 4 * h) * 4);
#pragma unroll
      for (int p = 0; p < 2; ++p) {
        const f32x2 a2 = {acc2[4 * g + 2 * p], acc2[4 * g + 2 * p + 1]}, w2 = {w1[2 * p], w1[2 * p + 1]};
        const unsigned zz = rn_pair(gelu2(up_bf(rn_pair((a2 - w2 * m1) * r1))));
        zp[2 * g + p] = zz;
        ss = dot2bf(zz, ONES_BF, ss); sq = dot2bf(zz, zz, sq);
      }
    }
    if constexpr (st == 43) {
      const float s2 = xsum32(ss), q2 = xsum32(sq);
      if (h == 0) *(LAS f32x2*)(smem + K1_P + ((32 * rh + l31) * 4 + cg) * 8) = (f32x2){s2, q2};
    }
    if constexpr (st == 44) {
      if (wave == 0) {
        const f32x4 p0 = *(LAS const f32x4*)(smem + K1_P + lane * 32), p1 = *(LAS const f32x4*)(smem + K1_P + lane * 32 + 16);
        const float s = (p0[0] + p0[2]) + (p1[0] + p1[2]), q = (p0[1] + p0[3]) + (p1[1] + p1[3]);
        u32x4 gr = {__float_as_uint(s), tag, __float_as_uint(q), tag};
        const char* xp = ws + WS_XCH + (size_t)ti * 64 * 128 + tj * 16;
        const unsigned vo = lane * 128;
        asm volatile("global_store_dwordx4 %0, %1, %2 sc1\n\ts_nop 1" ::"v"(vo), "v"(gr), "s"(xp) : "memory");
      }
    }
    __builtin_amdgcn_sched_barrier(0);
    if constexpr ((i & 1) == 0) {
      constexpr int ks = st >> 1;
      if constexpr (ks < 4) { K1_XPIECE(ks + 3); K1_WPIECE(ks + 3, 0); K1_WPIECE(ks + 3, 1); }
      else if constexpr (ks < 18) K1_WPIECE(5 + (ks >> 1), ks & 1);
    }
    if constexpr (st == 20) asm volatile("global_load_dwordx4 %0, %1, %2 sc1" : "=v"(g1) : "v"(g1o), "s"(g1p) : "memory");
    if constexpr (st >= 35 && st <= 39) {
      unsigned po = (unsigned)((b * 8 + wave) * 5 + (st - 35)) * 1024u;
      po = po > 8388608u - 1024u ? 8388608u - 1024u : po;
      glds16((const char*)w_sp + po, (unsigned)lane * 16u, (unsigned)(K1_X + wave * 7168 + (st - 35) * 1024));
    }
    __builtin_amdgcn_sched_barrier(0);
  };
  static_for<0, 56>(step);
#undef K1_FRAGS
#undef K1_XPIECE
#undef K1_WPIECE
  STAMP(0, 5);
  {
    const float w1s = xsum32(w1b);
    if (h == 0) *(LAS float*)(smem + K1_W1 + 1024 + (rh * 128 + 32 * cg + l31) * 4) = w1s;
  }
  BAR();
  u32x4 gr;
  const int row = tid >> 3, jj = tid & 7;
  const char* xp = ws + WS_XCH + (size_t)ti * 64 * 128;
  const unsigned vo = row * 128 + (jj < 7 ? jj : 0) * 16;
  asm volatile("global_load_dwordx4 %0, %1, %2 sc1" : "=v"(gr) : "v"(vo), "s"(xp) : "memory");
  {
    const int tt = 4 * ti + 2 * rh + (l31 >> 4);
#pragma unroll
    for (int g = 0; g < 4; ++g) {
      const f32x4 w1 = *(LAS const f32x4*)(smem + K1_W1 + 1024 + (32 * cg + 8 * g + 4 * h) * 4) + *(LAS const f32x4*)(smem + K1_W1 + 1024 + (128 + 32 * cg + 8 * g + 4 * h) * 4);
      unsigned o[2];
#pragma unroll
      for (int p = 0; p < 2; ++p) {
        const f32x2 a2 = {acc1[4 * g + 2 * p], acc1[4 * g + 2 * p + 1]}, w2 = {w1[2 * p], w1[2 * p + 1]};
        o[p] = rn_pair(gelu2(up_bf(rn_pair((a2 - w2 * m1) * r1))));
      }
      const int ft = 8 * tj + 2 * cg + (g >> 1), lp = (lane & 15) + 16 * (2 * (g & 1) + h);
      *(u32x2*)(ws + WS_Z1D + ((size_t)(tt * 56 + ft) * 64 + lp) * 8) = (u32x2){o[0], o[1]};
    }
  }
  STAMP(0, 6);
  asm volatile("s_waitcnt vmcnt(4)" : "+v"(gr)::"memory");
  poll_granule(gr, xp, vo, tag);
  {
    const float s = red8(jj < 7 ? __uint_as_float(gr[0]) : 0.f), q = red8(jj < 7 ? __uint_as_float(gr[2]) : 0.f);
    float mb, rr2;
    ln_scalars(s, q, 1.0f / 896.0f, mb, rr2);
    if (jj == 0) { *(LAS float*)(smem + K1_M2 + row * 4) = mb; *(LAS float*)(smem + K1_R2 + row * 4) = rr2; }
  }
  BAR();
  STAMP(0, 7);
  if (tj == 0 && tid == 0) __hip_atomic_store((GAS unsigned*)(ws + WS_EP + ti * 128), tag, __ATOMIC_RELAXED, __HIP_MEMORY_SCOPE_AGENT);
  {
    LAS char* tb = smem + K1_W + wave * 2304;
    const int srow = 32 * rh + l31;
    const float mb2 = *(LAS const float*)(smem + K1_M2 + srow * 4), rr2 = *(LAS const float*)(smem + K1_R2 + srow * 4);
#pragma unroll
    for (int g = 0; g < 4; ++g) {
      unsigned o[2];
#pragma unroll
      for (int p = 0; p < 2; ++p) o[p] = rn_pair(up_bf(rn_pair(up_bf(zp[2 * g + p]) - mb2)) * rr2);
      *(LAS u32x2*)(tb + l31 * 72 + (8 * g + 4 * h) * 2) = (u32x2){o[0], o[1]};
    }
    asm volatile("s_waitcnt lgkmcnt(0)" ::: "memory");
    const int q4 = lane >> 4, l15 = lane & 15;
#pragma unroll
    for (int n = 0; n < 2; ++n) {
      LAS const char* rp = tb + (8 * q4 + (l15 >> 2)) * 72 + n * 32 + (lane & 3) * 8;
      const s16x4 lo = tr_read(rp), hi = tr_read(rp + 4 * 72);
      const s16x8 o = __builtin_shufflevector(lo, hi, 0, 1, 2, 3, 4, 5, 6, 7);
      const int ft = 8 * tj + 2 * cg + n, sb = 2 * ti + rh;
      *(s16x8*)(ws + WS_Z2F + ((size_t)(ft * 64 + sb) * 64 + lane) * 16) = o;
    }
  }
  STAMP(0, 8);
}

constexpr int K2_RING = 0;
constexpr int K2_CH = 65536;
constexpr int K2_CHS = 49152;
constexpr int CH_FT = 0;
constexpr int CH_FW = 16416, CH_FS = 288;
constexpr int CH_CS = 4160;
constexpr int CH_U = 33280;
constexpr int CH_T = CH_U + 4608;
constexpr int CH_TS = 49 * 64;
constexpr int CH_CNT = CH_T + 2 * CH_TS;
constexpr int K2_LDS = 163840;

__device__ __forceinline__ void lds_sync(LAS char* cntp, unsigned target, int lane) {
  LAS unsigned* cnt = (LAS unsigned*)cntp;
  asm volatile("s_waitcnt lgkmcnt(0)" ::: "memory");
  if (lane == 0) __hip_atomic_fetch_add(cnt, 1u, __ATOMIC_RELAXED, __HIP_MEMORY_SCOPE_WORKGROUP);
  while ((unsigned)__builtin_amdgcn_readfirstlane(*(volatile LAS unsigned*)cnt) < target) __builtin_amdgcn_s_sleep(1);
  asm volatile("" ::: "memory");
}
__device__ __forceinline__ __amdgpu_buffer_rsrc_t mk_rsrc(const void* p, unsigned bytes) {
  return __builtin_amdgcn_make_buffer_rsrc((void*)p, (short)0, (int)bytes, 0x00020000);
}
__device__ __forceinline__ void bglds16(__amdgpu_buffer_rsrc_t rs, unsigned voff, unsigned soff, unsigned lds) {
  asm volatile("s_mov_b32 m0, %3\n\ts_nop 0\n\tbuffer_load_dwordx4 %0, %1, %2 offen lds" ::"v"(voff), "s"(rs), "s"(soff), "s"(lds) : "memory");
}
__device__ __forceinline__ void bload16(u32x4& dst, __amdgpu_buffer_rsrc_t rs, unsigned voff, unsigned soff) {
  asm volatile("buffer_load_dwordx4 %0, %1, %2, %3 offen" : "=v"(dst) : "v"(voff), "s"(rs), "s"(soff) : "memory");
}

template <int W>
__device__ __forceinline__ void k2_epi(LAS char* smem, char* __restrict__ ws, f32x4 (&acc)[7][4], const u32x2 (&z1v)[7], float bs, int lane, int tt, int J) {
  LAS char* cnt = smem + K2_CH + CH_CNT + 64;
  LAS char* mine = smem + K2_RING + W * 16384 + lane * 16;
  LAS char* other = smem + K2_RING + (W ^ 2) * 16384 + lane * 16;
  LAS char* o2 = smem + K2_RING + (W ^ 3) * 16384 + lane * 16;
  constexpr int K0 = W & 2, S0 = K0 ^ 2;
#pragma unroll
  for (int n = 0; n < 7; ++n) { *(LAS f32x4*)(mine + n * 1024) = acc[n][S0]; *(LAS f32x4*)(mine + (7 + n) * 1024) = acc[n][S0 + 1]; }
  lds_sync(cnt, 4, lane);
#pragma unroll
  for (int n = 0; n < 7; ++n) { acc[n][K0] += *(LAS const f32x4*)(other + n * 1024); acc[n][K0 + 1] += *(LAS const f32x4*)(other + (7 + n) * 1024); }
  asm volatile("s_waitcnt lgkmcnt(0)" ::: "memory");
#pragma unroll
  for (int n = 0; n < 7; ++n) *(LAS f32x4*)(other + n * 1024) = acc[n][W ^ 1];
  lds_sync(cnt, 8, lane);
  STAMP(1, 2);
  unsigned hp[7][2];
#pragma unroll
  for (int n = 0; n < 7; ++n) {
    const f32x4 v = acc[n][W] + *(LAS const f32x4*)(o2 + n * 1024);
#pragma unroll
    for (int p = 0; p < 2; ++p) {
      const f32x2 g0 = up_bf(pk_bf16(v[2 * p], v[2 * p + 1])) + bs;
      const unsigned zw = z1v[n][p];
      const f32x2 h = up_bf(pk_bf16(g0.x, g0.y)) * up_bf(zw);
      hp[n][p] = pk_bf16(h.x, h.y);
    }
  }
  char* hb = ws + WS_HF + ((size_t)(tt * 28 + (7 * J >> 1)) * 64 + lane) * 16;
  if (!(J & 1)) {
#pragma unroll
    for (int p = 0; p < 3; ++p) *(u32x4*)(hb + p * 1024) = (u32x4){hp[2 * p][0], hp[2 * p][1], hp[2 * p + 1][0], hp[2 * p + 1][1]};
    *(u32x2*)(hb + 3 * 1024) = (u32x2){hp[6][0], hp[6][1]};
  } else {
    *(u32x2*)(hb + 8) = (u32x2){hp[0][0], hp[0][1]};
#pragma unroll
    for (int p = 0; p < 3; ++p) *(u32x4*)(hb + (p + 1) * 1024) = (u32x4){hp[2 * p + 1][0], hp[2 * p + 1][1], hp[2 * p + 2][0], hp[2 * p + 2][1]};
  }
}

__device__ __forceinline__ void k2_gemm(LAS char* smem, const unsigned short* __restrict__ w_sp, const unsigned short* __restrict__ b_sp, char* __restrict__ ws,
                                        int wave, int lane, int T, int J, int rot) {
  const __amdgpu_buffer_rsrc_t zs = mk_rsrc(ws + WS_Z2F + (size_t)(7 * J * 64 + 16 * wave) * 1024, 7 * 65536 - 16 * wave * 1024);
  const __amdgpu_buffer_rsrc_t wr = mk_rsrc((const char*)w_sp + (size_t)T * 64 * 4096 + wave * 1024, 64 * 4096 - wave * 1024);
  const unsigned voffz = lane * 16;
  const unsigned voffw0 = (lane >> 3) * 4096 + (((lane & 7) ^ ((lane >> 4) & 3)) * 16);
  const unsigned voffw1 = (8 + (lane >> 3)) * 4096 + (((lane & 7) ^ (4 + ((lane >> 4) & 3))) * 16);
  const unsigned ring = K2_RING + wave * 16384;
  LAS const char* wrd0 = smem + ring + (lane & 15) * 128 + (((lane >> 4) ^ ((lane >> 1) & 7)) * 16);
  LAS const char* wrd1 = smem + ring + (lane & 15) * 128 + (((4 + (lane >> 4)) ^ ((lane >> 1) & 7)) * 16);
  u32x4 z[3][7];
  f32x4 acc[7][4];
#pragma unroll
  for (int n = 0; n < 7; ++n)
#pragma unroll
    for (int m = 0; m < 4; ++m) acc[n][m] = (f32x4){0.f, 0.f, 0.f, 0.f};
#define K2_ISSUE_W(pp)                                                                                      \
  do {                                                                                                      \
    const unsigned kw_ = (unsigned)((2 * (pp) + rot) & 15) * 64;                                            \
    _Pragma("unroll") for (int m = 0; m < 4; ++m) {                                                         \
      bglds16(wr, voffw0, m * 65536 + kw_, ring + ((pp) & 1) * 8192 + m * 2048);                            \
      bglds16(wr, voffw1, m * 65536 + kw_, ring + ((pp) & 1) * 8192 + m * 2048 + 1024);                     \
    }                                                                                                       \
  } while (0)
#define K2_ISSUE_Z(it)                                                                                      \
  do {                                                                                                      \
    const unsigned kz_ = (unsigned)(((it) + rot) & 15) * 1024;                                              \
    _Pragma("unroll") for (int n = 0; n < 7; ++n) bload16(z[(it) % 3][n], zs, voffz, n * 65536 + kz_);      \
  } while (0)
  STAMP(1, 0);
  K2_ISSUE_W(0); K2_ISSUE_Z(0); K2_ISSUE_Z(1); K2_ISSUE_W(1); K2_ISSUE_Z(2);
  const int tt = 4 * T + wave;
  u32x2 z1v[7];
  unsigned bsw;
#define K2_WAITZ(N) asm volatile("s_waitcnt vmcnt(" #N ")" : "+v"(zc[0]), "+v"(zc[1]), "+v"(zc[2]), "+v"(zc[3]), "+v"(zc[4]), "+v"(zc[5]), "+v"(zc[6])::"memory")
#pragma unroll
  for (int it = 0; it < 16; ++it) {
    u32x4(&zc)[7] = z[it % 3];
    if (it <= 13) K2_WAITZ(22); else if (it == 14) K2_WAITZ(15); else K2_WAITZ(8);
    s16x8 wf[4];
#pragma unroll
    for (int m = 0; m < 4; ++m) wf[m] = *(LAS const s16x8*)(((it & 1) ? wrd1 : wrd0) + ((it >> 1) & 1) * 8192 + m * 2048);
    const unsigned kz = (unsigned)((it + 3 + rot) & 15) * 1024, kw = (unsigned)((it + 3 + rot) & 15) * 64;
#pragma unroll
    for (int n = 0; n < 7; ++n) {
#pragma unroll
      for (int m = 0; m < 4; ++m) acc[n][m] = mfma_bf(__builtin_bit_cast(s16x8, zc[n]), wf[m], acc[n][m]);
      __builtin_amdgcn_sched_barrier(0);
      if (it + 3 < 16) {
        bload16(zc[n], zs, voffz, n * 65536 + kz);
        if ((it & 1) && n >= 1 && n <= 4) {
          const int m = n - 1;
          bglds16(wr, voffw0, m * 65536 + kw, ring + (((it + 3) >> 1) & 1) * 8192 + m * 2048);
          bglds16(wr, voffw1, m * 65536 + kw, ring + (((it + 3) >> 1) & 1) * 8192 + m * 2048 + 1024);
        }
      }
      if (it == 13) {
        const char* zb = ws + WS_Z1D + (size_t)(tt * 56 + 7 * J + n) * 512;
        const unsigned vo = lane * 8;
        asm volatile("global_load_dwordx2 %0, %1, %2" : "=v"(z1v[n]) : "v"(vo), "s"(zb) : "memory");
        if (n == 6) {
          const unsigned vb = (unsigned)(16 * tt + (lane & 15)) * 2u;
          asm volatile("global_load_ushort %0, %1, %2" : "=v"(bsw) : "v"(vb), "s"(b_sp) : "memory");
        }
      }
      __builtin_amdgcn_sched_barrier(0);
    }
  }
#undef K2_WAITZ
#undef K2_ISSUE_W
#undef K2_ISSUE_Z
  STAMP(1, 1);
  asm volatile("s_waitcnt vmcnt(0)" : "+v"(z1v[0]), "+v"(z1v[1]), "+v"(z1v[2]), "+v"(z1v[3]), "+v"(z1v[4]), "+v"(z1v[5]), "+v"(z1v[6]), "+v"(bsw)::"memory");
  const float bs = bf2f((unsigned short)bsw);
  if (wave == 0) k2_epi<0>(smem, ws, acc, z1v, bs, lane, tt, J);
  else if (wave == 1) k2_epi<1>(smem, ws, acc, z1v, bs, lane, tt, J);
  else if (wave == 2) k2_epi<2>(smem, ws, acc, z1v, bs, lane, tt, J);
  else k2_epi<3>(smem, ws, acc, z1v, bs, lane, tt, J);
  STAMP(1, 3);
}

__device__ __forceinline__ f32x2 cmul(f32x2 a, f32x2 b) {
  f32x2 t, r;
  asm("v_pk_mul_f32 %0, %1, %2 op_sel:[0,0] op_sel_hi:[0,1]" : "=v"(t) : "v"(a), "v"(b));
  asm("v_pk_fma_f32 %0, %1, %2, %3 op_sel:[1,1,0] op_sel_hi:[1,0,1] neg_lo:[1,0,0]" : "=v"(r) : "v"(a), "v"(b), "v"(t));
  return r;
}
__device__ __forceinline__ unsigned hmul(unsigned a, unsigned b) {
  unsigned t, r;
  asm("v_pk_mul_f16 %0, %1, %2 op_sel:[0,0] op_sel_hi:[0,1]" : "=v"(t) : "v"(a), "v"(b));
  asm("v_pk_fma_f16 %0, %1, %2, %3 op_sel:[1,1,0] op_sel_hi:[1,0,1] neg_lo:[1,0,0]" : "=v"(r) : "v"(a), "v"(b), "v"(t));
  return r;
}
__device__ __forceinline__ unsigned hcv(f32x2 v) { return pk_f16(v.x, v.y); }
__device__ __forceinline__ f32x2 cexp_p(float al, float be, float p) {
  const float e = __builtin_amdgcn_exp2f(al * p), th = __builtin_amdgcn_fractf(be * p);
  return (f32x2){__builtin_amdgcn_cosf(th), __builtin_amdgcn_sinf(th)} * e;
}
__device__ __forceinline__ f32x4 mfma_h(u32x4 a, u32x4 b, f32x4 c) {
  return __builtin_amdgcn_mfma_f32_16x16x32_f16(__builtin_bit_cast(f16x8, a), __builtin_bit_cast(f16x8, b), c, 0, 0, 0);
}
struct PosTab { unsigned short v[2064]; };
constexpr int pos_bf16(int j) {
  if (j < 256) return j;
  int sh = 1; while ((j >> sh) >= 256) ++sh;
  const int q = j >> sh, r = j & ((1 << sh) - 1), h = 1 << (sh - 1);
  return (r > h || (r == h && (q & 1)) ? q + 1 : q) << sh;
}
constexpr PosTab make_postab() {
  PosTab t = {};
  for (int x = 0; x < 2048; ++x) {
    const int p = pos_bf16(2047 - x);
    int col = 0, a = 0;
    if (p < 256) { a = p >> 4; col = p & 15; }
    else if (p < 512) { const int r = p - 256; a = r >> 4; col = 16 + ((r & 15) >> 1); }
    else if (p < 1024) { const int r = p - 512; a = (r & 255) >> 4; col = 24 + 4 * (r >> 8) + ((r & 15) >> 2); }
    else if (p < 2048) { const int r = p - 1024; a = (r & 255) >> 4; col = 32 + 2 * (r >> 8) + ((r & 15) >> 3); }
    else { a = 0; col = 40; }
    t.v[x] = (unsigned short)((col * 16 + a) * 4);
  }
  for (int x = 2048; x < 2064; ++x) t.v[x] = 48 * 64;
  return t;
}
__device__ const PosTab POSTAB = make_postab();

template <int HW>
__device__ __forceinline__ void s4d_conv(LAS char* cb, char* __restrict__ ws, int d, int lane, float iscale, float dsk) {
  constexpr int NF = HW ? 7 : 8;
  f32x4 acc[8] = {};
  const int r = lane & 15, h = lane >> 4;
  LAS const char* ab = cb + CH_FT + ((7 - r) & 7) * CH_CS + (255 - (r >> 3) - 2 * (h >> 1) + (h & 1)) * 16 - (64 * 7 + 512 * 7);
  LAS const char* bb = cb + CH_U + 512 + 32 * r - 32 * (h >> 1) + 16 * (h & 1) - 64 * 7;
  u32x4 A[2][8], B[2][8];
#define CONV_LOAD(ar, buf)                                                                                   \
  _Pragma("unroll") for (int k = 0; k < NF; ++k) {                                                           \
    B[buf][k] = *(LAS const u32x4*)(bb + 512 * k + 64 * (7 - (ar)));                                         \
    A[buf][k] = *(LAS const u32x4*)(ab + 512 * (7 - k) + 64 * (7 - (ar)));                                   \
  }
  CONV_LOAD(0, 0);
#pragma unroll
  for (int ar = 0; ar < 8; ++ar) {
    __builtin_amdgcn_sched_barrier(0);
    if (ar < 7) { CONV_LOAD(ar + 1, (ar + 1) & 1); }
#pragma unroll
    for (int al = 0; al < NF; ++al)
#pragma unroll
      for (int be = 0; be < NF; ++be) {
        const int tau = al + be;
        if (HW ? (tau >= 4 && tau <= 6) : (tau <= 3 || tau == 7)) acc[tau] = mfma_h(A[ar & 1][al], B[ar & 1][be], acc[tau]);
      }
#pragma unroll
    for (int i = 0; i < 16; ++i) { __builtin_amdgcn_sched_group_barrier(0x008, 1, 0); __builtin_amdgcn_sched_group_barrier(0x100, 1, 0); }
    __builtin_amdgcn_sched_barrier(0);
  }
#undef CONV_LOAD
  STAMP(1, 4);
  const int q = lane >> 4;
#pragma unroll
  for (int t = 0; t < 8; ++t) {
    if (HW ? (t >= 4 && t <= 6) : (t <= 3 || t == 7)) {
      const f32x4 v = acc[t];
      const int l0 = 256 * t + 16 * r + 4 * q;
      const u32x2 uw = *(LAS const u32x2*)(cb + CH_U + 512 + l0 * 2);
      const unsigned uw0 = uw[0], uw1 = uw[1];
      const f16x2 ua = __builtin_bit_cast(f16x2, uw0), ub = __builtin_bit_cast(f16x2, uw1);
      const float uu[4] = {(float)ua[0], (float)ua[1], (float)ub[0], (float)ub[1]};
      float sv[4];
#pragma unroll
      for (int j = 0; j < 4; ++j) sv[j] = rbf(v[j] * iscale) + rbf(uu[j] * dsk);
      *(u32x2*)(ws + WS_ST + ((size_t)d * SEQ + l0) * 2) = (u32x2){pk_bf16(sv[0], sv[1]), pk_bf16(sv[2], sv[3])};
    }
  }
}

__device__ __forceinline__ void k2_s4d(LAS char* cb, const unsigned short* __restrict__ logA, const unsigned short* __restrict__ Aim, const unsigned short* __restrict__ Cre,
                                       const unsigned short* __restrict__ Cim, const unsigned short* __restrict__ logdt, const unsigned short* __restrict__ Dsk,
                                       char* __restrict__ ws, int d, int hw, int lane) {
  LAS char* cnt = cb + CH_CNT;
  unsigned la_[2], ai_[2], cr_[4], ci_[4], ldt_, dsk_;
  u32x4 u0, u1, pt0, pt1, pt2;
  {
    const unsigned vo = (unsigned)(d * NST + 128 * hw + lane) * 2u, vo2 = (unsigned)(d * NST + ((128 * hw + 128 + lane) & 255)) * 2u, vd = (unsigned)d * 2u;
    asm volatile("global_load_ushort %0, %1, %2" : "=v"(la_[0]) : "v"(vo), "s"(logA) : "memory");
    asm volatile("global_load_ushort %0, %1, %2 offset:128" : "=v"(la_[1]) : "v"(vo), "s"(logA) : "memory");
    asm volatile("global_load_ushort %0, %1, %2" : "=v"(ai_[0]) : "v"(vo), "s"(Aim) : "memory");
    asm volatile("global_load_ushort %0, %1, %2 offset:128" : "=v"(ai_[1]) : "v"(vo), "s"(Aim) : "memory");
    asm volatile("global_load_ushort %0, %1, %2" : "=v"(cr_[0]) : "v"(vo), "s"(Cre) : "memory");
    asm volatile("global_load_ushort %0, %1, %2 offset:128" : "=v"(cr_[1]) : "v"(vo), "s"(Cre) : "memory");
    asm volatile("global_load_ushort %0, %1, %2" : "=v"(cr_[2]) : "v"(vo2), "s"(Cre) : "memory");
    asm volatile("global_load_ushort %0, %1, %2 offset:128" : "=v"(cr_[3]) : "v"(vo2), "s"(Cre) : "memory");
    asm volatile("global_load_ushort %0, %1, %2" : "=v"(ci_[0]) : "v"(vo), "s"(Cim) : "memory");
    asm volatile("global_load_ushort %0, %1, %2 offset:128" : "=v"(ci_[1]) : "v"(vo), "s"(Cim) : "memory");
    asm volatile("global_load_ushort %0, %1, %2" : "=v"(ci_[2]) : "v"(vo2), "s"(Cim) : "memory");
    asm volatile("global_load_ushort %0, %1, %2 offset:128" : "=v"(ci_[3]) : "v"(vo2), "s"(Cim) : "memory");
    asm volatile("global_load_ushort %0, %1, %2" : "=v"(ldt_) : "v"(vd), "s"(logdt) : "memory");
    asm volatile("global_load_ushort %0, %1, %2" : "=v"(dsk_) : "v"(vd), "s"(Dsk) : "memory");
    const char* ub = ws + WS_XT + (size_t)d * SEQ * 2 + hw * 2048;
    const unsigned vu = lane * 16;
    asm volatile("global_load_dwordx4 %0, %1, %2" : "=v"(u0) : "v"(vu), "s"(ub) : "memory");
    asm volatile("global_load_dwordx4 %0, %1, %2 offset:1024" : "=v"(u1) : "v"(vu), "s"(ub) : "memory");
    const char* pb = (const char*)POSTAB.v + hw * 2048;
    const unsigned vp = lane * 32;
    asm volatile("global_load_dwordx4 %0, %1, %2" : "=v"(pt0) : "v"(vp), "s"(pb) : "memory");
    asm volatile("global_load_dwordx4 %0, %1, %2 offset:16" : "=v"(pt1) : "v"(vp), "s"(pb) : "memory");
    asm volatile("global_load_dwordx4 %0, %1, %2 offset:32" : "=v"(pt2) : "v"(vp), "s"(pb) : "memory");
    if (hw == 0 && lane < 32) *(LAS u32x4*)(cb + CH_U + lane * 16) = (u32x4){0u, 0u, 0u, 0u};
    if (lane < 4) *(LAS u32x4*)(cb + CH_T + hw * CH_TS + 48 * 64 + lane * 16) = (u32x4){0u, 0u, 0u, 0u};
  }
  BAR();
#define K2_PRIO 3
  __builtin_amdgcn_s_setprio(K2_PRIO);
  STAMP(1, 0);
  asm volatile("s_waitcnt vmcnt(0)" : "+v"(la_[0]), "+v"(la_[1]), "+v"(ai_[0]), "+v"(ai_[1]), "+v"(cr_[0]), "+v"(cr_[1]), "+v"(cr_[2]), "+v"(cr_[3]), "+v"(ci_[0]), "+v"(ci_[1]),
               "+v"(ci_[2]), "+v"(ci_[3]), "+v"(ldt_), "+v"(dsk_), "+v"(u0), "+v"(u1), "+v"(pt0), "+v"(pt1), "+v"(pt2)::"memory");
  float cr[4], ci[4];
#pragma unroll
  for (int p = 0; p < 4; ++p) { cr[p] = bf2f((unsigned short)cr_[p]); ci[p] = bf2f((unsigned short)ci_[p]); }
  const float dt = rbf(__builtin_amdgcn_exp2f(bf2f((unsigned short)ldt_) * 1.4426950408889634f)), dsk = bf2f((unsigned short)dsk_);
  *(LAS u32x4*)(cb + CH_U + 512 + hw * 2048 + lane * 16) = u0;
  *(LAS u32x4*)(cb + CH_U + 512 + hw * 2048 + 1024 + lane * 16) = u1;
  float sabs = 0.f;
#pragma unroll
  for (int p = 0; p < 4; ++p) sabs += __builtin_fabsf(cr[p]) + __builtin_fabsf(ci[p]);
  sabs = xsum16_32(red16(sabs));
  sabs *= 2.0f * dt;
  int ex = (int)((__float_as_uint(sabs) >> 23) & 0xff) - 126;
  ex = ex < -100 ? -100 : (ex > 100 ? 100 : ex);
  const float scale = __uint_as_float((unsigned)(127 + 15 - ex) << 23), iscale = __uint_as_float((unsigned)(127 - 15 + ex) << 23);
  STAMP(1, 1);
  f32x4 kacc[3] = {};
  LAS char* fw = cb + CH_FT + hw * CH_FW;
#pragma unroll
  for (int p = 0; p < 2; ++p) {
    const float lap = bf2f((unsigned short)(p ? la_[1] : la_[0])), aip = bf2f((unsigned short)(p ? ai_[1] : ai_[0]));
    const float crp = p ? cr[1] : cr[0], cip = p ? ci[1] : ci[0];
    const float lre = -rbf(__builtin_amdgcn_exp2f(lap * 1.4426950408889634f)) * dt, lim = aip * dt;
    const float al = lre * 1.4426950408889634f, be = lim * 0.15915494309189535f;
    LAS char* wp = fw + lane * 4;
#define PUTH(row, v) *(LAS unsigned*)(wp + (row) * CH_FS) = (v)
    {
      const f32x2 w = cexp_p(al, be, 16.f);
      const float cs = 2.0f * dt * scale;
      const f32x2 c0 = {crp * cs, cip * cs};
      const f32x2 w2 = cmul(w, w), c1 = cmul(c0, w), w4 = cmul(w2, w2), c2 = cmul(c0, w2), c3 = cmul(c1, w2), w8 = cmul(w4, w4), w12 = cmul(w8, w4);
      const unsigned hc[4] = {hcv(c0), hcv(c1), hcv(c2), hcv(c3)}, hw4[3] = {hcv(w4), hcv(w8), hcv(w12)};
#pragma unroll
      for (int j = 0; j < 4; ++j) {
        PUTH(j, hc[j]);
#pragma unroll
        for (int k = 0; k < 3; ++k) PUTH(4 * (k + 1) + j, hmul(hc[j], hw4[k]));
      }
    }
    {
      const f32x2 e1 = cexp_p(al, -be, 1.f);
      const f32x2 e2 = cmul(e1, e1), e3 = cmul(e2, e1), e4 = cmul(e2, e2), e8 = cmul(e4, e4), e12 = cmul(e8, e4);
      unsigned E[16];
      E[0] = 0x00003c00u; E[1] = hcv(e1); E[2] = hcv(e2); E[3] = hcv(e3); E[4] = hcv(e4); E[8] = hcv(e8); E[12] = hcv(e12);
#pragma unroll
      for (int m = 1; m < 4; ++m)
#pragma unroll
        for (int j = 1; j < 4; ++j) E[4 * m + j] = hmul(E[4 * m], E[j]);
#pragma unroll
      for (int k = 0; k < 16; ++k) PUTH(16 + k, E[k]);
      const f32x2 z256 = cexp_p(al, -be, 256.f), z512 = cexp_p(al, -be, 512.f), z1024 = cexp_p(al, -be, 1024.f), z2048 = cexp_p(al, -be, 2048.f);
      const f32x2 z768 = cmul(z512, z256);
      const unsigned h256 = hcv(z256), h512 = hcv(z512), h768 = hcv(z768);
      PUTH(32, h256);
#pragma unroll
      for (int k = 1; k < 8; ++k) PUTH(32 + k, hmul(h256, E[2 * k]));
      PUTH(40, h512); PUTH(44, h768);
#pragma unroll
      for (int k = 1; k < 4; ++k) { PUTH(40 + k, hmul(h512, E[4 * k])); PUTH(44 + k, hmul(h768, E[4 * k])); }
      const unsigned hb[4] = {hcv(z1024), hcv(cmul(z1024, z256)), hcv(cmul(z1024, z512)), hcv(cmul(z1024, z768))};
#pragma unroll
      for (int k = 0; k < 4; ++k) { PUTH(48 + 2 * k, hb[k]); PUTH(49 + 2 * k, hmul(hb[k], E[8])); }
      PUTH(56, hcv(z2048));
    }
#undef PUTH
    asm volatile("s_waitcnt lgkmcnt(0)" ::: "memory");
    LAS const char* rp = fw + (lane & 15) * CH_FS + (lane >> 4) * 16;
    u32x4 fa[4], fb[4][3];
#pragma unroll
    for (int kk = 0; kk < 4; ++kk) {
      fa[kk] = *(LAS const u32x4*)(rp + kk * 64);
#pragma unroll
      for (int t = 0; t < 3; ++t) fb[kk][t] = *(LAS const u32x4*)(rp + (16 + 16 * t) * CH_FS + kk * 64);
    }
#pragma unroll
    for (int kk = 0; kk < 4; ++kk)
#pragma unroll
      for (int t = 0; t < 3; ++t) kacc[t] = mfma_h(fa[kk], fb[kk][t], kacc[t]);
    asm volatile("" ::: "memory");
  }
  STAMP(1, 2);
#pragma unroll
  for (int t = 0; t < 3; ++t) *(LAS f32x4*)(cb + CH_T + hw * CH_TS + (16 * t + (lane & 15)) * 64 + (lane >> 4) * 16) = kacc[t];
  lds_sync(cnt, 2, lane);
  {
    const int y = 2 * (64 * hw + lane);
    const unsigned po[12] = {pt0[0], pt0[1], pt0[2], pt0[3], pt1[0], pt1[1], pt1[2], pt1[3], pt2[0], pt2[1], pt2[2], pt2[3]};
    unsigned W[12], V[11];
#pragma unroll
    for (int k = 0; k < 12; ++k) {
      LAS const char* a0 = cb + CH_T + (po[k] & 0xffffu);
      LAS const char* a1 = cb + CH_T + (po[k] >> 16);
      W[k] = pk_f16(*(LAS const float*)a0 + *(LAS const float*)(a0 + CH_TS), *(LAS const float*)a1 + *(LAS const float*)(a1 + CH_TS));
    }
#pragma unroll
    for (int i = 0; i < 11; ++i) V[i] = __builtin_amdgcn_alignbit(W[i + 1], W[i], 16);
#pragma unroll
    for (int m = 0; m < 4; ++m) {
      *(LAS u32x4*)(cb + CH_FT + (2 * m) * CH_CS + y * 16) = (u32x4){W[m], W[m + 1], W[m + 2], W[m + 3]};
      *(LAS u32x4*)(cb + CH_FT + (2 * m) * CH_CS + y * 16 + 16) = (u32x4){W[m + 4], W[m + 5], W[m + 6], W[m + 7]};
      *(LAS u32x4*)(cb + CH_FT + (2 * m + 1) * CH_CS + y * 16) = (u32x4){V[m], V[m + 1], V[m + 2], V[m + 3]};
      *(LAS u32x4*)(cb + CH_FT + (2 * m + 1) * CH_CS + y * 16 + 16) = (u32x4){V[m + 4], V[m + 5], V[m + 6], V[m + 7]};
    }
    if (hw == 1 && lane < 8) *(LAS u32x4*)(cb + CH_FT + lane * CH_CS + 256 * 16) = (u32x4){0u, 0u, 0u, 0u};
  }
  lds_sync(cnt, 4, lane);
  STAMP(1, 3);
  if (hw) s4d_conv<1>(cb, ws, d, lane, iscale, dsk); else s4d_conv<0>(cb, ws, d, lane, iscale, dsk);
  STAMP(1, 5);
}

__global__ __launch_bounds__(512, 2) void k2_mix(const unsigned short* __restrict__ w_sp, const unsigned short* __restrict__ b_sp, const unsigned short* __restrict__ logA,
                                                 const unsigned short* __restrict__ Aim, const unsigned short* __restrict__ Cre, const unsigned short* __restrict__ Cim,
                                                 const unsigned short* __restrict__ logdt, const unsigned short* __restrict__ Dsk, const unsigned short* __restrict__ w_out,
                                                 char* __restrict__ ws) {
  extern __shared__ __attribute__((aligned(16))) char smem_raw[];
  LAS char* smem = (LAS char*)smem_raw;
  const int tid = threadIdx.x, lane = tid & 63;
  const int wave = __builtin_amdgcn_readfirstlane(tid >> 6);
  const int b = blockIdx.x, xcd = b & 7, rr = b >> 3;
  if (tid < 64) {
    if (lane < 32) *(LAS unsigned*)(smem + K2_CH + CH_CNT + lane * 4) = 0u;
    else *(LAS unsigned*)(smem + K2_CH + K2_CHS + CH_CNT + (lane - 32) * 4) = 0u;
  }
  if (wave < 4) {
    BAR();
    const int T = 8 * (xcd >> 1) + (rr & 7), J = 4 * (xcd & 1) + (rr >> 3);
#define K2_ROT 0
    k2_gemm(smem, w_sp, b_sp, ws, wave, lane, T, J, K2_ROT == 1 ? (2 * (rr & 7) + 4 * (rr >> 3)) & 15 : K2_ROT == 2 ? 2 * (rr & 1) : K2_ROT == 3 ? (2 * J) & 15 : K2_ROT == 4 ? 4 * (rr >> 3) : K2_ROT == 5 ? 2 * (rr >> 3) : 0);
  } else {
    const int sw = wave - 4;
    if (b < 224) {
      k2_s4d(smem + K2_CH + (sw >> 1) * K2_CHS, logA, Aim, Cre, Cim, logdt, Dsk, ws, 2 * b + (sw >> 1), sw & 1, lane);
    } else {
      BAR();
      for (int fr = (b - 224) * 4 + sw; fr < 784; fr += 128) {
        const int dt = fr / 28, fb = fr - dt * 28;
        const int dcol = 16 * dt + (lane & 15), q = lane >> 4;
        unsigned short v[8];
#pragma unroll
        for (int jj = 0; jj < 8; ++jj) v[jj] = w_out[(size_t)(32 * fb + 16 * (jj >> 2) + 4 * q + (jj & 3)) * DM + dcol];
        u32x4 o = {(unsigned)v[0] | ((unsigned)v[1] << 16), (unsigned)v[2] | ((unsigned)v[3] << 16), (unsigned)v[4] | ((unsigned)v[5] << 16), (unsigned)v[6] | ((unsigned)v[7] << 16)};
        *(u32x4*)(ws + WS_WOF + ((size_t)fr * 64 + lane) * 16) = o;
      }
    }
  }
}

constexpr int K3_PART = 0;
constexpr int K3_XS = 65536;
constexpr int K3_SS = K3_XS + 64 * 136;
constexpr int K3_OS = K3_SS + 64 * 136;
constexpr int K3_LDS = K3_OS + 64 * 144;
__global__ __launch_bounds__(512, 2) void k3_out(const unsigned short* __restrict__ x, char* __restrict__ ws, unsigned short* __restrict__ out) {
  extern __shared__ __attribute__((aligned(16))) char smem_raw[];
  LAS char* smem = (LAS char*)smem_raw;
  const int tid = threadIdx.x, lane = tid & 63;
  const int wave = __builtin_amdgcn_readfirstlane(tid >> 6);
  const int b = blockIdx.x, xcd = b & 7, rr = b >> 3;
  const int T = 4 * xcd + rr / 7, Dt = rr % 7;
  STAMP(2, 0);
  if (wave < 4) {
    const u32x4* hp = (const u32x4*)(ws + WS_HF + ((size_t)(4 * T * 28 + 7 * wave) * 64 + lane) * 16);
    const u32x4* wp = (const u32x4*)(ws + WS_WOF + ((size_t)(4 * Dt * 28 + 7 * wave) * 64 + lane) * 16);
    u32x4 hf[7][4], wf[7][4];
    f32x4 acc[4][4];
#pragma unroll
    for (int n = 0; n < 4; ++n)
#pragma unroll
      for (int m = 0; m < 4; ++m) acc[n][m] = (f32x4){0.f, 0.f, 0.f, 0.f};
#pragma unroll
    for (int k = 0; k < 5; ++k)
#pragma unroll
      for (int m = 0; m < 4; ++m) { hf[k][m] = hp[(m * 28 + k) * 64]; wf[k][m] = wp[(m * 28 + k) * 64]; }
    __builtin_amdgcn_sched_barrier(0);
#pragma unroll
    for (int k = 0; k < 7; ++k) {
#pragma unroll
      for (int n = 0; n < 4; ++n)
#pragma unroll
        for (int m = 0; m < 4; ++m) acc[n][m] = mfma_bf(__builtin_bit_cast(s16x8, wf[k][n]), __builtin_bit_cast(s16x8, hf[k][m]), acc[n][m]);
      __builtin_amdgcn_sched_barrier(0);
      if (k + 5 < 7) {
#pragma unroll
        for (int m = 0; m < 4; ++m) { hf[k + 5][m] = hp[(m * 28 + k + 5) * 64]; wf[k + 5][m] = wp[(m * 28 + k + 5) * 64]; }
      }
      __builtin_amdgcn_sched_barrier(0);
    }
    STAMP(2, 1);
#pragma unroll
    for (int n = 0; n < 4; ++n)
#pragma unroll
      for (int m = 0; m < 4; ++m) *(LAS f32x4*)(smem + K3_PART + ((wave * 4 + m) * 4 + n) * 1024 + lane * 16) = acc[n][m];
  } else {
    const int u = tid - 256, row = u >> 2, c = u & 3;
    const u32x4 s0 = *(const u32x4*)(ws + WS_ST + ((size_t)(64 * Dt + row) * SEQ + 64 * T + 16 * c) * 2);
    const u32x4 s1 = *(const u32x4*)(ws + WS_ST + ((size_t)(64 * Dt + row) * SEQ + 64 * T + 16 * c + 8) * 2);
    const u32x4 x0 = *(const u32x4*)(x + (size_t)(64 * T + row) * DM + 64 * Dt + 16 * c);
    const u32x4 x1 = *(const u32x4*)(x + (size_t)(64 * T + row) * DM + 64 * Dt + 16 * c + 8);
#pragma unroll
    for (int e = 0; e < 2; ++e) {
      *(LAS u32x2*)(smem + K3_SS + row * 136 + c * 32 + e * 8) = (u32x2){s0[2 * e], s0[2 * e + 1]};
      *(LAS u32x2*)(smem + K3_SS + row * 136 + c * 32 + 16 + e * 8) = (u32x2){s1[2 * e], s1[2 * e + 1]};
      *(LAS u32x2*)(smem + K3_XS + row * 136 + c * 32 + e * 8) = (u32x2){x0[2 * e], x0[2 * e + 1]};
      *(LAS u32x2*)(smem + K3_XS + row * 136 + c * 32 + 16 + e * 8) = (u32x2){x1[2 * e], x1[2 * e + 1]};
    }
  }
  BAR();
  {
    const int m = wave & 3, l15 = lane & 15, q = lane >> 4, t = 16 * m + l15;
#pragma unroll
    for (int nn = 0; nn < 2; ++nn) {
      const int n = 2 * (wave >> 2) + nn;
      f32x4 o = *(LAS const f32x4*)(smem + K3_PART + ((0 * 4 + m) * 4 + n) * 1024 + lane * 16);
#pragma unroll
      for (int sw = 1; sw < 4; ++sw) o += *(LAS const f32x4*)(smem + K3_PART + ((sw * 4 + m) * 4 + n) * 1024 + lane * 16);
      const s16x4 sv = tr_read(smem + K3_SS + (16 * n + 4 * q + (l15 >> 2)) * 136 + (16 * m + 4 * (lane & 3)) * 2);
      const u32x2 sw2 = __builtin_bit_cast(u32x2, sv);
      const u32x2 xw = *(LAS const u32x2*)(smem + K3_XS + t * 136 + (16 * n + 4 * q) * 2);
      unsigned ow[2];
#pragma unroll
      for (int p = 0; p < 2; ++p) {
        const f32x2 v = up_bf(rn_pair((f32x2){o[2 * p], o[2 * p + 1]}));
        const f32x2 e = v * -1.4426950408889634f;
        const f32x2 dd = (f32x2){__builtin_amdgcn_exp2f(e.x), __builtin_amdgcn_exp2f(e.y)} + 1.0f;
        const f32x2 g = up_bf(rn_pair(v * (f32x2){__builtin_amdgcn_rcpf(dd.x), __builtin_amdgcn_rcpf(dd.y)}));
        const unsigned s_ = sw2[p], x_ = xw[p];
        ow[p] = rn_pair(up_bf(x_) + up_bf(rn_pair(up_bf(s_) * g)));
      }
      *(LAS u32x2*)(smem + K3_OS + t * 144 + (16 * n + 4 * q) * 2) = (u32x2){ow[0], ow[1]};
    }
  }
  BAR();
  {
    const int row = tid >> 3, c = tid & 7;
    *(u32x4*)(out + (size_t)(64 * T + row) * DM + 64 * Dt + 8 * c) = *(LAS const u32x4*)(smem + K3_OS + row * 144 + c * 16);
  }
  STAMP(2, 2);
}

extern "C" void kernel_launch(void* const* d_in, const int* in_sizes, int n_in, void* d_out, int out_size, void* d_ws, size_t ws_size, hipStream_t stream) {
  const unsigned short* x = (const unsigned short*)d_in[0];
  const unsigned short* logA = (const unsigned short*)d_in[1];
  const unsigned short* Aim = (const unsigned short*)d_in[2];
  const unsigned short* Cre = (const unsigned short*)d_in[3];
  const unsigned short* Cim = (const unsigned short*)d_in[4];
  const unsigned short* logdt = (const unsigned short*)d_in[5];
  const unsigned short* Dsk = (const unsigned short*)d_in[6];
  const unsigned short* w_in = (const unsigned short*)d_in[8];
  const unsigned short* w_sp = (const unsigned short*)d_in[9];
  const unsigned short* b_sp = (const unsigned short*)d_in[10];
  const unsigned short* w_out = (const unsigned short*)d_in[11];
  char* ws = (char*)d_ws;
  (void)hipFuncSetAttribute((const void*)k1_gemm1, hipFuncAttributeMaxDynamicSharedMemorySize, K1_LDS);
  (void)hipFuncSetAttribute((const void*)k2_mix, hipFuncAttributeMaxDynamicSharedMemorySize, K2_LDS);
  (void)hipFuncSetAttribute((const void*)k3_out, hipFuncAttributeMaxDynamicSharedMemorySize, K3_LDS);
  k1_gemm1<<<256, 512, K1_LDS, stream>>>(x, w_in, w_sp, ws);
  k2_mix<<<256, 512, K2_LDS, stream>>>(w_sp, b_sp, logA, Aim, Cre, Cim, logdt, Dsk, w_out, ws);
  k3_out<<<224, 512, K3_LDS, stream>>>(x, ws, (unsigned short*)d_out);
}
```
